```python
import torch

def _softmax(x, axis=-1):
    return torch.softmax(x, dim=axis)
def _gelu(x):
    return torch.nn.functional.gelu(x, approximate="tanh")
def _silu(x):
    return torch.nn.functional.silu(x)
def _sigmoid(x):
    return torch.sigmoid(x)
def _layernorm(x, axis=-1, eps=1e-5):
    m = x.mean(dim=axis, keepdim=True)
    v = x.var(dim=axis, keepdim=True, unbiased=False)
    return (x - m) / torch.sqrt(v + eps)
def _scatter_add(tgt, idx, src, axis=0):
    return tgt.clone().index_add_(axis, idx, src)
def _segment_sum(src, seg, n):
    out = torch.zeros((n,) + src.shape[1:], dtype=src.dtype)
    return out.index_add_(0, seg, src)
def _pad(x, pw):
    flat = tuple(v for pair in reversed(pw) for v in pair)
    return torch.nn.functional.pad(x, flat)

kernel_name = 'parallel_sum__grouped_kv_mqa__mixer_channel_mlp__prompt'

def setup_inputs():

    _w = (lambda a: (lambda t: t.to(torch.bfloat16) if t.is_floating_point() else t)(torch.as_tensor(a)))
    def _A_setup():
        B, S, D, H = 1, 2048, 576, 32
        Dh = D // H
        return {
            "x": _w((lambda *s: torch.randn(*s, dtype=torch.bfloat16) * 0.05)(B, S, D)),
            "wq": _w((lambda *s: torch.randn(*s, dtype=torch.bfloat16) * 0.05)(D, H * Dh)),
            "wk": _w((lambda *s: torch.randn(*s, dtype=torch.bfloat16) * 0.05)(D, Dh)),
            "wv": _w((lambda *s: torch.randn(*s, dtype=torch.bfloat16) * 0.05)(D, Dh)),
            "wo": _w((lambda *s: torch.randn(*s, dtype=torch.bfloat16) * 0.05)(H * Dh, D)),
        }
    _A_in = _A_setup()

    def _B_setup():
        B, S, D, F = 1, 2048, 576, 4608
        return {
            "x": _w((lambda *s: torch.randn(*s, dtype=torch.bfloat16) * 0.05)(B, S, D)),
            "w1": _w((lambda *s: torch.randn(*s, dtype=torch.bfloat16) * 3.2)(D, F) * (D ** -0.5)),
            "b1": _w((lambda *s: torch.randn(*s, dtype=torch.bfloat16) * 0.8)(F)),
            "w2": _w((lambda *s: torch.randn(*s, dtype=torch.bfloat16) * 0.1)(F, D) * (F ** -0.5)),
            "b2": _w((lambda *s: torch.randn(*s, dtype=torch.bfloat16) * 0.1)(D)),
        }
    _B_in = _B_setup()

    return {
        "A_x": _A_in["x"],
        "A_wq": _A_in["wq"],
        "A_wk": _A_in["wk"],
        "A_wv": _A_in["wv"],
        "A_wo": _A_in["wo"],
        "B_x": _B_in["x"],
        "B_w1": _B_in["w1"],
        "B_b1": _B_in["b1"],
        "B_w2": _B_in["w2"],
        "B_b2": _B_in["b2"],
    }

def reference(A_x, A_wq, A_wk, A_wv, A_wo, B_x, B_w1, B_b1, B_w2, B_b2):
    def _A_ref(x, wq, wk, wv, wo):
        B, S, D = x.shape
        H = 32
        Dh = D // H
        q = (lambda x, *d: x.permute(*d))((x @ wq).reshape(B, S, H, Dh), 0, 2, 1, 3)
        k = x @ wk
        v = x @ wv
        scores = torch.einsum("bhqd,bkd->bhqk", q, k) * (Dh ** -0.5)
        p = _softmax(scores, axis=-1)
        o = torch.einsum("bhqk,bkd->bhqd", p, v)
        return (lambda x, *d: x.permute(*d))(o, 0, 2, 1, 3).reshape(B, S, H * Dh) @ wo

    def _B_ref(x, w1, b1, w2, b2):
        y = _layernorm(x, axis=-1)
        h = _gelu(torch.einsum("bsd,df->bsf", y, w1) + b1)
        return torch.einsum("bsf,fd->bsd", h, w2) + b2

    x = A_x
    xn = _layernorm(x, axis=-1)
    return x + _A_ref(xn, A_wq, A_wk, A_wv, A_wo) + _B_ref(xn, B_w1, B_b1, B_w2, B_b2)
```

```cpp
#include <hip/hip_runtime.h>
#include <hip/hip_bf16.h>
#include <stdint.h>

#define DI __device__ __forceinline__
#define LDSP __attribute__((address_space(3)))
#define GLP __attribute__((address_space(1)))

typedef unsigned short u16;
typedef __attribute__((ext_vector_type(8))) short s16x8;
typedef __attribute__((ext_vector_type(4))) short s16x4;
typedef __attribute__((ext_vector_type(16))) float f32x16;
typedef __attribute__((ext_vector_type(4))) float f32x4;
typedef __attribute__((ext_vector_type(2))) float f32x2;
typedef __attribute__((ext_vector_type(2))) __bf16 bf16x2;
typedef __attribute__((ext_vector_type(4))) unsigned u32x4;
typedef __attribute__((ext_vector_type(2))) unsigned u32x2;

constexpr size_t OFF_H    = 0;
constexpr size_t OFF_W2T  = OFF_H + 18874368;
constexpr size_t OFF_WOT  = OFF_W2T + 5308416;
constexpr size_t OFF_Q    = OFF_WOT + 663552;
constexpr size_t OFF_KV   = OFF_Q + 2359296;
constexpr size_t OFF_AO   = OFF_KV + 151552;
constexpr size_t OFF_PART = OFF_AO + 2359296;
constexpr size_t OFF_RED  = OFF_PART + 18874368;
constexpr int KV_K1 = 65536, KV_VT = 73728, KV_BYTES = 151552;

#define MFMA32(a, b, c) __builtin_amdgcn_mfma_f32_32x32x16_bf16((a), (b), (c), 0, 0, 0)
#define WAIT_VM(n) asm volatile("s_waitcnt vmcnt(" #n ")" ::: "memory")
#define WAIT_LGKM0() asm volatile("s_waitcnt lgkmcnt(0)" ::: "memory")
#define BAR() __builtin_amdgcn_s_barrier()

DI unsigned pack2(float a, float b) {
  f32x2 v = {a, b};
  return __builtin_bit_cast(unsigned, __builtin_convertvector(v, bf16x2));
}
DI float bf2f(u16 v) { return __builtin_bit_cast(float, (unsigned)v << 16); }
DI float dot2(unsigned a, unsigned b, float c) {
  return __builtin_amdgcn_fdot2_f32_bf16(__builtin_bit_cast(bf16x2, a), __builtin_bit_cast(bf16x2, b), c, false);
}
DI void glds16(const void* g, LDSP char* l) {
  __builtin_amdgcn_global_load_lds((const GLP void*)g, (LDSP void*)l, 16, 0, 0);
}
DI void glds4(const void* g, LDSP char* l) {
  __builtin_amdgcn_global_load_lds((const GLP void*)g, (LDSP void*)l, 4, 0, 0);
}
DI s16x8 tr_frag(LDSP char* p, int second_off) {
  s16x4 lo = __builtin_amdgcn_ds_read_tr16_b64_v4i16((LDSP s16x4*)p);
  s16x4 hi = __builtin_amdgcn_ds_read_tr16_b64_v4i16((LDSP s16x4*)(p + second_off));
  return __builtin_shufflevector(lo, hi, 0, 1, 2, 3, 4, 5, 6, 7);
}
DI float other_half(float v) {
  unsigned u = __builtin_bit_cast(unsigned, v);
  auto r = __builtin_amdgcn_permlane32_swap(u, u, false, false);
  unsigned o = (threadIdx.x & 32) ? r[0] : r[1];
  return __builtin_bit_cast(float, o);
}
DI float gelu_tanh(float z) {
  const float c0 = -2.302208198f;
  const float c1 = -0.1029432397f;
  float t = z * z;
  float p = __builtin_fmaf(t, c1, c0);
  float e = __builtin_amdgcn_exp2f(z * p);
  return z * __builtin_amdgcn_rcpf(1.0f + e);
}

constexpr int K1_SLOT = 36864;
constexpr int K1_BIAS = 3 * K1_SLOT;
constexpr int K1_LDS  = K1_BIAS + 288 * 4;

#define SCHED() __builtin_amdgcn_sched_barrier(0)
#define SGB(mask, n) __builtin_amdgcn_sched_group_barrier((mask), (n), 0)

struct GeluTile {
  float v[2];
  unsigned pk[4];
  f32x4 bb;
};
DI void gelu_step(GeluTile& g, const f32x16& a, int i, float r, LDSP char* bias_p, char* out_p) {
  if ((i & 3) == 0) g.bb = *(LDSP f32x4*)(bias_p + (i >> 2) * 32);
  g.v[i & 1] = gelu_tanh(__builtin_fmaf(a[i], r, g.bb[i & 3]));
  if (i & 1) g.pk[(i >> 1) & 3] = pack2(g.v[0], g.v[1]);
  if ((i & 7) == 7) {
    u32x4 o = {g.pk[0], g.pk[1], g.pk[2], g.pk[3]};
    *(u32x4*)(out_p + (i >> 3) * 1024) = o;
  }
}

template <int KT, typename F>
DI void w1_stage(LDSP char* sb, s16x8 (&xf)[36], f32x16 (&acc)[3], F&& extra) {
  s16x8 a[2][3];
#pragma unroll
  for (int t = 0; t < 3; ++t) a[0][t] = tr_frag(sb + t * 64, 768);
#pragma unroll
  for (int ks = 0; ks < 12; ++ks) {
    const int cur = ks & 1;
    if (ks + 1 < 12) {
#pragma unroll
      for (int t = 0; t < 3; ++t) a[cur ^ 1][t] = tr_frag(sb + (ks + 1) * 3072 + t * 64, 768);
    }
    extra(ks);
#pragma unroll
    for (int t = 0; t < 3; ++t) acc[t] = MFMA32(a[cur][t], xf[12 * KT + ks], acc[t]);
    SGB(0x100, 7);
    SGB(0x8, 1); SGB(0x402, 6);
    SGB(0x8, 1); SGB(0x402, 6);
    SGB(0x8, 1); SGB(0x402, 8);
    SCHED();
  }
}

template <typename F>
DI void qkv_stage(LDSP char* sb, s16x8 (&xf)[36], f32x16& qa, F&& extra) {
  s16x8 a[3];
  a[0] = tr_frag(sb, 256);
  a[1] = tr_frag(sb + 1024, 256);
#pragma unroll
  for (int s = 0; s < 36; ++s) {
    if (s + 2 < 36) a[(s + 2) % 3] = tr_frag(sb + (s + 2) * 1024, 256);
    extra(s);
    qa = MFMA32(a[s % 3], xf[s], qa);
    SCHED();
  }
}

__global__ __launch_bounds__(256, 1)
void k1(const u16* __restrict__ x, const u16* __restrict__ wq, const u16* __restrict__ wk, const u16* __restrict__ wv,
        const u16* __restrict__ wo, const u16* __restrict__ w1, const u16* __restrict__ b1, const u16* __restrict__ w2,
        char* __restrict__ ws) {
  extern __shared__ __attribute__((aligned(16))) char smem_[];
  LDSP char* smem = (LDSP char*)smem_;
  const int tid = threadIdx.x, lane = tid & 63;
  const int w = __builtin_amdgcn_readfirstlane(tid >> 6);
  const int b = blockIdx.x, xcd = b & 7, idx = b >> 3;
  const int tg = (xcd & 1) * 8 + (idx & 7), j = (xcd >> 1) * 4 + (idx >> 3);
  const int t32 = lane & 31, hh = lane >> 5;
  const int tt = tg * 4 + w;
  const int token = tt * 32 + t32;
  const bool heavy = (j & 3) == 0;

  s16x8 xf[36];
  {
    const char* xr = (const char*)x + (size_t)token * 1152 + hh * 16;
#pragma unroll
    for (int s = 0; s < 36; ++s) xf[s] = *(const s16x8*)(xr + s * 32);
  }
  unsigned off3[3];
#pragma unroll
  for (int i = 0; i < 3; ++i) {
    unsigned bb = 1024 * i + 16 * lane;
    off3[i] = (bb / 192) * 9216 + (bb % 192);
  }
  const char* w1w = (const char*)w1 + (size_t)(48 * w) * 9216 + (size_t)j * 576;
  auto issue_w1 = [&](int st) {
    int c = st / 3, kt = st - 3 * c;
    const char* g = w1w + c * 192 + (size_t)kt * 192 * 9216;
    LDSP char* l = smem + (st % 3) * K1_SLOT + w * 9216;
#pragma unroll
    for (int i = 0; i < 9; ++i) glds16(g + (i / 3) * 16 * 9216 + off3[i % 3], l + i * 1024);
  };
  auto issue_qkv = [&](int tile, int slot) {
    int lane = tid & 63; asm volatile("" : "+v"(lane));
    LDSP char* l = smem + slot * K1_SLOT + w * 9216;
    if (tile < 18) {
      const char* g = (const char*)wq + (size_t)(144 * w + (lane >> 2)) * 1152 + tile * 64 + (lane & 3) * 16;
#pragma unroll
      for (int i = 0; i < 9; ++i) glds16(g + i * 16 * 1152, l + i * 1024);
    } else {
      const u16* src = (tile == 18) ? wk : wv;
      int dw = lane & 15; dw = dw > 8 ? 8 : dw;
      const char* g = (const char*)src + (size_t)(144 * w + (lane >> 4)) * 36 + dw * 4;
#pragma unroll
      for (int i = 0; i < 36; ++i) glds4(g + i * 4 * 36, l + i * 256);
    }
  };
  issue_w1(0);
  issue_w1(1);
  for (int i = tid; i < 288; i += 256) *(LDSP float*)(smem + K1_BIAS + i * 4) = bf2f(b1[j * 288 + i]);

  float sum4[4] = {0.f, 0.f, 0.f, 0.f};
#pragma unroll
  for (int s = 0; s < 36; ++s) {
    u32x4 u = __builtin_bit_cast(u32x4, xf[s]);
#pragma unroll
    for (int i = 0; i < 4; ++i) sum4[i] = dot2(u[i], 0x3f803f80u, sum4[i]);
  }
  float sum = (sum4[0] + sum4[1]) + (sum4[2] + sum4[3]);
  sum += other_half(sum);
  const float negm = -sum * (1.0f / 576.0f);
  float ssq = 0.f;
  auto center = [&](int s) {
    u32x4 u = __builtin_bit_cast(u32x4, xf[s]);
#pragma unroll
    for (int i = 0; i < 4; ++i) {
      float lo = __builtin_bit_cast(float, u[i] << 16) + negm;
      float hi = __builtin_bit_cast(float, u[i] & 0xffff0000u) + negm;
      u[i] = pack2(lo, hi);
      ssq = dot2(u[i], u[i], ssq);
    }
    xf[s] = __builtin_bit_cast(s16x8, u);
  };

  const int q4 = (lane & 15) >> 2, p4 = lane & 3, g16 = (lane >> 4) & 1;
  const int lo_w1 = (8 * hh + q4) * 192 + 32 * g16 + 8 * p4;
  const int lo_q  = (8 * hh + q4) * 64 + 32 * g16 + 8 * p4;

  f32x16 acc[3], pacc[3];
  char* hout = ws + OFF_H + ((size_t)tt * 288 + (size_t)j * 18) * 1024 + lane * 16;
  LDSP char* biasl = smem + K1_BIAS + hh * 16;

#pragma unroll
  for (int t = 0; t < 3; ++t)
#pragma unroll
    for (int i = 0; i < 16; ++i) acc[t][i] = 0.f;
  center(0);
  WAIT_VM(9); WAIT_LGKM0(); BAR();
  issue_w1(2);
  w1_stage<0>(smem + 0 * K1_SLOT + lo_w1, xf, acc, [&](int ks) { center(ks + 1); });
  WAIT_VM(9); WAIT_LGKM0(); BAR();
  issue_w1(3);
  w1_stage<1>(smem + 1 * K1_SLOT + lo_w1, xf, acc, [&](int ks) { center(12 + ks + 1); });
  WAIT_VM(9); WAIT_LGKM0(); BAR();
  issue_w1(4);
  w1_stage<2>(smem + 2 * K1_SLOT + lo_w1, xf, acc, [&](int ks) { if (ks < 11) center(24 + ks + 1); });
  ssq += other_half(ssq);
  const float var = ssq * (1.0f / 576.0f);
  const float r1 = __builtin_amdgcn_rsqf(var + 1e-5f);
  const float rm = r1 * __builtin_amdgcn_rsqf(var * r1 * r1 + 1e-5f);

#pragma unroll
  for (int c = 1; c < 3; ++c) {
#pragma unroll
    for (int t = 0; t < 3; ++t) {
      pacc[t] = acc[t];
#pragma unroll
      for (int i = 0; i < 16; ++i) acc[t][i] = 0.f;
    }
#pragma unroll
    for (int kt = 0; kt < 3; ++kt) {
      const int st = 3 * c + kt;
      if (kt == 0) { WAIT_VM(9); } else if (kt == 1) { WAIT_VM(11); } else { WAIT_VM(13); }
      WAIT_LGKM0(); BAR();
      if (st + 2 < 9) issue_w1(st + 2);
      else if (st + 2 == 9) issue_qkv(j, 0);
      else {
        if (heavy) issue_qkv(16 + (j >> 2), 1);
        else {
          int lw = (tg * 12 + (j - (j >> 2) - 1)) * 4 + w;
          if (lw < 648) {
            int lane = tid & 63; asm volatile("" : "+v"(lane));
            LDSP char* l = smem + K1_SLOT + w * 9216;
#pragma unroll
            for (int i = 0; i < 9; ++i) {
              int bb = 1024 * i + 16 * lane;
              int row = bb / 576, cb = bb % 576;
              const char* g;
              if (lw < 576) {
                g = (const char*)w2 + (size_t)((lw >> 1) * 16 + row) * 1152 + (lw & 1) * 576 + cb;
              } else {
                int u = lw - 576, ks = u >> 1;
                int crow = ks < 32 ? ks * 18 + row : 18 * (8 * (ks - 32) + (row >> 1)) + 16 + (row & 1);
                g = (const char*)wo + (size_t)crow * 1152 + (u & 1) * 576 + cb;
              }
              glds16(g, l + i * 1024);
            }
          }
        }
      }
      GeluTile gt;
      LDSP char* bp = biasl + ((c - 1) * 96 + kt * 32) * 4;
      char* op = hout + ((c - 1) * 6 + kt * 2) * 1024;
      auto ex = [&](int ks) {
#pragma unroll
        for (int i = 0; i < 16; ++i)
          if ((i * 3) / 4 == ks) gelu_step(gt, pacc[kt], i, rm, bp, op);
      };
      if (kt == 0) w1_stage<0>(smem + 0 * K1_SLOT + lo_w1, xf, acc, ex);
      else if (kt == 1) w1_stage<1>(smem + 1 * K1_SLOT + lo_w1, xf, acc, ex);
      else w1_stage<2>(smem + 2 * K1_SLOT + lo_w1, xf, acc, ex);
    }
  }

  auto qkv_epilogue = [&](const f32x16& qa, int tile) {
    int lane = tid & 63; asm volatile("" : "+v"(lane));
    const int t32 = lane & 31, hh = lane >> 5, token = tt * 32 + t32;
    if (tile < 18) {
      const float sc = r1 * (0.23570226039551584f * 1.4426950408889634f);
      char* qp = ws + OFF_Q + (size_t)token * 1152 + tile * 64 + hh * 8;
#pragma unroll
      for (int g = 0; g < 4; ++g) {
        u32x2 o = {pack2(qa[4 * g] * sc, qa[4 * g + 1] * sc), pack2(qa[4 * g + 2] * sc, qa[4 * g + 3] * sc)};
        *(u32x2*)(qp + g * 16) = o;
      }
    } else if (tile == 18) {
      u32x4 o = {pack2(qa[0] * r1, qa[1] * r1), pack2(qa[2] * r1, qa[3] * r1),
                 pack2(qa[4] * r1, qa[5] * r1), pack2(qa[6] * r1, qa[7] * r1)};
      *(u32x4*)(ws + OFF_KV + (size_t)tt * 1024 + lane * 16) = o;
      if (hh == 0) *(unsigned*)(ws + OFF_KV + KV_K1 + (size_t)token * 4) = pack2(qa[8] * r1, qa[9] * r1);
    } else {
      const int s = t32 >> 4, hk = (t32 >> 2) & 1, jj = ((t32 >> 3) & 1) * 4 + (t32 & 3);
      char* vp = ws + OFF_KV + KV_VT + (size_t)((tt * 2 + s) * 2 + hk) * 304 + jj * 2;
#pragma unroll
      for (int i = 0; i < 8; ++i) {
        const int d = (i & 3) + 8 * (i >> 2) + 4 * hh;
        *(u16*)(vp + d * 16) = (u16)(pack2(qa[i] * r1, 0.f) & 0xffffu);
      }
      if (hh == 0) {
        *(u16*)(vp + 16 * 16) = (u16)(pack2(qa[8] * r1, 0.f) & 0xffffu);
        *(u16*)(vp + 17 * 16) = (u16)(pack2(qa[9] * r1, 0.f) & 0xffffu);
        *(u16*)(vp + 18 * 16) = (u16)0x3f80;
      }
    }
  };
  {
    WAIT_VM(0); WAIT_LGKM0(); BAR();
    f32x16 qa;
#pragma unroll
    for (int i = 0; i < 16; ++i) qa[i] = 0.f;
    GeluTile gt;
    qkv_stage(smem + lo_q, xf, qa, [&](int s) {
      const int t = s / 12, ks = s % 12;
#pragma unroll
      for (int i = 0; i < 16; ++i)
        if ((i * 3) / 4 == ks) gelu_step(gt, acc[t], i, rm, biasl + (2 * 96 + t * 32) * 4, hout + (2 * 6 + t * 2) * 1024);
    });
    qkv_epilogue(qa, j);
  }
  if (heavy) {
    f32x16 qa;
#pragma unroll
    for (int i = 0; i < 16; ++i) qa[i] = 0.f;
    qkv_stage(smem + K1_SLOT + lo_q, xf, qa, [&](int s) {});
    qkv_epilogue(qa, 16 + (j >> 2));
  } else {
    int lw = (tg * 12 + (j - (j >> 2) - 1)) * 4 + w;
    if (lw < 648) {
      int lane = tid & 63; asm volatile("" : "+v"(lane));
      const int hh = lane >> 5, q4 = (lane & 15) >> 2, p4 = lane & 3, g16 = (lane >> 4) & 1;
      LDSP char* sb = smem + K1_SLOT + w * 9216 + (4 * hh + q4) * 576 + 32 * g16 + 8 * p4;
      char* dst = (lw < 576) ? ws + OFF_W2T + ((size_t)(lw >> 1) * 18 + (lw & 1) * 9) * 1024
                             : ws + OFF_WOT + ((size_t)((lw - 576) >> 1) * 18 + (lw & 1) * 9) * 1024;
#pragma unroll
      for (int n = 0; n < 9; ++n) {
        s16x8 f = tr_frag(sb + n * 64, 8 * 576);
        *(s16x8*)(dst + n * 1024 + lane * 16) = f;
      }
    }
  }
}

constexpr int K2_FLAG = KV_BYTES;
constexpr int K2_LDS  = KV_BYTES + 64;

DI void attn_main(LDSP char* smem, int lane, const s16x8 (&q0)[2], const s16x8 (&q1)[2], f32x16 (&o)[2]) {
  const int t32 = lane & 31, hh = lane >> 5;
  int vr = t32;
  if (vr > 18) vr = (vr >= 20 && vr < 28) ? 0 : 4;
  LDSP char* kp0 = smem + lane * 16;
  LDSP char* kp1 = smem + KV_K1 + t32 * 4;
  LDSP char* vp = smem + KV_VT + hh * 304 + vr * 16;
#pragma unroll
  for (int qb = 0; qb < 2; ++qb)
#pragma unroll
    for (int i = 0; i < 16; ++i) o[qb][i] = 0.f;
  f32x16 zero;
#pragma unroll
  for (int i = 0; i < 16; ++i) zero[i] = 0.f;
#pragma unroll 2
  for (int T = 0; T < 64; ++T) {
    s16x8 k0 = *(LDSP s16x8*)(kp0 + T * 1024);
    u32x4 k1u = {*(LDSP unsigned*)(kp1 + T * 128), 0x3f803f80u, 0u, 0u};
    s16x8 k1 = __builtin_bit_cast(s16x8, k1u);
    s16x8 v0 = *(LDSP s16x8*)(vp + T * 1216);
    s16x8 v1 = *(LDSP s16x8*)(vp + T * 1216 + 608);
#pragma unroll
    for (int qb = 0; qb < 2; ++qb) {
      f32x16 s = MFMA32(k0, q0[qb], zero);
      s = MFMA32(k1, q1[qb], s);
      u32x4 p0, p1;
#pragma unroll
      for (int i = 0; i < 4; ++i) {
        p0[i] = pack2(__builtin_amdgcn_exp2f(s[2 * i]), __builtin_amdgcn_exp2f(s[2 * i + 1]));
        p1[i] = pack2(__builtin_amdgcn_exp2f(s[8 + 2 * i]), __builtin_amdgcn_exp2f(s[8 + 2 * i + 1]));
      }
      o[qb] = MFMA32(v0, __builtin_bit_cast(s16x8, p0), o[qb]);
      o[qb] = MFMA32(v1, __builtin_bit_cast(s16x8, p1), o[qb]);
    }
  }
}

DI void attn_rowmax(LDSP char* smem, int lane, const s16x8 (&q0)[2], const s16x8 (&q1z)[2], int nT, float (&m)[2]) {
  const int t32 = lane & 31;
  LDSP char* kp0 = smem + lane * 16;
  LDSP char* kp1 = smem + KV_K1 + t32 * 4;
  f32x16 zero;
#pragma unroll
  for (int i = 0; i < 16; ++i) zero[i] = 0.f;
  m[0] = -INFINITY; m[1] = -INFINITY;
#pragma unroll 1
  for (int T = 0; T < nT; ++T) {
    s16x8 k0 = *(LDSP s16x8*)(kp0 + T * 1024);
    u32x4 k1u = {*(LDSP unsigned*)(kp1 + T * 128), 0x3f803f80u, 0u, 0u};
    s16x8 k1 = __builtin_bit_cast(s16x8, k1u);
#pragma unroll
    for (int qb = 0; qb < 2; ++qb) {
      f32x16 s = MFMA32(k0, q0[qb], zero);
      s = MFMA32(k1, q1z[qb], s);
#pragma unroll
      for (int i = 0; i < 16; ++i) m[qb] = __builtin_fmaxf(m[qb], s[i]);
    }
  }
#pragma unroll
  for (int qb = 0; qb < 2; ++qb) m[qb] = __builtin_fmaxf(m[qb], other_half(m[qb]));
}

template <int NT>
DI void gemm2_load(const char* hp, const char* wp, const char* wsp, unsigned lo16, int k, s16x8 (&hb)[2], s16x8 (&wb)[5]) {
  const char* h0 = hp + (size_t)k * 1024;
  const char* w0 = wp + (size_t)k * 18432;
  hb[0] = *(const s16x8*)(h0 + lo16);
  hb[1] = *(const s16x8*)(h0 + (size_t)288 * 1024 + lo16);
#pragma unroll
  for (int n = 0; n < 4; ++n) wb[n] = *(const s16x8*)(w0 + n * 1024 + lo16);
  if (NT == 5) wb[4] = *(const s16x8*)(wsp + (size_t)k * 18432 + lo16);
}
template <int NT>
DI void gemm2_mma(const s16x8 (&hb)[2], const s16x8 (&wb)[5], f32x16 (&acc)[2][5]) {
#pragma unroll
  for (int n = 0; n < NT; ++n) {
    acc[0][n] = MFMA32(wb[n], hb[0], acc[0][n]);
    acc[1][n] = MFMA32(wb[n], hb[1], acc[1][n]);
  }
}
template <int NT>
DI void gemm2_half(const char* hp, const char* wp, const char* wsp, unsigned lo16, f32x16 (&acc)[2][5]) {
  s16x8 hA[2], wA[5], hB[2], wB[5];
  gemm2_load<NT>(hp, wp, wsp, lo16, 0, hA, wA);
#pragma unroll 1
  for (int k = 0; k < 18; k += 2) {
    gemm2_load<NT>(hp, wp, wsp, lo16, k + 1, hB, wB);
    SCHED();
    gemm2_mma<NT>(hA, wA, acc);
    SCHED();
    gemm2_load<NT>(hp, wp, wsp, lo16, (k + 2 < 18) ? k + 2 : k + 1, hA, wA);
    SCHED();
    gemm2_mma<NT>(hB, wB, acc);
    SCHED();
  }
}

__global__ __launch_bounds__(512, 2)
void k2(char* __restrict__ ws) {
  extern __shared__ __attribute__((aligned(16))) char smem_[];
  LDSP char* smem = (LDSP char*)smem_;
  const int tid = threadIdx.x, lane = tid & 63;
  const int w = __builtin_amdgcn_readfirstlane(tid >> 6);
  const int b = blockIdx.x;
  const int t32 = lane & 31, hh = lane >> 5;

  {
    const char* g = ws + OFF_KV + lane * 16;
    for (int i = w; i < 148; i += 8) glds16(g + i * 1024, smem + i * 1024);
  }
  if (tid < 16) *(LDSP unsigned*)(smem + K2_FLAG + tid * 4) = 0u;

  if (w < 4) {
    const int tt = b >> 2, hg = b & 3;
    const int token = tt * 32 + t32;
    u32x4 q0u[2], q1u[2];
#pragma unroll
    for (int qb = 0; qb < 2; ++qb) {
      const int head = hg * 8 + 2 * w + qb;
      const char* qp = ws + OFF_Q + (size_t)token * 1152 + head * 36;
      q0u[qb][0] = *(const unsigned*)(qp + 8 * hh);
      q0u[qb][1] = *(const unsigned*)(qp + 8 * hh + 4);
      q0u[qb][2] = *(const unsigned*)(qp + 16 + 8 * hh);
      q0u[qb][3] = *(const unsigned*)(qp + 16 + 8 * hh + 4);
      unsigned e = *(const unsigned*)(qp + 32);
      q1u[qb][0] = hh ? 0u : e;
      q1u[qb][1] = 0u; q1u[qb][2] = 0u; q1u[qb][3] = 0u;
    }
    WAIT_VM(0); WAIT_LGKM0(); BAR();
    s16x8 q0[2], q1[2];
    f32x16 o[2];
    float linv[2];
    int nT = 1;
#pragma unroll 1
    for (int attempt = 0; attempt < 2; ++attempt) {
      float m[2];
#pragma unroll
      for (int qb = 0; qb < 2; ++qb) {
        q1u[qb][1] = 0u;
        q0[qb] = __builtin_bit_cast(s16x8, q0u[qb]);
        q1[qb] = __builtin_bit_cast(s16x8, q1u[qb]);
      }
      attn_rowmax(smem, lane, q0, q1, nT, m);
#pragma unroll
      for (int qb = 0; qb < 2; ++qb) {
        unsigned hi = pack2(-m[qb], 0.f) & 0xffffu;
        float rem = -m[qb] - __builtin_bit_cast(float, hi << 16);
        unsigned lo = pack2(rem, 0.f) & 0xffffu;
        q1u[qb][1] = hh ? 0u : (hi | (lo << 16));
        q1[qb] = __builtin_bit_cast(s16x8, q1u[qb]);
      }
      attn_main(smem, lane, q0, q1, o);
      bool bad = false;
#pragma unroll
      for (int qb = 0; qb < 2; ++qb) {
        float l = o[qb][10];
        float lo_ = other_half(l);
        l = hh ? lo_ : l;
        linv[qb] = 1.0f / l;
        float mx = l;
#pragma unroll
        for (int i = 0; i < 8; ++i) mx = __builtin_fmaxf(mx, __builtin_fabsf(o[qb][i]));
        if (hh == 0) mx = __builtin_fmaxf(mx, __builtin_fmaxf(__builtin_fabsf(o[qb][8]), __builtin_fabsf(o[qb][9])));
        float chk = l + o[qb][0] + o[qb][1] + o[qb][2] + o[qb][3] + o[qb][4] + o[qb][5] + o[qb][6] + o[qb][7];
        if (hh == 0) chk += o[qb][8] + o[qb][9];
        bad = bad || !(mx < 1e30f) || !(l < 1e18f) || (chk != chk);
      }
      if (!__any(bad)) break;
      nT = 64;
    }
#pragma unroll
    for (int qb = 0; qb < 2; ++qb) {
      const int head = hg * 8 + 2 * w + qb;
      const float li = linv[qb];
      u32x4 ov = {pack2(o[qb][0] * li, o[qb][1] * li), pack2(o[qb][2] * li, o[qb][3] * li),
                  pack2(o[qb][4] * li, o[qb][5] * li), pack2(o[qb][6] * li, o[qb][7] * li)};
      char* ap = ws + OFF_AO + (size_t)tt * 36 * 1024;
      *(u32x4*)(ap + head * 1024 + lane * 16) = ov;
      if (hh == 0) {
        const int h8 = head & 7;
        *(unsigned*)(ap + (32 + (head >> 3)) * 1024 + (t32 + 32 * ((h8 >> 1) & 1)) * 16 + ((h8 >> 2) * 2 + (h8 & 1)) * 4) =
            pack2(o[qb][8] * li, o[qb][9] * li);
      }
    }
  } else {
    const int g = w - 4;
    const int xcd = b & 7, idx = b >> 3;
    const int kh = xcd & 1, tp = (xcd >> 1) * 8 + (idx & 7), ng = idx >> 3;
    const int nt0 = (ng >> 1) * 9 + (ng & 1) * 5;
    const int nts = (ng >> 1) * 9 + 4;
    const int fb0 = kh * 144 + g * 36;
    const char* hp0 = ws + OFF_H + ((size_t)(2 * tp) * 288 + fb0) * 1024;
    const char* wp = ws + OFF_W2T + ((size_t)fb0 * 18 + nt0) * 1024;
    const char* wsp = ws + OFF_W2T + ((size_t)fb0 * 18 + nts) * 1024;
    const unsigned lo16 = lane * 16;
    f32x16 acc[2][5];
#pragma unroll
    for (int a = 0; a < 2; ++a)
#pragma unroll
      for (int n = 0; n < 5; ++n)
#pragma unroll
        for (int i = 0; i < 16; ++i) acc[a][n][i] = 0.f;
    WAIT_VM(0); WAIT_LGKM0(); BAR();
    if (ng & 1) {
      gemm2_half<4>(hp0, wp, wsp, lo16, acc);
      gemm2_half<5>(hp0 + 18 * 1024, wp + 18 * 18432, wsp + 18 * 18432, lo16, acc);
    } else {
      gemm2_half<5>(hp0, wp, wsp, lo16, acc);
      gemm2_half<4>(hp0 + 18 * 1024, wp + 18 * 18432, wsp + 18 * 18432, lo16, acc);
    }
    char* red = ws + OFF_RED + (size_t)b * 4 * 10 * 4096;
#pragma unroll
    for (int a = 0; a < 2; ++a)
#pragma unroll
      for (int n = 0; n < 5; ++n) {
        const int ti = a * 5 + n;
        if ((ti & 3) != g) {
          char* rp = red + (size_t)(g * 10 + ti) * 4096 + lane * 16;
#pragma unroll
          for (int q = 0; q < 4; ++q) {
            f32x4 v = {acc[a][n][4 * q], acc[a][n][4 * q + 1], acc[a][n][4 * q + 2], acc[a][n][4 * q + 3]};
            *(f32x4*)(rp + q * 1024) = v;
          }
        }
        SCHED();
      }
    WAIT_VM(0);
    __builtin_amdgcn_fence(__ATOMIC_RELEASE, "workgroup");
    volatile LDSP unsigned* flags = (volatile LDSP unsigned*)(smem + K2_FLAG);
    if (lane == 0) flags[g] = 1u;
    while (true) {
      unsigned f0 = flags[0], f1 = flags[1], f2 = flags[2], f3 = flags[3];
      if (f0 & f1 & f2 & f3) break;
      __builtin_amdgcn_s_sleep(2);
    }
    __builtin_amdgcn_fence(__ATOMIC_ACQUIRE, "workgroup");
#pragma unroll
    for (int a = 0; a < 2; ++a)
#pragma unroll
      for (int n = 0; n < 5; ++n) {
        const int ti = a * 5 + n;
        if ((ti & 3) == g) {
          f32x4 s4[4];
#pragma unroll
          for (int q = 0; q < 4; ++q) s4[q] = f32x4{0.f, 0.f, 0.f, 0.f};
#pragma unroll
          for (int o = 0; o < 4; ++o) {
#pragma unroll
            for (int q = 0; q < 4; ++q) {
              f32x4 v;
              if (o == (ti & 3)) v = f32x4{acc[a][n][4 * q], acc[a][n][4 * q + 1], acc[a][n][4 * q + 2], acc[a][n][4 * q + 3]};
              else v = *(const f32x4*)(red + (size_t)(o * 10 + ti) * 4096 + lane * 16 + q * 1024);
              s4[q] += v;
            }
          }
          const int nt = (n < 4) ? nt0 + n : nts;
          const int slot = (n < 4) ? kh : kh + 2 * (ng & 1);
          char* pp = ws + OFF_PART + (((size_t)slot * 64 + (2 * tp + a)) * 18 + nt) * 4096 + lane * 16;
#pragma unroll
          for (int q = 0; q < 4; ++q) *(f32x4*)(pp + q * 1024) = s4[q];
        }
        SCHED();
      }
  }
}

__global__ __launch_bounds__(256)
void k3(const u16* __restrict__ x, const u16* __restrict__ b2, const char* __restrict__ ws, u16* __restrict__ out) {
  __shared__ __attribute__((aligned(16))) float red[4][4][4][64][4];
  const int tid = threadIdx.x, lane = tid & 63;
  const int w = __builtin_amdgcn_readfirstlane(tid >> 6);
  const int b = blockIdx.x;
  const int tp = b / 9, np = b % 9;
  const int t32 = lane & 31, hh = lane >> 5;
  const char* ap = ws + OFF_AO + ((size_t)(2 * tp) * 36 + w * 9) * 1024 + lane * 16;
  const char* wp = ws + OFF_WOT + ((size_t)(w * 9) * 18 + 2 * np) * 1024 + lane * 16;
  s16x8 af[2][9], wf[2][9];
#pragma unroll
  for (int k = 0; k < 9; ++k) {
    af[0][k] = *(const s16x8*)(ap + k * 1024);
    af[1][k] = *(const s16x8*)(ap + 36 * 1024 + k * 1024);
    wf[0][k] = *(const s16x8*)(wp + k * 18432);
    wf[1][k] = *(const s16x8*)(wp + k * 18432 + 1024);
  }
  const int a = w >> 1, n = w & 1;
  const int tt = 2 * tp + a, nt = 2 * np + n;
  f32x4 fin[4];
  {
    const char* pp = ws + OFF_PART + ((size_t)tt * 18 + nt) * 4096 + lane * 16;
    const size_t SL = (size_t)64 * 18 * 4096;
#pragma unroll
    for (int q = 0; q < 4; ++q) fin[q] = *(const f32x4*)(pp + q * 1024) + *(const f32x4*)(pp + SL + q * 1024);
    if (nt == 4 || nt == 13) {
#pragma unroll
      for (int q = 0; q < 4; ++q) fin[q] += *(const f32x4*)(pp + 2 * SL + q * 1024) + *(const f32x4*)(pp + 3 * SL + q * 1024);
    }
    const int token = tt * 32 + t32;
#pragma unroll
    for (int q = 0; q < 4; ++q) {
      const int col = nt * 32 + 8 * q + 4 * hh;
      u32x2 xv = *(const u32x2*)((const char*)x + (size_t)token * 1152 + col * 2);
      u32x2 bv = *(const u32x2*)((const char*)b2 + col * 2);
      fin[q][0] += __builtin_bit_cast(float, xv[0] << 16) + __builtin_bit_cast(float, bv[0] << 16);
      fin[q][1] += __builtin_bit_cast(float, xv[0] & 0xffff0000u) + __builtin_bit_cast(float, bv[0] & 0xffff0000u);
      fin[q][2] += __builtin_bit_cast(float, xv[1] << 16) + __builtin_bit_cast(float, bv[1] << 16);
      fin[q][3] += __builtin_bit_cast(float, xv[1] & 0xffff0000u) + __builtin_bit_cast(float, bv[1] & 0xffff0000u);
    }
  }
  f32x16 acc[2][2];
#pragma unroll
  for (int i = 0; i < 2; ++i)
#pragma unroll
    for (int jn = 0; jn < 2; ++jn)
#pragma unroll
      for (int r = 0; r < 16; ++r) acc[i][jn][r] = 0.f;
#pragma unroll
  for (int k = 0; k < 9; ++k)
#pragma unroll
    for (int i = 0; i < 2; ++i)
#pragma unroll
      for (int jn = 0; jn < 2; ++jn) acc[i][jn] = MFMA32(wf[jn][k], af[i][k], acc[i][jn]);
#pragma unroll
  for (int i = 0; i < 2; ++i)
#pragma unroll
    for (int jn = 0; jn < 2; ++jn)
#pragma unroll
      for (int q = 0; q < 4; ++q) {
        f32x4 v = {acc[i][jn][4 * q], acc[i][jn][4 * q + 1], acc[i][jn][4 * q + 2], acc[i][jn][4 * q + 3]};
        *(f32x4*)&red[w][i * 2 + jn][q][lane][0] = v;
      }
  __syncthreads();
  const int token = tt * 32 + t32;
#pragma unroll
  for (int q = 0; q < 4; ++q) {
    f32x4 v = fin[q];
#pragma unroll
    for (int o = 0; o < 4; ++o) v += *(const f32x4*)&red[o][w][q][lane][0];
    u32x2 ov = {pack2(v[0], v[1]), pack2(v[2], v[3])};
    *(u32x2*)((char*)out + (size_t)token * 1152 + (nt * 32 + 8 * q + 4 * hh) * 2) = ov;
  }
}

extern "C" void kernel_launch(void* const* d_in, const int* in_sizes, int n_in,
                              void* d_out, int out_size, void* d_ws, size_t ws_size,
                              hipStream_t stream) {
  const u16* x  = (const u16*)d_in[0];
  const u16* wq = (const u16*)d_in[1];
  const u16* wk = (const u16*)d_in[2];
  const u16* wv = (const u16*)d_in[3];
  const u16* wo = (const u16*)d_in[4];
  const u16* w1 = (const u16*)d_in[6];
  const u16* b1 = (const u16*)d_in[7];
  const u16* w2 = (const u16*)d_in[8];
  const u16* b2 = (const u16*)d_in[9];
  char* ws = (char*)d_ws;
  (void)hipFuncSetAttribute((const void*)k1, hipFuncAttributeMaxDynamicSharedMemorySize, K1_LDS);
  (void)hipFuncSetAttribute((const void*)k2, hipFuncAttributeMaxDynamicSharedMemorySize, K2_LDS);
  k1<<<256, 256, K1_LDS, stream>>>(x, wq, wk, wv, wo, w1, b1, w2, ws);
  k2<<<256, 512, K2_LDS, stream>>>(ws);
  k3<<<288, 256, 0, stream>>>(x, b2, ws, (u16*)d_out);
}
```

```cpp
#include <hip/hip_runtime.h>
#include <hip/hip_bf16.h>
#include <stdint.h>

#define DI __device__ __forceinline__
#define LDSP __attribute__((address_space(3)))
#define GLP __attribute__((address_space(1)))

typedef unsigned short u16;
typedef __attribute__((ext_vector_type(8))) short s16x8;
typedef __attribute__((ext_vector_type(4))) short s16x4;
typedef __attribute__((ext_vector_type(16))) float f32x16;
typedef __attribute__((ext_vector_type(4))) float f32x4;
typedef __attribute__((ext_vector_type(2))) float f32x2;
typedef __attribute__((ext_vector_type(2))) __bf16 bf16x2;
typedef __attribute__((ext_vector_type(4))) unsigned u32x4;
typedef __attribute__((ext_vector_type(2))) unsigned u32x2;

constexpr size_t OFF_H    = 0;
constexpr size_t OFF_W2T  = OFF_H + 18874368;
constexpr size_t OFF_WOT  = OFF_W2T + 5308416;
constexpr size_t OFF_Q    = OFF_WOT + 663552;
constexpr size_t OFF_KV   = OFF_Q + 2359296;
constexpr size_t OFF_AO   = OFF_KV + 159744;
constexpr size_t OFF_PART = OFF_AO + 2359296;
constexpr size_t OFF_RED  = OFF_PART + 18874368;
constexpr int KV_GRP = 9984, KV_TILE = 2240, KV_BYTES = 159744, KV_HALF = 79872;

#define MFMA32(a, b, c) __builtin_amdgcn_mfma_f32_32x32x16_bf16((a), (b), (c), 0, 0, 0)
#define WAIT_VM(n) asm volatile("s_waitcnt vmcnt(" #n ")" ::: "memory")
#define WAIT_LGKM0() asm volatile("s_waitcnt lgkmcnt(0)" ::: "memory")
#define BAR() __builtin_amdgcn_s_barrier()
#define TS_DECL
#define STAMP(i)
#define TS_FLUSH(kid)

DI unsigned pack2(float a, float b) {
  f32x2 v = {a, b};
  return __builtin_bit_cast(unsigned, __builtin_convertvector(v, bf16x2));
}
DI float bf2f(u16 v) { return __builtin_bit_cast(float, (unsigned)v << 16); }
DI float dot2(unsigned a, unsigned b, float c) {
  return __builtin_amdgcn_fdot2_f32_bf16(__builtin_bit_cast(bf16x2, a), __builtin_bit_cast(bf16x2, b), c, false);
}
DI void glds16(const void* gbase, unsigned voff, LDSP char* l) {
  unsigned keep;
  asm volatile("s_mov_b32 %0, m0\n\ts_mov_b32 m0, %3\n\ts_nop 0\n\tglobal_load_lds_dwordx4 %1, %2\n\ts_mov_b32 m0, %0"
               : "=&s"(keep) : "v"(voff), "s"(gbase), "s"((unsigned)(size_t)l) : "memory");
}
DI void glds4(const void* gbase, unsigned voff, LDSP char* l) {
  unsigned keep;
  asm volatile("s_mov_b32 %0, m0\n\ts_mov_b32 m0, %3\n\ts_nop 0\n\tglobal_load_lds_dword %1, %2\n\ts_mov_b32 m0, %0"
               : "=&s"(keep) : "v"(voff), "s"(gbase), "s"((unsigned)(size_t)l) : "memory");
}
DI s16x8 tr_frag(LDSP char* p, int second_off) {
  s16x4 lo = __builtin_amdgcn_ds_read_tr16_b64_v4i16((LDSP s16x4*)p);
  s16x4 hi = __builtin_amdgcn_ds_read_tr16_b64_v4i16((LDSP s16x4*)(p + second_off));
  return __builtin_shufflevector(lo, hi, 0, 1, 2, 3, 4, 5, 6, 7);
}
DI float other_half(float v) {
  unsigned u = __builtin_bit_cast(unsigned, v);
  auto r = __builtin_amdgcn_permlane32_swap(u, u, false, false);
  unsigned o = (threadIdx.x & 32) ? r[0] : r[1];
  return __builtin_bit_cast(float, o);
}
DI float gelu_tanh(float z) {
  const float c0 = -2.302208198f;
  const float c1 = -0.1029432397f;
  float t = z * z;
  float p = __builtin_fmaf(t, c1, c0);
  float e = __builtin_amdgcn_exp2f(z * p);
  return z * __builtin_amdgcn_rcpf(1.0f + e);
}

constexpr int K1_SLOT = 36864;
constexpr int K1_BIAS = 4 * K1_SLOT;
constexpr int K1_CS   = K1_BIAS + 1152;
constexpr int K1_CSQ  = K1_CS + 1152;
constexpr int K1_STAT = K1_CSQ + 1024;
constexpr int K1_LDS  = K1_STAT + 1024;

#define SCHED() __builtin_amdgcn_sched_barrier(0)
#define SGB(mask, n) __builtin_amdgcn_sched_group_barrier((mask), (n), 0)

DI u32x2 gelu4(float a0, float a1, float a2, float a3, const f32x4& cs, const f32x4& bb, float negm, float r, float c0) {
  float z0, z1, z2, z3, t0, t1, t2, t3;
  unsigned o0, o1;
  asm volatile(
      "v_fma_f32 %2, %18, %14, %10\n\tv_fma_f32 %3, %18, %15, %11\n\tv_fma_f32 %4, %18, %16, %12\n\tv_fma_f32 %5, %18, %17, %13\n\t"
      "v_fma_f32 %2, %19, %2, %21\n\tv_fma_f32 %3, %19, %3, %22\n\tv_fma_f32 %4, %19, %4, %23\n\tv_fma_f32 %5, %19, %5, %24\n\t"
      "v_mul_f32 %6, %2, %2\n\tv_mul_f32 %7, %3, %3\n\tv_mul_f32 %8, %4, %4\n\tv_mul_f32 %9, %5, %5\n\t"
      "v_fmamk_f32 %6, %6, 0xbdd2d3e8, %20\n\tv_fmamk_f32 %7, %7, 0xbdd2d3e8, %20\n\tv_fmamk_f32 %8, %8, 0xbdd2d3e8, %20\n\tv_fmamk_f32 %9, %9, 0xbdd2d3e8, %20\n\t"
      "v_mul_f32 %6, %2, %6\n\tv_mul_f32 %7, %3, %7\n\tv_mul_f32 %8, %4, %8\n\tv_mul_f32 %9, %5, %9\n\t"
      "v_exp_f32 %6, %6\n\tv_exp_f32 %7, %7\n\tv_exp_f32 %8, %8\n\tv_exp_f32 %9, %9\n\t"
      "v_add_f32 %6, 1.0, %6\n\tv_add_f32 %7, 1.0, %7\n\tv_add_f32 %8, 1.0, %8\n\tv_add_f32 %9, 1.0, %9\n\t"
      "v_rcp_f32 %6, %6\n\tv_rcp_f32 %7, %7\n\tv_rcp_f32 %8, %8\n\tv_rcp_f32 %9, %9\n\t"
      "v_mul_f32 %2, %2, %6\n\tv_mul_f32 %3, %3, %7\n\tv_mul_f32 %4, %4, %8\n\tv_mul_f32 %5, %5, %9\n\t"
      "v_cvt_pk_bf16_f32 %0, %2, %3\n\tv_cvt_pk_bf16_f32 %1, %4, %5"
      : "=&v"(o0), "=&v"(o1), "=&v"(z0), "=&v"(z1), "=&v"(z2), "=&v"(z3), "=&v"(t0), "=&v"(t1), "=&v"(t2), "=&v"(t3)
      : "v"(a0), "v"(a1), "v"(a2), "v"(a3), "v"(cs[0]), "v"(cs[1]), "v"(cs[2]), "v"(cs[3]), "v"(negm), "v"(r), "v"(c0),
        "v"(bb[0]), "v"(bb[1]), "v"(bb[2]), "v"(bb[3]));
  u32x2 o = {o0, o1};
  return o;
}

template <typename F>
DI void gelu_tile(const f32x16& a, LDSP char* cs_p, LDSP char* bias_p, char* out_p, float negm, float r, F&& between) {
  const float c0 = -2.302208198f;
#pragma unroll
  for (int hf = 0; hf < 2; ++hf) {
    f32x4 cs0 = *(LDSP f32x4*)(cs_p + hf * 64), cs1 = *(LDSP f32x4*)(cs_p + hf * 64 + 32);
    f32x4 bb0 = *(LDSP f32x4*)(bias_p + hf * 64), bb1 = *(LDSP f32x4*)(bias_p + hf * 64 + 32);
    between(2 * hf);
    u32x2 lo = gelu4(a[8 * hf], a[8 * hf + 1], a[8 * hf + 2], a[8 * hf + 3], cs0, bb0, negm, r, c0);
    between(2 * hf + 1);
    u32x2 hi = gelu4(a[8 * hf + 4], a[8 * hf + 5], a[8 * hf + 6], a[8 * hf + 7], cs1, bb1, negm, r, c0);
    u32x4 o = {lo[0], lo[1], hi[0], hi[1]};
    *(u32x4*)(out_p + hf * 1024) = o;
  }
}

DI void colsum_frag(const s16x8& a, const s16x8& sel, f32x4& cs) {
  cs = __builtin_amdgcn_mfma_f32_16x16x32_bf16(sel, a, cs, 0, 0, 0);
}

template <int KT>
DI void w1_stage(LDSP char* sb0, LDSP char* sb1, LDSP char* sb2, const s16x8 (&xf)[36], f32x16 (&acc)[3], const s16x8& sel, f32x4& cs) {
  asm volatile("" : "+v"(sb0), "+v"(sb1), "+v"(sb2));
  s16x8 a[2][3];
  a[0][0] = tr_frag(sb0, 768); a[0][1] = tr_frag(sb1, 768); a[0][2] = tr_frag(sb2, 768);
#pragma unroll
  for (int ks = 0; ks < 12; ++ks) {
    const int cur = ks & 1;
    if (ks + 1 < 12) {
      a[cur ^ 1][0] = tr_frag(sb0 + (ks + 1) * 3072, 768);
      a[cur ^ 1][1] = tr_frag(sb1 + (ks + 1) * 3072, 768);
      a[cur ^ 1][2] = tr_frag(sb2 + (ks + 1) * 3072, 768);
    }
#pragma unroll
    for (int t = 0; t < 3; ++t) acc[t] = MFMA32(a[cur][t], xf[12 * KT + ks], acc[t]);
    colsum_frag(a[cur][0], sel, cs);
    SGB(0x100, 2); SGB(0x8, 1); SGB(0x100, 2); SGB(0x8, 1); SGB(0x100, 2); SGB(0x8, 2);
    SCHED();
  }
}

DI void qkv_stage(LDSP char* sb, const s16x8 (&xf)[36], f32x16& qa, const s16x8& sel, f32x4& cs) {
  asm volatile("" : "+v"(sb));
  s16x8 a[3];
  a[0] = tr_frag(sb, 256);
  a[1] = tr_frag(sb + 1024, 256);
#pragma unroll
  for (int s = 0; s < 36; ++s) {
    if (s + 2 < 36) a[(s + 2) % 3] = tr_frag(sb + (s + 2) * 1024, 256);
    qa = MFMA32(a[s % 3], xf[s], qa);
    colsum_frag(a[s % 3], sel, cs);
    SCHED();
  }
}

__global__ __launch_bounds__(512, 2)
void k1(const u16* __restrict__ x, const u16* __restrict__ wq, const u16* __restrict__ wk, const u16* __restrict__ wv,
        const u16* __restrict__ wo, const u16* __restrict__ w1, const u16* __restrict__ b1, const u16* __restrict__ w2,
        char* __restrict__ ws) {
  extern __shared__ __attribute__((aligned(16))) char smem_[];
  LDSP char* smem = (LDSP char*)smem_;
  const int tid = threadIdx.x, lane = tid & 63;
  const int w8 = __builtin_amdgcn_readfirstlane(tid >> 6);
  const int w = w8 & 3, grp = w8 >> 2;
  const int b = blockIdx.x, xcd = b & 7, idx = b >> 3;
  const int tg = (xcd >> 1) * 4 + (idx & 3), j = (xcd & 1) * 8 + (idx >> 2);
  const int t32 = lane & 31, hh = lane >> 5;
  const int tt = tg * 4 + w;
  const bool heavy = (j & 3) == 0;
  TS_DECL; STAMP(0);

  const char* w1w = (const char*)w1 + (size_t)(48 * w) * 9216 + (size_t)j * 576;
  unsigned off3[3];
#pragma unroll
  for (int i = 0; i < 3; ++i) {
    const unsigned bb = 1024 * i + 16 * lane;
    off3[i] = (bb / 192) * 9216 + (bb % 192);
  }
  auto issue_w1_rows = [&](int st, int r) {
    const int c = st / 3, kt = st - 3 * c;
    const char* g = w1w + c * 192 + (size_t)kt * 192 * 9216 + r * 16 * 9216;
    LDSP char* l = smem + kt * K1_SLOT + w * 9216 + r * 3072;
#pragma unroll
    for (int i = 0; i < 3; ++i) glds16(g, off3[i], l + i * 1024);
  };
  auto issue_w1 = [&](int st) {
#pragma unroll
    for (int r = 0; r < 3; ++r) issue_w1_rows(st, r);
  };
  auto issue_x = [&](int q, int slot) {
    int ln = tid & 63; asm volatile("" : "+v"(ln));
    const char* g = (const char*)x + (size_t)(tt * 32) * 1152 + q * 288;
    LDSP char* l = smem + slot * K1_SLOT + w * 9216;
#pragma unroll
    for (int i = 0; i < 9; ++i) {
      const unsigned p = 64 * i + ln, row = p / 18, pos = p % 18;
      glds16(g, row * 1152 + ((pos ^ ((row >> 3) & 1)) << 4), l + i * 1024);
    }
  };
  auto issue_qkv = [&](int tile, int slot) {
    int ln = tid & 63; asm volatile("" : "+v"(ln));
    LDSP char* l = smem + slot * K1_SLOT + w * 9216;
    if (tile < 18) {
      const char* g = (const char*)wq + (size_t)(144 * w) * 1152 + tile * 64;
      const unsigned vo = (ln >> 2) * 1152 + (ln & 3) * 16;
#pragma unroll
      for (int i = 0; i < 9; ++i) glds16(g + i * 16 * 1152, vo, l + i * 1024);
    } else {
      const u16* src = (tile == 18) ? wk : wv;
      int dw = ln & 15; dw = dw > 8 ? 8 : dw;
      const char* g = (const char*)src + (size_t)(144 * w) * 36;
      const unsigned vo = (ln >> 4) * 36 + dw * 4;
#pragma unroll
      for (int i = 0; i < 36; ++i) glds4(g + i * 4 * 36, vo, l + i * 256);
    }
  };
  const int lw = (tg * 12 + (j - (j >> 2) - 1)) * 4 + w;
  auto issue_unit = [&]() {
    if (lw < 648) {
      int ln = tid & 63; asm volatile("" : "+v"(ln));
      LDSP char* l = smem + 3 * K1_SLOT + w * 9216;
#pragma unroll
      for (int i = 0; i < 9; ++i) {
        const int bb = 1024 * i + 16 * ln;
        const int row = bb / 576, cb = bb % 576;
        if (lw < 576) {
          glds16((const char*)w2 + (size_t)((lw >> 1) * 16) * 1152 + (lw & 1) * 576, row * 1152 + cb, l + i * 1024);
        } else {
          const int u = lw - 576, ks = u >> 1;
          const int crow = ks < 32 ? ks * 18 + row : 18 * (8 * (ks - 32) + (row >> 1)) + 16 + (row & 1);
          glds16((const char*)wo + (u & 1) * 576, crow * 1152 + cb, l + i * 1024);
        }
      }
    }
  };

  if (grp == 0) { issue_x(0, 3); issue_x(1, 1); }
  else          { issue_w1(0);   issue_x(2, 2); }
  if (tid < 288) *(LDSP float*)(smem + K1_BIAS + tid * 4) = bf2f(b1[j * 288 + tid]);
  WAIT_VM(0); WAIT_LGKM0(); BAR();
  STAMP(1);
  s16x8 xf[36];
  const int xl = w * 9216 + t32 * 288 + ((hh ^ ((t32 >> 3) & 1)) << 4);
#pragma unroll
  for (int s = 0; s < 9; ++s) {
    xf[s]      = *(LDSP s16x8*)(smem + 3 * K1_SLOT + xl + s * 32);
    xf[9 + s]  = *(LDSP s16x8*)(smem + 1 * K1_SLOT + xl + s * 32);
    xf[18 + s] = *(LDSP s16x8*)(smem + 2 * K1_SLOT + xl + s * 32);
  }
  WAIT_LGKM0(); BAR();
  STAMP(2);

  const int q4 = (lane & 15) >> 2, p4 = lane & 3, g16 = (lane >> 4) & 1;
  const int rot = (w == 3) ? 0 : w;
  const int tl0 = rot, tl1 = (rot + 1) % 3, tl2 = (rot + 2) % 3;
  const int lo_w1 = (8 * hh + q4) * 192 + 32 * g16 + 8 * p4;
  const int lo_q  = (8 * hh + q4) * 64 + 32 * g16 + 8 * p4;
  char* hout = ws + OFF_H + ((size_t)tt * 288 + (size_t)j * 18) * 1024 + lane * 16;
  f32x16 acc[3];
  float negm = 0.f, r1 = 0.f, rm = 0.f;
  const unsigned selv = ((lane & 15) == ((lane >> 4) & 1)) ? 0x3f803f80u : 0u;
  const u32x4 selu = {selv, selv, selv, selv};
  const s16x8 sel = __builtin_bit_cast(s16x8, selu);

  auto read_x3 = [&]() {
#pragma unroll
    for (int s = 0; s < 9; ++s) xf[27 + s] = *(LDSP s16x8*)(smem + 3 * K1_SLOT + xl + s * 32);
  };
  auto load_stats = [&]() {
    f32x2 st = *(LDSP f32x2*)(smem + K1_STAT + (w * 32 + t32) * 8);
    negm = st[0];
    r1 = __builtin_amdgcn_rsqf(st[1] + 1e-5f);
    rm = r1 * __builtin_amdgcn_rsqf(st[1] * r1 * r1 + 1e-5f);
  };
  auto mfma_chunk = [&](int c, bool first) {
    f32x4 cs = {0.f, 0.f, 0.f, 0.f};
#pragma unroll
    for (int t = 0; t < 3; ++t)
#pragma unroll
      for (int i = 0; i < 16; ++i) acc[t][i] = 0.f;
    LDSP char* s0 = smem + lo_w1 + tl0 * 64;
    LDSP char* s1 = smem + lo_w1 + tl1 * 64;
    LDSP char* s2 = smem + lo_w1 + tl2 * 64;
    w1_stage<0>(s0, s1, s2, xf, acc, sel, cs);
    WAIT_VM(0); WAIT_LGKM0(); BAR();
    if (first) read_x3();
    w1_stage<1>(s0 + K1_SLOT, s1 + K1_SLOT, s2 + K1_SLOT, xf, acc, sel, cs);
    WAIT_LGKM0(); BAR();
    w1_stage<2>(s0 + 2 * K1_SLOT, s1 + 2 * K1_SLOT, s2 + 2 * K1_SLOT, xf, acc, sel, cs);
    if (lane < 16 && w < 3) {
      *(LDSP float*)(smem + K1_CS + (c * 96 + tl0 * 32 + lane) * 4) = cs[0];
      *(LDSP float*)(smem + K1_CS + (c * 96 + tl0 * 32 + 16 + lane) * 4) = cs[1];
    }
    WAIT_LGKM0(); BAR();
  };
  auto gelu_t = [&](int c, int t, int tl, int st_issue) {
    gelu_tile(acc[t], smem + K1_CS + (c * 96 + tl * 32) * 4 + hh * 16, smem + K1_BIAS + (c * 96 + tl * 32) * 4 + hh * 16,
              hout + (c * 6 + tl * 2) * 1024, negm, rm, [&](int i) { if (st_issue >= 0 && i < 3) issue_w1_rows(st_issue, i); });
  };

  if (grp == 0) {
    mfma_chunk(0, true);
    STAMP(3);
    load_stats();
    gelu_t(0, 0, tl0, 5); WAIT_VM(11); WAIT_LGKM0(); BAR();
    gelu_t(0, 1, tl1, 6); WAIT_VM(11); WAIT_LGKM0(); BAR();
    gelu_t(0, 2, tl2, 7); WAIT_VM(11); WAIT_LGKM0(); BAR();
    STAMP(4);
    mfma_chunk(2, false);
    STAMP(5);
    gelu_t(2, 0, tl0, -1); gelu_t(2, 1, tl1, -1); gelu_t(2, 2, tl2, -1);
    STAMP(6);
  } else {
    float sm[2] = {0.f, 0.f}, sq[2] = {0.f, 0.f};
    auto stats_frag = [&](int s) {
      u32x4 u = __builtin_bit_cast(u32x4, xf[s]);
      float l0, l1, h0, h1;
#pragma unroll
      for (int i = 0; i < 4; i += 2) {
        asm volatile("v_lshlrev_b32 %4, 16, %8\n\tv_and_b32 %5, 0xffff0000, %8\n\tv_lshlrev_b32 %6, 16, %9\n\tv_and_b32 %7, 0xffff0000, %9\n\t"
                     "v_add_f32 %0, %0, %4\n\tv_add_f32 %1, %1, %5\n\tv_fmac_f32 %2, %4, %4\n\tv_fmac_f32 %3, %5, %5\n\t"
                     "v_add_f32 %0, %0, %6\n\tv_add_f32 %1, %1, %7\n\tv_fmac_f32 %2, %6, %6\n\tv_fmac_f32 %3, %7, %7"
                     : "+v"(sm[0]), "+v"(sm[1]), "+v"(sq[0]), "+v"(sq[1]), "=&v"(l0), "=&v"(h0), "=&v"(l1), "=&v"(h1)
                     : "v"(u[i]), "v"(u[i + 1]));
      }
    };
    issue_w1(1);
    issue_x(3, 3);
#pragma unroll
    for (int s = 0; s < 9; ++s) { stats_frag(s); if (s % 3 == 2) issue_w1_rows(2, s / 3); }
    WAIT_VM(9); WAIT_LGKM0(); BAR();
    read_x3();
#pragma unroll
    for (int s = 9; s < 24; ++s) { if (s % 5 == 4) issue_w1_rows(3, (s - 9) / 5); stats_frag(s); }
    WAIT_VM(9); WAIT_LGKM0(); BAR();
    if (heavy) issue_qkv(16 + (j >> 2), 3); else issue_unit();
#pragma unroll
    for (int s = 24; s < 36; ++s) { if (s % 4 == 0) issue_w1_rows(4, (s - 24) / 4); stats_frag(s); }
    {
      float s1 = sm[0] + sm[1], s2 = sq[0] + sq[1];
      s1 += other_half(s1); s2 += other_half(s2);
      const float mean = s1 * (1.0f / 576.0f);
      const float var = __builtin_fmaxf(s2 * (1.0f / 576.0f) - mean * mean, 0.f);
      f32x2 st = {-mean, var};
      if (hh == 0) *(LDSP f32x2*)(smem + K1_STAT + (w * 32 + t32) * 8) = st;
    }
    WAIT_VM(9); WAIT_LGKM0(); BAR();
    STAMP(3);
    mfma_chunk(1, false);
    STAMP(4);
    load_stats();
    gelu_t(1, 0, tl0, 8); WAIT_VM(11); WAIT_LGKM0(); BAR();
    issue_qkv(j, 0);  gelu_t(1, 1, tl1, -1); WAIT_VM(11); WAIT_LGKM0(); BAR();
    gelu_t(1, 2, tl2, -1);
    WAIT_VM(2); WAIT_LGKM0(); BAR();
    STAMP(5);

    auto qkv_tile = [&](int tile, int slot) {
      f32x16 qa;
#pragma unroll
      for (int i = 0; i < 16; ++i) qa[i] = 0.f;
      f32x4 cs = {0.f, 0.f, 0.f, 0.f};
      qkv_stage(smem + slot * K1_SLOT + lo_q, xf, qa, sel, cs);
      int ln = tid & 63; asm volatile("" : "+v"(ln));
      const int t32 = ln & 31, hh = ln >> 5, token = tt * 32 + t32;
      LDSP char* cp = smem + K1_CSQ + (w * 2 + (slot & 1)) * 128;
      if (ln < 16) { *(LDSP float*)(cp + ln * 4) = cs[0]; *(LDSP float*)(cp + 64 + ln * 4) = cs[1]; }
      WAIT_LGKM0();
#pragma unroll
      for (int g = 0; g < 4; ++g) {
        f32x4 c4 = *(LDSP f32x4*)(cp + g * 32 + hh * 16);
#pragma unroll
        for (int i = 0; i < 4; ++i) qa[4 * g + i] = __builtin_fmaf(negm, c4[i], qa[4 * g + i]);
      }
      if (tile < 18) {
        const float sc = r1 * (0.23570226039551584f * 1.4426950408889634f);
        char* qp = ws + OFF_Q + (size_t)tt * 36864;
#pragma unroll
        for (int i = 0; i < 8; ++i) {
          const unsigned n = tile * 32 + 8 * (i >> 1) + 4 * hh + 2 * (i & 1);
          const unsigned head = n / 18, d = n - head * 18;
          const unsigned off = d < 16 ? (t32 + 32 * ((d >> 2) & 1)) * 16 + ((d >> 3) * 2 + ((d >> 1) & 1)) * 4 : 1024 + t32 * 4;
          *(unsigned*)(qp + head * 1152 + off) = pack2(qa[2 * i] * sc, qa[2 * i + 1] * sc);
        }
      } else if (tile == 18) {
        u32x4 o = {pack2(qa[0] * r1, qa[1] * r1), pack2(qa[2] * r1, qa[3] * r1),
                   pack2(qa[4] * r1, qa[5] * r1), pack2(qa[6] * r1, qa[7] * r1)};
        char* gp = ws + OFF_KV + (size_t)tg * KV_GRP;
        *(u32x4*)(gp + 1024 + w * KV_TILE + ln * 16) = o;
        if (hh == 0) *(unsigned*)(gp + (w >> 1) * 512 + t32 * 16 + (w & 1) * 4) = pack2(qa[8] * r1, qa[9] * r1);
        else if ((w & 1) == 0) {
          u32x2 cst = {w == 0 ? 0x3f803f80u : 0u, 0u};
          *(u32x2*)(gp + (w >> 1) * 512 + t32 * 16 + 8) = cst;
        }
      } else {
        const int s = t32 >> 4, hk = (t32 >> 2) & 1, jj = ((t32 >> 3) & 1) * 4 + (t32 & 3);
        char* vp = ws + OFF_KV + (size_t)tg * KV_GRP + 2048 + w * KV_TILE + (s * 2 + hk) * 304 + jj * 2;
#pragma unroll
        for (int i = 0; i < 8; ++i) {
          const int d = (i & 3) + 8 * (i >> 2) + 4 * hh;
          *(u16*)(vp + d * 16) = (u16)(pack2(qa[i] * r1, 0.f) & 0xffffu);
        }
        if (hh == 0) {
          *(u16*)(vp + 16 * 16) = (u16)(pack2(qa[8] * r1, 0.f) & 0xffffu);
          *(u16*)(vp + 17 * 16) = (u16)(pack2(qa[9] * r1, 0.f) & 0xffffu);
          *(u16*)(vp + 18 * 16) = (u16)0x3f80;
        }
      }
    };
    qkv_tile(j, 0);
    STAMP(6);
    if (heavy) {
      qkv_tile(16 + (j >> 2), 3);
    } else if (lw < 648) {
      int ln = tid & 63; asm volatile("" : "+v"(ln));
      const int hh = ln >> 5, q4 = (ln & 15) >> 2, p4 = ln & 3, g16 = (ln >> 4) & 1;
      LDSP char* sb = smem + 3 * K1_SLOT + w * 9216 + (4 * hh + q4) * 576 + 32 * g16 + 8 * p4;
      char* dst = (lw < 576) ? ws + OFF_W2T + ((size_t)(lw >> 1) * 18 + (lw & 1) * 9) * 1024
                             : ws + OFF_WOT + ((size_t)((lw - 576) >> 1) * 18 + (lw & 1) * 9) * 1024;
#pragma unroll
      for (int n = 0; n < 9; ++n) {
        s16x8 f = tr_frag(sb + n * 64, 8 * 576);
        *(s16x8*)(dst + n * 1024 + ln * 16) = f;
      }
    }
  }
  STAMP(7);
  STAMP(8);
  TS_FLUSH(0);
}

constexpr int K2_FLAG = KV_BYTES;
constexpr int K2_PF   = KV_BYTES + 128;
constexpr int K2_LDS  = KV_BYTES + 128 + 256;
enum { FL_KVA = 0, FL_KVG = 1, FL_PROG = 2, FL_GW = 3, FL_DONE = 4, FL_SENT = 5, FL_GW2 = 6 };

#define EXP2(x) __builtin_amdgcn_exp2f(x)

DI void flag_set(LDSP char* smem, int set, int i, int lane) {
  if (lane == 0) *(volatile LDSP unsigned*)(smem + K2_FLAG + set * 16 + i * 4) = 1u;
}
DI void flag_wait_all(LDSP char* smem, int set) {
  while (true) {
    u32x4 f = *(volatile LDSP u32x4*)(smem + K2_FLAG + set * 16);
    if (f[0] & f[1] & f[2] & f[3]) break;
    __builtin_amdgcn_s_sleep(2);
  }
  asm volatile("" ::: "memory");
}
DI void flag_wait_one(LDSP char* smem, int set, int i) {
  while (*(volatile LDSP unsigned*)(smem + K2_FLAG + set * 16 + i * 4) == 0u) __builtin_amdgcn_s_sleep(1);
  asm volatile("" ::: "memory");
}

DI void attn_step(f32x16& scur, f32x16& snext, const s16x8& k0, const s16x8& k1, const s16x8& qn0, const s16x8& qn1,
                  const s16x8& vprev1, f32x16& oprev, const s16x8& vcur0, f32x16& ocur, u32x4& p0, u32x4& p1) {
  f32x16 zero;
#pragma unroll
  for (int i = 0; i < 16; ++i) zero[i] = 0.f;
#define EX(i) scur[i] = EXP2(scur[i])
#define PRIO_LO() do { SCHED(); __builtin_amdgcn_s_setprio(0); SCHED(); } while (0)
#define PRIO_HI() do { SCHED(); __builtin_amdgcn_s_setprio(2); SCHED(); } while (0)
  p1[2] = pack2(snext[12], snext[13]);
  p1[3] = pack2(snext[14], snext[15]);
  SCHED();
  snext = MFMA32(k0, qn0, zero);
  SCHED();
  EX(0); EX(1); EX(2);
  SCHED();
  snext = MFMA32(k1, qn1, snext);
  SCHED();
  EX(3); EX(4);
  PRIO_LO();
  EX(5); EX(6);
  PRIO_HI();
  EX(7);
  SCHED();
  p0[0] = pack2(scur[0], scur[1]);
  p0[1] = pack2(scur[2], scur[3]);
  p0[2] = pack2(scur[4], scur[5]);
  p0[3] = pack2(scur[6], scur[7]);
  SCHED();
  oprev = MFMA32(vprev1, __builtin_bit_cast(s16x8, p1), oprev);
  SCHED();
  EX(8); EX(9); EX(10);
  SCHED();
  ocur = MFMA32(vcur0, __builtin_bit_cast(s16x8, p0), ocur);
  SCHED();
  EX(11); EX(12);
  PRIO_LO();
  EX(13); EX(14);
  PRIO_HI();
  EX(15);
  SCHED();
  p1[0] = pack2(scur[8], scur[9]);
  p1[1] = pack2(scur[10], scur[11]);
  SCHED();
#undef EX
}

DI s16x8 make_q1(unsigned qe, unsigned mneg, int i, int hh) {
  const unsigned a = (hh == (i >> 1)) ? qe : 0u;
  u32x4 r = {(i & 1) ? 0u : a, (i & 1) ? a : 0u, hh ? 0u : mneg, 0u};
  return __builtin_bit_cast(s16x8, r);
}
DI unsigned neg_hi_lo(float m) {
  unsigned hi = pack2(-m, 0.f) & 0xffffu;
  float rem = -m - __builtin_bit_cast(float, hi << 16);
  unsigned lo = pack2(rem, 0.f) & 0xffffu;
  return hi | (lo << 16);
}
DI int v_row(int t32) { return t32 > 18 ? ((t32 >= 20 && t32 < 28) ? 0 : 4) : t32; }

DI f32x16 attn_exact(const char* kv, int lane, u32x4 q0u, unsigned qe) {
  const int t32 = lane & 31, hh = lane >> 5;
  const s16x8 q0 = __builtin_bit_cast(s16x8, q0u);
  f32x16 zero, o;
#pragma unroll
  for (int i = 0; i < 16; ++i) { zero[i] = 0.f; o[i] = 0.f; }
  float m = -INFINITY;
#pragma unroll 1
  for (int T = 0; T < 64; ++T) {
    const char* gp = kv + (T >> 2) * KV_GRP;
    s16x8 k0 = *(const s16x8*)(gp + 1024 + (T & 3) * KV_TILE + lane * 16);
    s16x8 k1 = *(const s16x8*)(gp + lane * 16);
    f32x16 s = MFMA32(k0, q0, zero);
    s = MFMA32(k1, make_q1(qe, 0u, T & 3, hh), s);
#pragma unroll
    for (int i = 0; i < 16; ++i) m = __builtin_fmaxf(m, s[i]);
  }
  m = __builtin_fmaxf(m, other_half(m));
  const unsigned mneg = neg_hi_lo(m);
#pragma unroll 1
  for (int T = 0; T < 64; ++T) {
    const char* gp = kv + (T >> 2) * KV_GRP;
    const char* tp = gp + 1024 + (T & 3) * KV_TILE;
    s16x8 k0 = *(const s16x8*)(tp + lane * 16);
    s16x8 k1 = *(const s16x8*)(gp + lane * 16);
    s16x8 v0 = *(const s16x8*)(tp + 1024 + hh * 304 + v_row(t32) * 16);
    s16x8 v1 = *(const s16x8*)(tp + 1024 + 608 + hh * 304 + v_row(t32) * 16);
    f32x16 s = MFMA32(k0, q0, zero);
    s = MFMA32(k1, make_q1(qe, mneg, T & 3, hh), s);
    u32x4 p0, p1;
#pragma unroll
    for (int i = 0; i < 4; ++i) {
      p0[i] = pack2(EXP2(s[2 * i]), EXP2(s[2 * i + 1]));
      p1[i] = pack2(EXP2(s[8 + 2 * i]), EXP2(s[8 + 2 * i + 1]));
    }
    o = MFMA32(v0, __builtin_bit_cast(s16x8, p0), o);
    o = MFMA32(v1, __builtin_bit_cast(s16x8, p1), o);
  }
  return o;
}

template <int NT>
DI void gemm2_load(const char* hp, const char* wp, const char* wsp, unsigned lo16, int k, s16x8 (&hb)[2], s16x8 (&wb)[5]) {
  const GLP char* h0 = (const GLP char*)hp + (size_t)k * 1024;
  const GLP char* h1 = h0 + (size_t)288 * 1024;
  const GLP char* w0 = (const GLP char*)wp + (size_t)k * 18432;
  const GLP char* w4 = (const GLP char*)wsp + (size_t)k * 18432;
  asm volatile("" : "+s"(h0), "+s"(h1), "+s"(w0), "+s"(w4));
  hb[0] = *(const GLP s16x8*)(h0 + lo16);
  wb[0] = *(const GLP s16x8*)(w0 + lo16);
  hb[1] = *(const GLP s16x8*)(h1 + lo16);
#pragma unroll
  for (int n = 1; n < 4; ++n) wb[n] = *(const GLP s16x8*)(w0 + n * 1024 + lo16);
  if (NT == 5) wb[4] = *(const GLP s16x8*)(w4 + lo16);
}
#define GEM_PAUSE()
template <int NT, int NTN>
DI void gemm2_step(const char* hp, const char* wp, const char* wsp, unsigned lo16, int kn, s16x8 (&hb)[2], s16x8 (&wb)[5],
                   f32x16 (&acc)[2][5]) {
  const GLP char* h0 = (const GLP char*)hp + (size_t)kn * 1024 + lo16;
  const GLP char* w0 = (const GLP char*)wp + (size_t)kn * 18432 + lo16;
  const GLP char* w4 = (const GLP char*)wsp + (size_t)kn * 18432 + lo16;
#pragma unroll
  for (int n = 0; n < NT; ++n) {
    acc[0][n] = MFMA32(wb[n], hb[0], acc[0][n]);
    SCHED(); GEM_PAUSE(); SCHED();
    acc[1][n] = MFMA32(wb[n], hb[1], acc[1][n]);
    SCHED();
    if (n < 4) wb[n] = *(const GLP s16x8*)(w0 + n * 1024);
    else if (NTN == 5) wb[4] = *(const GLP s16x8*)(w4);
    GEM_PAUSE(); SCHED();
  }
  if (NT == 4 && NTN == 5) wb[4] = *(const GLP s16x8*)(w4);
  hb[0] = *(const GLP s16x8*)(h0);
  hb[1] = *(const GLP s16x8*)(h0 + (size_t)288 * 1024);
  SCHED();
}
template <int N1, int N2, typename F, typename G>
DI void gemm2_all(const char* hp, const char* wp, const char* wsp, unsigned lo16, f32x16 (&acc)[2][5], F&& after_first_loads, G&& after_first_step) {
  s16x8 hA[2], wA[5], hB[2], wB[5];
  after_first_loads();
  gemm2_load<N1>(hp, wp, wsp, lo16, 0, hA, wA);
  gemm2_load<N1>(hp, wp, wsp, lo16, 1, hB, wB);
  SCHED();
  after_first_step();
  gemm2_step<N1, N1>(hp, wp, wsp, lo16, 2, hA, wA, acc);
  gemm2_step<N1, N1>(hp, wp, wsp, lo16, 3, hB, wB, acc);
#pragma unroll 1
  for (int k = 2; k < 16; k += 2) {
    gemm2_step<N1, N1>(hp, wp, wsp, lo16, k + 2, hA, wA, acc);
    gemm2_step<N1, N1>(hp, wp, wsp, lo16, k + 3, hB, wB, acc);
  }
  gemm2_step<N1, N2>(hp, wp, wsp, lo16, 18, hA, wA, acc);
  gemm2_step<N1, N2>(hp, wp, wsp, lo16, 19, hB, wB, acc);
#pragma unroll 1
  for (int k = 18; k < 36; k += 2) {
    gemm2_step<N2, N2>(hp, wp, wsp, lo16, (k + 2 < 36) ? k + 2 : 35, hA, wA, acc);
    gemm2_step<N2, N2>(hp, wp, wsp, lo16, (k + 3 < 36) ? k + 3 : 35, hB, wB, acc);
  }
}

__global__ __launch_bounds__(512, 2)
void k2(char* __restrict__ ws) {
  extern __shared__ __attribute__((aligned(16))) char smem_[];
  LDSP char* smem = (LDSP char*)smem_;
  const int tid = threadIdx.x, lane = tid & 63;
  const int w = __builtin_amdgcn_readfirstlane(tid >> 6);
  const int b = blockIdx.x;
  const int t32 = lane & 31, hh = lane >> 5;
  TS_DECL; STAMP(0);
  if (tid < 32) *(LDSP unsigned*)(smem + K2_FLAG + tid * 4) = 0u;
  const char* kvg = ws + OFF_KV;

  if (w < 4) {
#define ATT_PRIO 2
#define GEM_PRIO 1
    __builtin_amdgcn_s_setprio(ATT_PRIO);
    const int tt = b >> 2, hg = b & 3;
    const int rs = w & 1, kh2 = w >> 1;
    const int head0 = hg * 8 + rs * 4;
#pragma unroll
    for (int i = 0; i < 5; ++i) {
      glds16(kvg + (i * 4 + w) * 1024, lane * 16, smem + (i * 4 + w) * 1024);
      glds16(kvg + KV_HALF + (i * 4 + w) * 1024, lane * 16, smem + KV_HALF + (i * 4 + w) * 1024);
    }
    u32x4 q0u[4];
    unsigned qe[4];
#pragma unroll
    for (int qb = 0; qb < 4; ++qb) {
      const char* qp = ws + OFF_Q + (size_t)tt * 36864 + (head0 + qb) * 1152;
      q0u[qb] = *(const u32x4*)(qp + lane * 16);
      qe[qb] = *(const unsigned*)(qp + 1024 + t32 * 4);
    }
    const char* pf_base;
    unsigned pf_off, pf_step;
    {
      const int xcd = b & 7, a = (b >> 3) * 4 + w, kh = xcd & 1;
      if (a < 48) {
        const unsigned L = (a % 3) * 64 + lane;
        pf_base = ws + OFF_H + ((size_t)((xcd >> 1) * 16 + a / 3) * 288 + kh * 144) * 1024;
        pf_off = (L / 48) * 36 * 1024 + (L % 48) * 128;
        pf_step = 6 * 1024;
      } else {
        const unsigned L = ((a < 102 ? a : 101) - 48) * 64 + lane;
        pf_base = ws + OFF_W2T + (size_t)kh * 144 * 18432;
        pf_off = (L / 864) * 36 * 18432 + (L % 864) * 128;
        pf_step = 6 * 18432;
      }
    }
    WAIT_LGKM0(); BAR();
    WAIT_VM(0);
    flag_set(smem, FL_KVA, w, lane);
    flag_wait_all(smem, FL_KVA);
    STAMP(1);
    s16x8 q0[4], q1[4][4];
    f32x16 o[4];
    f32x16 sa, sb;
#pragma unroll
    for (int i = 0; i < 16; ++i) sb[i] = 0.f;
    {
      s16x8 k0 = *(LDSP s16x8*)(smem + 1024 + lane * 16);
      s16x8 k1 = *(LDSP s16x8*)(smem + lane * 16);
#pragma unroll
      for (int qb = 0; qb < 4; ++qb) {
        asm volatile("" : "+v"(q0u[qb]), "+v"(qe[qb]));
        q0[qb] = __builtin_bit_cast(s16x8, q0u[qb]);
        f32x16 s = MFMA32(k0, q0[qb], sb);
        s = MFMA32(k1, make_q1(qe[qb], 0u, 0, hh), s);
        float m = s[0];
#pragma unroll
        for (int i = 1; i < 16; ++i) m = __builtin_fmaxf(m, s[i]);
        m = __builtin_fmaxf(m, other_half(m));
        const unsigned mneg = neg_hi_lo(m);
#pragma unroll
        for (int i = 0; i < 4; ++i) q1[i][qb] = make_q1(qe[qb], mneg, i, hh);
#pragma unroll
        for (int i = 0; i < 16; ++i) o[qb][i] = 0.f;
      }
    }
    LDSP char* kp = smem + kh2 * KV_HALF + lane * 16;
    LDSP char* vp = smem + kh2 * KV_HALF + 2048 + hh * 304 + v_row(t32) * 16;
    u32x4 p0 = {0u, 0u, 0u, 0u}, p1 = {0u, 0u, 0u, 0u};
    s16x8 k1c = *(LDSP s16x8*)(kp), k1n;
    s16x8 k0b[2], v0b[2], v1b[2];
    k0b[0] = *(LDSP s16x8*)(kp + 1024);
    v0b[0] = *(LDSP s16x8*)(vp);
    v1b[1] = v0b[0];
    sa = MFMA32(k0b[0], q0[0], sb);
    sa = MFMA32(k1c, q1[0][0], sa);
#pragma unroll 1
    for (int g = 0; g < 8; ++g) {
      if (g == 1) flag_wait_all(smem, FL_KVG);
      if (g == 5) flag_set(smem, FL_PROG, w, lane);
      const int nxg = (g < 7) ? KV_GRP : 0;
#pragma unroll
      for (int i = 0; i < 4; ++i) {
        const int c = i & 1, n = c ^ 1;
        const int nt_off = (i < 3) ? (i + 1) * KV_TILE : nxg;
        v1b[c] = *(LDSP s16x8*)(vp + i * KV_TILE + 608);
        SCHED();
        attn_step(sa, sb, k0b[c], k1c, q0[1], q1[i][1], v1b[n], o[3], v0b[c], o[0], p0, p1);
        k0b[n] = *(LDSP s16x8*)(kp + 1024 + nt_off);
        if (i == 3) k1n = *(LDSP s16x8*)(kp + nxg);
        SCHED();
        attn_step(sb, sa, k0b[c], k1c, q0[2], q1[i][2], v1b[c], o[0], v0b[c], o[1], p0, p1);
        v0b[n] = *(LDSP s16x8*)(vp + nt_off);
        SCHED();
        attn_step(sa, sb, k0b[c], k1c, q0[3], q1[i][3], v1b[c], o[1], v0b[c], o[2], p0, p1);
        attn_step(sb, sa, k0b[n], (i == 3) ? k1n : k1c, q0[0], q1[(i + 1) & 3][0], v1b[c], o[2], v0b[c], o[3], p0, p1);
      }
      k1c = k1n;
      kp += KV_GRP; vp += KV_GRP;
    }
    p1[2] = pack2(sb[12], sb[13]);
    p1[3] = pack2(sb[14], sb[15]);
    o[3] = MFMA32(v1b[1], __builtin_bit_cast(s16x8, p1), o[3]);
    STAMP(2);
    f32x16 keep[2], send[2];
    u32x4 qk0[2];
    unsigned qke[2];
#pragma unroll
    for (int q = 0; q < 2; ++q) {
#pragma unroll
      for (int i = 0; i < 11; ++i) {
        keep[q][i] = kh2 ? o[2 + q][i] : o[q][i];
        send[q][i] = kh2 ? o[q][i] : o[2 + q][i];
      }
#pragma unroll
      for (int i = 11; i < 16; ++i) { keep[q][i] = 0.f; send[q][i] = 0.f; }
#pragma unroll
      for (int i = 0; i < 4; ++i) qk0[q][i] = kh2 ? q0u[2 + q][i] : q0u[q][i];
      { unsigned a_ = qe[q], b_ = qe[2 + q]; asm volatile("" : "+v"(a_), "+v"(b_)); qke[q] = kh2 ? b_ : a_; }
    }
    WAIT_LGKM0();
    flag_set(smem, FL_DONE, w, lane);
    flag_wait_one(smem, FL_DONE, w ^ 1);
    {
      LDSP char* xp = smem + kh2 * KV_HALF + 7 * KV_GRP + rs * 4864;
#pragma unroll
      for (int q = 0; q < 2; ++q) {
        const f32x16 ov = send[q];
        *(LDSP f32x4*)(xp + q * 2432 + lane * 16) = f32x4{ov[0], ov[1], ov[2], ov[3]};
        *(LDSP f32x4*)(xp + q * 2432 + 1024 + lane * 16) = f32x4{ov[4], ov[5], ov[6], ov[7]};
        if (hh == 0) {
          *(LDSP float*)(xp + q * 2432 + 2048 + t32 * 4) = ov[8];
          *(LDSP float*)(xp + q * 2432 + 2176 + t32 * 4) = ov[9];
          *(LDSP float*)(xp + q * 2432 + 2304 + t32 * 4) = ov[10];
        }
      }
    }
    WAIT_LGKM0();
    flag_set(smem, FL_SENT, w, lane);
    flag_wait_one(smem, FL_SENT, w ^ 2);
#pragma unroll
    for (int q = 0; q < 2; ++q) {
      f32x16 ov = keep[q];
      LDSP char* xp = smem + (kh2 ^ 1) * KV_HALF + 7 * KV_GRP + rs * 4864 + q * 2432;
      f32x4 a0 = *(LDSP f32x4*)(xp + lane * 16), a1 = *(LDSP f32x4*)(xp + 1024 + lane * 16);
#pragma unroll
      for (int i = 0; i < 4; ++i) { ov[i] += a0[i]; ov[4 + i] += a1[i]; }
      ov[8] += *(LDSP float*)(xp + 2048 + t32 * 4);
      ov[9] += *(LDSP float*)(xp + 2176 + t32 * 4);
      ov[10] += *(LDSP float*)(xp + 2304 + t32 * 4);
      const int qb = 2 * kh2 + q;
#pragma unroll 1
      for (int attempt = 0; attempt < 2; ++attempt) {
        float l = ov[10];
        const float lo_ = other_half(l);
        l = hh ? lo_ : l;
        float mx = l, chk = l;
#pragma unroll
        for (int i = 0; i < 8; ++i) { mx = __builtin_fmaxf(mx, __builtin_fabsf(ov[i])); chk += ov[i]; }
        if (hh == 0) { mx = __builtin_fmaxf(mx, __builtin_fmaxf(__builtin_fabsf(ov[8]), __builtin_fabsf(ov[9]))); chk += ov[8] + ov[9]; }
        const bool bad = !(mx < 1e30f) || !(l < 1e18f) || (chk != chk);
        if (attempt == 0 && __any(bad)) {
          ov = attn_exact(kvg, lane, qk0[q], qke[q]);
          continue;
        }
        const float li = __builtin_amdgcn_rcpf(l);
        const int head = head0 + qb;
        u32x4 o4 = {pack2(ov[0] * li, ov[1] * li), pack2(ov[2] * li, ov[3] * li),
                    pack2(ov[4] * li, ov[5] * li), pack2(ov[6] * li, ov[7] * li)};
        char* ap = ws + OFF_AO + (size_t)tt * 36 * 1024;
        *(u32x4*)(ap + head * 1024 + lane * 16) = o4;
        if (hh == 0) {
          const int h8 = head & 7;
          *(unsigned*)(ap + (32 + (head >> 3)) * 1024 + (t32 + 32 * ((h8 >> 1) & 1)) * 16 + ((h8 >> 2) * 2 + (h8 & 1)) * 4) =
              pack2(ov[8] * li, ov[9] * li);
        }
        break;
      }
    }
    STAMP(3);
  } else {
    __builtin_amdgcn_s_setprio(GEM_PRIO);
    const int g = w - 4;
    const int xcd = b & 7, idx = b >> 3;
    const int kh = xcd & 1, tp = (xcd >> 1) * 8 + (idx & 7), ng = idx >> 3;
    const int nt0 = (ng >> 1) * 9 + (ng & 1) * 5;
    const int nts = (ng >> 1) * 9 + 4;
    const int fb0 = kh * 144 + g * 36;
    const char* hp0 = ws + OFF_H + ((size_t)(2 * tp) * 288 + fb0) * 1024;
    const char* wp = ws + OFF_W2T + ((size_t)fb0 * 18 + nt0) * 1024;
    const char* wsp = ws + OFF_W2T + ((size_t)fb0 * 18 + nts) * 1024;
    const unsigned lo16 = lane * 16;
    BAR();
    f32x16 acc[2][5];
#pragma unroll
    for (int a = 0; a < 2; ++a)
#pragma unroll
      for (int n = 0; n < 5; ++n)
#pragma unroll
        for (int i = 0; i < 16; ++i) acc[a][n][i] = 0.f;
    auto stage_kv = [&]() {
      __builtin_amdgcn_s_setprio(3);
#pragma unroll 1
      for (int i = g; i < 58; i += 4) {
        glds16(kvg + (20 + i) * 1024, lo16, smem + (20 + i) * 1024);
        glds16(kvg + KV_HALF + (20 + i) * 1024, lo16, smem + KV_HALF + (20 + i) * 1024);
      }
      __builtin_amdgcn_s_setprio(GEM_PRIO);
      STAMP(6);
    };
    auto kv_ready = [&]() {
      WAIT_VM(12);
      flag_set(smem, FL_KVG, g, lane);
      STAMP(1);
    };
    if (ng & 1) gemm2_all<4, 5>(hp0, wp, wsp, lo16, acc, stage_kv, kv_ready);
    else        gemm2_all<5, 4>(hp0, wp, wsp, lo16, acc, stage_kv, kv_ready);
    STAMP(2);
    flag_wait_all(smem, FL_PROG);
    STAMP(3);
    LDSP char* mine = smem + (g >> 1) * KV_HALF + (g & 1) * 24576 + lo16;
    {
      f32x16 kp_[5];
#pragma unroll
      for (int n = 0; n < 5; ++n) {
#pragma unroll
        for (int q = 0; q < 4; ++q) {
          f32x4 v;
#pragma unroll
          for (int i = 0; i < 4; ++i) v[i] = (g & 1) ? acc[0][n][4 * q + i] : acc[1][n][4 * q + i];
          *(LDSP f32x4*)(mine + (n * 4 + q) * 1024) = v;
        }
#pragma unroll
        for (int i = 0; i < 16; ++i) kp_[n][i] = (g & 1) ? acc[1][n][i] : acc[0][n][i];
      }
      WAIT_LGKM0();
      flag_set(smem, FL_GW, g, lane);
      flag_wait_one(smem, FL_GW, g ^ 1);
      LDSP char* theirs = smem + (g >> 1) * KV_HALF + ((g & 1) ^ 1) * 24576 + lo16;
#pragma unroll
      for (int n = 0; n < 5; ++n)
#pragma unroll
        for (int q = 0; q < 4; ++q) {
          f32x4 v = *(LDSP f32x4*)(theirs + (n * 4 + q) * 1024);
#pragma unroll
          for (int i = 0; i < 4; ++i) kp_[n][4 * q + i] += v[i];
        }
      WAIT_LGKM0();
#pragma unroll
      for (int n = 0; n < 5; ++n)
#pragma unroll
        for (int q = 0; q < 2; ++q) {
          f32x4 v;
#pragma unroll
          for (int i = 0; i < 4; ++i) v[i] = (g >> 1) ? kp_[n][4 * q + i] : kp_[n][8 + 4 * q + i];
          *(LDSP f32x4*)(theirs + (n * 2 + q) * 1024) = v;
        }
      WAIT_LGKM0();
      flag_set(smem, FL_GW2, g, lane);
      flag_wait_one(smem, FL_GW2, g ^ 2);
      LDSP char* other = smem + ((g >> 1) ^ 1) * KV_HALF + ((g & 1) ^ 1) * 24576 + lo16;
      const int hf = g >> 1, a = g & 1;
#pragma unroll
      for (int n = 0; n < 5; ++n) {
        const int nt = (n < 4) ? nt0 + n : nts;
        const int slot = (n < 4) ? kh : kh + 2 * (ng & 1);
        char* pp = ws + OFF_PART + (((size_t)slot * 64 + (2 * tp + a)) * 18 + nt) * 4096 + hf * 2048 + lo16;
#pragma unroll
        for (int q = 0; q < 2; ++q) {
          f32x4 v = *(LDSP f32x4*)(other + (n * 2 + q) * 1024);
#pragma unroll
          for (int i = 0; i < 4; ++i) v[i] += hf ? kp_[n][8 + 4 * q + i] : kp_[n][4 * q + i];
          *(f32x4*)(pp + q * 1024) = v;
        }
      }
    }
  }
  STAMP(4);
  STAMP(5);
  TS_FLUSH(1);
}

__global__ __launch_bounds__(256)
void k3(const u16* __restrict__ x, const u16* __restrict__ b2, const char* __restrict__ ws, u16* __restrict__ out) {
  __shared__ __attribute__((aligned(16))) float red[4][4][4][64][4];
  const int tid = threadIdx.x, lane = tid & 63;
  const int w = __builtin_amdgcn_readfirstlane(tid >> 6);
  const int b = blockIdx.x;
  const int tp = (b & 7) * 4 + (b >> 3) / 9, np = (b >> 3) % 9;
  const int t32 = lane & 31, hh = lane >> 5;
  const char* ap = ws + OFF_AO + ((size_t)(2 * tp) * 36 + w * 9) * 1024 + lane * 16;
  const char* wp = ws + OFF_WOT + ((size_t)(w * 9) * 18 + 2 * np) * 1024 + lane * 16;
  s16x8 af[2][9], wf[2][9];
#pragma unroll
  for (int k = 0; k < 9; ++k) {
    af[0][k] = *(const s16x8*)(ap + k * 1024);
    af[1][k] = *(const s16x8*)(ap + 36 * 1024 + k * 1024);
    wf[0][k] = *(const s16x8*)(wp + k * 18432);
    wf[1][k] = *(const s16x8*)(wp + k * 18432 + 1024);
  }
  const int a = w >> 1, n = w & 1;
  const int tt = 2 * tp + a, nt = 2 * np + n;
  f32x4 fin[4];
  {
    const char* pp = ws + OFF_PART + ((size_t)tt * 18 + nt) * 4096 + lane * 16;
    const size_t SL = (size_t)64 * 18 * 4096;
#pragma unroll
    for (int q = 0; q < 4; ++q) fin[q] = *(const f32x4*)(pp + q * 1024) + *(const f32x4*)(pp + SL + q * 1024);
    if (nt == 4 || nt == 13) {
#pragma unroll
      for (int q = 0; q < 4; ++q) fin[q] += *(const f32x4*)(pp + 2 * SL + q * 1024) + *(const f32x4*)(pp + 3 * SL + q * 1024);
    }
    const int token = tt * 32 + t32;
#pragma unroll
    for (int q = 0; q < 4; ++q) {
      const int col = nt * 32 + 8 * q + 4 * hh;
      u32x2 xv = *(const u32x2*)((const char*)x + (size_t)token * 1152 + col * 2);
      u32x2 bv = *(const u32x2*)((const char*)b2 + col * 2);
      fin[q][0] += __builtin_bit_cast(float, xv[0] << 16) + __builtin_bit_cast(float, bv[0] << 16);
      fin[q][1] += __builtin_bit_cast(float, xv[0] & 0xffff0000u) + __builtin_bit_cast(float, bv[0] & 0xffff0000u);
      fin[q][2] += __builtin_bit_cast(float, xv[1] << 16) + __builtin_bit_cast(float, bv[1] << 16);
      fin[q][3] += __builtin_bit_cast(float, xv[1] & 0xffff0000u) + __builtin_bit_cast(float, bv[1] & 0xffff0000u);
    }
  }
  f32x16 acc[2][2];
#pragma unroll
  for (int i = 0; i < 2; ++i)
#pragma unroll
    for (int jn = 0; jn < 2; ++jn)
#pragma unroll
      for (int r = 0; r < 16; ++r) acc[i][jn][r] = 0.f;
#pragma unroll
  for (int k = 0; k < 9; ++k)
#pragma unroll
    for (int i = 0; i < 2; ++i)
#pragma unroll
      for (int jn = 0; jn < 2; ++jn) acc[i][jn] = MFMA32(wf[jn][k], af[i][k], acc[i][jn]);
#pragma unroll
  for (int i = 0; i < 2; ++i)
#pragma unroll
    for (int jn = 0; jn < 2; ++jn)
#pragma unroll
      for (int q = 0; q < 4; ++q) {
        f32x4 v = {acc[i][jn][4 * q], acc[i][jn][4 * q + 1], acc[i][jn][4 * q + 2], acc[i][jn][4 * q + 3]};
        *(f32x4*)&red[w][i * 2 + jn][q][lane][0] = v;
      }
  __syncthreads();
  const int token = tt * 32 + t32;
#pragma unroll
  for (int q = 0; q < 4; ++q) {
    f32x4 v = fin[q];
#pragma unroll
    for (int o = 0; o < 4; ++o) v += *(const f32x4*)&red[o][w][q][lane][0];
    u32x2 ov = {pack2(v[0], v[1]), pack2(v[2], v[3])};
    *(u32x2*)((char*)out + (size_t)token * 1152 + (nt * 32 + 8 * q + 4 * hh) * 2) = ov;
  }
}

extern "C" void kernel_launch(void* const* d_in, const int* in_sizes, int n_in,
                              void* d_out, int out_size, void* d_ws, size_t ws_size,
                              hipStream_t stream) {
  const u16* x  = (const u16*)d_in[0];
  const u16* wq = (const u16*)d_in[1];
  const u16* wk = (const u16*)d_in[2];
  const u16* wv = (const u16*)d_in[3];
  const u16* wo = (const u16*)d_in[4];
  const u16* w1 = (const u16*)d_in[6];
  const u16* b1 = (const u16*)d_in[7];
  const u16* w2 = (const u16*)d_in[8];
  const u16* b2 = (const u16*)d_in[9];
  char* ws = (char*)d_ws;
  (void)hipFuncSetAttribute((const void*)k1, hipFuncAttributeMaxDynamicSharedMemorySize, K1_LDS);
  (void)hipFuncSetAttribute((const void*)k2, hipFuncAttributeMaxDynamicSharedMemorySize, K2_LDS);
  k1<<<256, 512, K1_LDS, stream>>>(x, wq, wk, wv, wo, w1, b1, w2, ws);
  k2<<<256, 512, K2_LDS, stream>>>(ws);
  k3<<<288, 256, 0, stream>>>(x, b2, ws, (u16*)d_out);
}
```

```cpp
#include <hip/hip_runtime.h>
#include <hip/hip_bf16.h>
#include <stdint.h>
#include <type_traits>

#define DI __device__ __forceinline__
#define LDSP __attribute__((address_space(3)))
#define GLP __attribute__((address_space(1)))

typedef unsigned short u16;
typedef __attribute__((ext_vector_type(8))) short s16x8;
typedef __attribute__((ext_vector_type(4))) short s16x4;
typedef __attribute__((ext_vector_type(16))) float f32x16;
typedef __attribute__((ext_vector_type(4))) float f32x4;
typedef __attribute__((ext_vector_type(2))) float f32x2;
typedef __attribute__((ext_vector_type(2))) __bf16 bf16x2;
typedef __attribute__((ext_vector_type(4))) unsigned u32x4;
typedef __attribute__((ext_vector_type(2))) unsigned u32x2;

constexpr size_t OFF_H    = 0;
constexpr size_t OFF_W2T  = OFF_H + 18874368;
constexpr size_t OFF_WOT  = OFF_W2T + 5308416;
constexpr size_t OFF_Q    = OFF_WOT + 663552;
constexpr size_t OFF_KV   = OFF_Q + 2359296;
constexpr size_t OFF_AO   = OFF_KV + 159744;
constexpr size_t OFF_PART = OFF_AO + 2359296;
constexpr size_t OFF_RED  = OFF_PART + 18874368;
constexpr int KV_GRP = 9984, KV_TILE = 2240, KV_BYTES = 159744, KV_HALF = 79872;

#define MFMA32(a, b, c) __builtin_amdgcn_mfma_f32_32x32x16_bf16((a), (b), (c), 0, 0, 0)
#define WAIT_VM(n) asm volatile("s_waitcnt vmcnt(" #n ")" ::: "memory")
#define WAIT_LGKM0() asm volatile("s_waitcnt lgkmcnt(0)" ::: "memory")
#define BAR() __builtin_amdgcn_s_barrier()
#define TS_DECL
#define STAMP(i)
#define TS_FLUSH(kid)

DI unsigned pack2(float a, float b) {
  f32x2 v = {a, b};
  return __builtin_bit_cast(unsigned, __builtin_convertvector(v, bf16x2));
}
DI float bf2f(u16 v) { return __builtin_bit_cast(float, (unsigned)v << 16); }
DI float dot2(unsigned a, unsigned b, float c) {
  return __builtin_amdgcn_fdot2_f32_bf16(__builtin_bit_cast(bf16x2, a), __builtin_bit_cast(bf16x2, b), c, false);
}
DI void glds16(const void* gbase, unsigned voff, LDSP char* l) {
  unsigned keep;
  asm volatile("s_mov_b32 %0, m0\n\ts_mov_b32 m0, %3\n\ts_nop 0\n\tglobal_load_lds_dwordx4 %1, %2\n\ts_mov_b32 m0, %0"
               : "=&s"(keep) : "v"(voff), "s"(gbase), "s"((unsigned)(size_t)l) : "memory");
}
DI void glds4(const void* gbase, unsigned voff, LDSP char* l) {
  unsigned keep;
  asm volatile("s_mov_b32 %0, m0\n\ts_mov_b32 m0, %3\n\ts_nop 0\n\tglobal_load_lds_dword %1, %2\n\ts_mov_b32 m0, %0"
               : "=&s"(keep) : "v"(voff), "s"(gbase), "s"((unsigned)(size_t)l) : "memory");
}
#define ST_FLAVOR 1
DI void store16(void* p, u32x4 v) {
  asm volatile("global_store_dwordx4 %0, %1, off sc1\n\ts_nop 1" :: "v"(p), "v"(v) : "memory");
}
DI s16x8 tr_frag(LDSP char* p, int second_off) {
  s16x4 lo = __builtin_amdgcn_ds_read_tr16_b64_v4i16((LDSP s16x4*)p);
  s16x4 hi = __builtin_amdgcn_ds_read_tr16_b64_v4i16((LDSP s16x4*)(p + second_off));
  return __builtin_shufflevector(lo, hi, 0, 1, 2, 3, 4, 5, 6, 7);
}
DI float other_half(float v) {
  unsigned u = __builtin_bit_cast(unsigned, v);
  auto r = __builtin_amdgcn_permlane32_swap(u, u, false, false);
  unsigned o = (threadIdx.x & 32) ? r[0] : r[1];
  return __builtin_bit_cast(float, o);
}
DI float gelu_tanh(float z) {
  const float c0 = -2.302208198f;
  const float c1 = -0.1029432397f;
  float t = z * z;
  float p = __builtin_fmaf(t, c1, c0);
  float e = __builtin_amdgcn_exp2f(z * p);
  return z * __builtin_amdgcn_rcpf(1.0f + e);
}

constexpr int K1_SLOT = 36864;
constexpr int K1_BIAS = 4 * K1_SLOT;
constexpr int K1_CS   = K1_BIAS + 1152;
constexpr int K1_CSQ  = K1_CS + 1152;
constexpr int K1_STAT = K1_CSQ + 1024;
constexpr int K1_PF   = K1_STAT + 1024;
constexpr int K1_LDS  = K1_PF + 256;

__host__ __device__ constexpr int w_slot(int st) { return (0x231023120ll >> (4 * st)) & 3; }

#define SCHED() __builtin_amdgcn_sched_barrier(0)
#define SGB(mask, n) __builtin_amdgcn_sched_group_barrier((mask), (n), 0)

DI u32x2 gelu4(float a0, float a1, float a2, float a3, const f32x4& cs, const f32x4& bb, float negm, float r, float c0) {
  float z0, z1, z2, z3, t0, t1, t2, t3;
  unsigned o0, o1;
  asm volatile(
      "v_fma_f32 %2, %18, %14, %10\n\tv_fma_f32 %3, %18, %15, %11\n\tv_fma_f32 %4, %18, %16, %12\n\tv_fma_f32 %5, %18, %17, %13\n\t"
      "v_fma_f32 %2, %19, %2, %21\n\tv_fma_f32 %3, %19, %3, %22\n\tv_fma_f32 %4, %19, %4, %23\n\tv_fma_f32 %5, %19, %5, %24\n\t"
      "v_mul_f32 %6, %2, %2\n\tv_mul_f32 %7, %3, %3\n\tv_mul_f32 %8, %4, %4\n\tv_mul_f32 %9, %5, %5\n\t"
      "v_fmamk_f32 %6, %6, 0xbdd2d3e8, %20\n\tv_fmamk_f32 %7, %7, 0xbdd2d3e8, %20\n\tv_fmamk_f32 %8, %8, 0xbdd2d3e8, %20\n\tv_fmamk_f32 %9, %9, 0xbdd2d3e8, %20\n\t"
      "v_mul_f32 %6, %2, %6\n\tv_mul_f32 %7, %3, %7\n\tv_mul_f32 %8, %4, %8\n\tv_mul_f32 %9, %5, %9\n\t"
      "v_exp_f32 %6, %6\n\tv_exp_f32 %7, %7\n\tv_exp_f32 %8, %8\n\tv_exp_f32 %9, %9\n\t"
      "v_add_f32 %6, 1.0, %6\n\tv_add_f32 %7, 1.0, %7\n\tv_add_f32 %8, 1.0, %8\n\tv_add_f32 %9, 1.0, %9\n\t"
      "v_rcp_f32 %6, %6\n\tv_rcp_f32 %7, %7\n\tv_rcp_f32 %8, %8\n\tv_rcp_f32 %9, %9\n\t"
      "v_mul_f32 %2, %2, %6\n\tv_mul_f32 %3, %3, %7\n\tv_mul_f32 %4, %4, %8\n\tv_mul_f32 %5, %5, %9\n\t"
      "v_cvt_pk_bf16_f32 %0, %2, %3\n\tv_cvt_pk_bf16_f32 %1, %4, %5"
      : "=&v"(o0), "=&v"(o1), "=&v"(z0), "=&v"(z1), "=&v"(z2), "=&v"(z3), "=&v"(t0), "=&v"(t1), "=&v"(t2), "=&v"(t3)
      : "v"(a0), "v"(a1), "v"(a2), "v"(a3), "v"(cs[0]), "v"(cs[1]), "v"(cs[2]), "v"(cs[3]), "v"(negm), "v"(r), "v"(c0),
        "v"(bb[0]), "v"(bb[1]), "v"(bb[2]), "v"(bb[3]));
  u32x2 o = {o0, o1};
  return o;
}

template <typename F>
DI void gelu_tile(const f32x16& a, LDSP char* cs_p, LDSP char* bias_p, char* out_p, float negm, float r, F&& between) {
  const float c0 = -2.302208198f;
#pragma unroll
  for (int hf = 0; hf < 2; ++hf) {
    f32x4 cs0 = *(LDSP f32x4*)(cs_p + hf * 64), cs1 = *(LDSP f32x4*)(cs_p + hf * 64 + 32);
    f32x4 bb0 = *(LDSP f32x4*)(bias_p + hf * 64), bb1 = *(LDSP f32x4*)(bias_p + hf * 64 + 32);
    between(2 * hf);
    u32x2 lo = gelu4(a[8 * hf], a[8 * hf + 1], a[8 * hf + 2], a[8 * hf + 3], cs0, bb0, negm, r, c0);
    between(2 * hf + 1);
    u32x2 hi = gelu4(a[8 * hf + 4], a[8 * hf + 5], a[8 * hf + 6], a[8 * hf + 7], cs1, bb1, negm, r, c0);
    u32x4 o = {lo[0], lo[1], hi[0], hi[1]};
    store16(out_p + hf * 1024, o);
  }
}

DI void colsum_frag(const s16x8& a, const s16x8& sel, f32x4& cs) {
  cs = __builtin_amdgcn_mfma_f32_16x16x32_bf16(sel, a, cs, 0, 0, 0);
}

template <int KT>
DI void w1_stage(LDSP char* sb0, LDSP char* sb1, LDSP char* sb2, const s16x8 (&xf)[36], f32x16 (&acc)[3], const s16x8& sel, f32x4& cs) {
  asm volatile("" : "+v"(sb0), "+v"(sb1), "+v"(sb2));
  s16x8 a[2][3];
  a[0][0] = tr_frag(sb0, 768); a[0][1] = tr_frag(sb1, 768); a[0][2] = tr_frag(sb2, 768);
#pragma unroll
  for (int ks = 0; ks < 12; ++ks) {
    const int cur = ks & 1;
    if (ks + 1 < 12) {
      a[cur ^ 1][0] = tr_frag(sb0 + (ks + 1) * 3072, 768);
      a[cur ^ 1][1] = tr_frag(sb1 + (ks + 1) * 3072, 768);
      a[cur ^ 1][2] = tr_frag(sb2 + (ks + 1) * 3072, 768);
    }
#pragma unroll
    for (int t = 0; t < 3; ++t) acc[t] = MFMA32(a[cur][t], xf[12 * KT + ks], acc[t]);
    colsum_frag(a[cur][0], sel, cs);
    SGB(0x100, 2); SGB(0x8, 1); SGB(0x100, 2); SGB(0x8, 1); SGB(0x100, 2); SGB(0x8, 2);
    SCHED();
  }
}

DI void qkv_stage(LDSP char* sb, const s16x8 (&xf)[36], f32x16& qa, const s16x8& sel, f32x4& cs) {
  asm volatile("" : "+v"(sb));
  s16x8 a[3];
  a[0] = tr_frag(sb, 256);
  a[1] = tr_frag(sb + 1024, 256);
#pragma unroll
  for (int s = 0; s < 36; ++s) {
    if (s + 2 < 36) a[(s + 2) % 3] = tr_frag(sb + (s + 2) * 1024, 256);
    qa = MFMA32(a[s % 3], xf[s], qa);
    colsum_frag(a[s % 3], sel, cs);
    SCHED();
  }
}

__global__ __launch_bounds__(512, 2)
void k1(const u16* __restrict__ x, const u16* __restrict__ wq, const u16* __restrict__ wk, const u16* __restrict__ wv,
        const u16* __restrict__ wo, const u16* __restrict__ w1, const u16* __restrict__ b1, const u16* __restrict__ w2,
        char* __restrict__ ws) {
  extern __shared__ __attribute__((aligned(16))) char smem_[];
  LDSP char* smem = (LDSP char*)smem_;
  const int tid = threadIdx.x, lane = tid & 63;
  const int w8 = __builtin_amdgcn_readfirstlane(tid >> 6);
  const int w = w8 & 3, grp = w8 >> 2;
  const int b = blockIdx.x, xcd = b & 7, idx = b >> 3;
#define K1MAP 0
  const int tg = (xcd >> 1) * 4 + (idx & 3), j = (xcd & 1) * 8 + (idx >> 2);
  const int t32 = lane & 31, hh = lane >> 5;
  const int tt = tg * 4 + w;
  const bool heavy = (j & 3) == 0;
  const int rk = (j + tg) % 3, rc = ((tg & 3) + (j >> 1)) % 3;
  TS_DECL; STAMP(0);

  const char* w1w = (const char*)w1 + (size_t)(48 * w) * 9216 + (size_t)j * 576;
  unsigned off3[3];
#pragma unroll
  for (int i = 0; i < 3; ++i) {
    const unsigned bb = 1024 * i + 16 * lane;
    off3[i] = (bb / 192) * 9216 + (bb % 192);
  }
  auto issue_w1_rows = [&](int st, int r) {
    const int c = st / 3, kt = st - 3 * c;
    const char* g = w1w + ((c + rc) % 3) * 192 + (size_t)((kt + rk) % 3) * 192 * 9216 + r * 16 * 9216;
    LDSP char* l = smem + w_slot(st) * K1_SLOT + w * 9216 + r * 3072;
#pragma unroll
    for (int i = 0; i < 3; ++i) glds16(g, off3[i], l + i * 1024);
  };
  auto issue_w1 = [&](int st) {
#pragma unroll
    for (int r = 0; r < 3; ++r) issue_w1_rows(st, r);
  };
  auto issue_x = [&](int q, int slot) {
    int ln = tid & 63; asm volatile("" : "+v"(ln));
    const char* g = (const char*)x + (size_t)(tt * 32) * 1152;
    LDSP char* l = smem + slot * K1_SLOT + w * 9216;
    unsigned row = (unsigned)ln / 18, pos = (unsigned)ln % 18;
    const unsigned b0 = q * 288 + rk * 384;
#pragma unroll
    for (int i = 0; i < 9; ++i) {
      unsigned cb = b0 + ((pos ^ ((row >> 3) & 1)) << 4);
      cb = cb >= 1152 ? cb - 1152 : cb;
      glds16(g, row * 1152 + cb, l + i * 1024);
      pos += 10; row += 3;
      if (pos >= 18) { pos -= 18; row += 1; }
    }
  };
  auto issue_qkv = [&](int tile, int slot) {
    int ln = tid & 63; asm volatile("" : "+v"(ln));
    LDSP char* l = smem + slot * K1_SLOT + w * 9216;
    if (tile < 18) {
      const char* g = (const char*)wq + tile * 64;
      const unsigned vo = (ln >> 2) * 1152 + (ln & 3) * 16;
#pragma unroll
      for (int i = 0; i < 9; ++i) glds16(g + (size_t)((144 * w + 16 * i + 192 * rk) % 576) * 1152, vo, l + i * 1024);
    } else {
      const u16* src = (tile == 18) ? wk : wv;
      int dw = ln & 15; dw = dw > 8 ? 8 : dw;
      const unsigned vo = (ln >> 4) * 36 + dw * 4;
#pragma unroll
      for (int i = 0; i < 36; ++i) glds4((const char*)src + ((144 * w + 4 * i + 192 * rk) % 576) * 36, vo, l + i * 256);
    }
  };
  const int lw = (tg * 12 + (j - (j >> 2) - 1)) * 4 + w;
  auto issue_unit = [&]() {
    if (lw < 648) {
      int ln = tid & 63; asm volatile("" : "+v"(ln));
      LDSP char* l = smem + 0 * K1_SLOT + w * 9216;
      const int u = lw - 576, ks = u >> 1;
      const bool plain = ks < 32;
      const char* g = lw < 576 ? (const char*)w2 + (size_t)((lw >> 1) * 16) * 1152 + (lw & 1) * 576
                               : (const char*)wo + (size_t)(plain ? ks * 18 : 144 * (ks - 32) + 16) * 1152 + (u & 1) * 576;
      unsigned row = (unsigned)ln / 36, pos = (unsigned)ln % 36;
#pragma unroll
      for (int i = 0; i < 9; ++i) {
        const unsigned srow = plain ? row : 18 * (row >> 1) + (row & 1);
        glds16(g, srow * 1152 + pos * 16, l + i * 1024);
        pos += 28; row += 1;
        if (pos >= 36) { pos -= 36; row += 1; }
      }
    }
  };

  unsigned bias_raw = 0, bias_raw2 = 0;
  if (grp == 1) {
    asm volatile("global_load_ushort %0, %2, %4\n\tglobal_load_ushort %1, %3, %4" : "=&v"(bias_raw), "=&v"(bias_raw2)
                 : "v"((unsigned)((tid - 256) * 2)), "v"((unsigned)((tid < 288 ? tid : 287) * 2)), "s"(b1 + j * 288) : "memory");
  }
  if (grp == 0) { issue_x(0, 3); issue_x(1, 1); }
  else          { issue_w1(0);   issue_w1(1); }
  s16x8 xf[36];
  const int xl = w * 9216 + t32 * 288 + ((hh ^ ((t32 >> 3) & 1)) << 4);
  auto read_x = [&](int q, int slot) {
#pragma unroll
    for (int s = 0; s < 9; ++s) xf[9 * q + s] = *(LDSP s16x8*)(smem + slot * K1_SLOT + xl + s * 32);
  };
  if (grp == 0) WAIT_VM(9);
  BAR();
  read_x(0, 3);
  if (grp == 0) { WAIT_VM(0); }
  else {
    asm volatile("s_waitcnt vmcnt(9)" : "+v"(bias_raw), "+v"(bias_raw2) :: "memory");
    *(LDSP float*)(smem + K1_BIAS + (tid - 256) * 4) = __builtin_bit_cast(float, bias_raw << 16);
    if (tid < 288) *(LDSP float*)(smem + K1_BIAS + tid * 4) = __builtin_bit_cast(float, bias_raw2 << 16);
  }
  WAIT_LGKM0(); BAR();
  STAMP(1);
  read_x(1, 1);
  if (grp == 1) issue_x(2, 3);
  WAIT_LGKM0(); BAR();
  STAMP(2);

  const int q4 = (lane & 15) >> 2, p4 = lane & 3, g16 = (lane >> 4) & 1;
  const int rot = (w == 3) ? 0 : w;
  const int tl0 = rot, tl1 = (rot + 1) % 3, tl2 = (rot + 2) % 3;
  const int lo_w1 = (8 * hh + q4) * 192 + 32 * g16 + 8 * p4;
  const int lo_q  = (8 * hh + q4) * 64 + 32 * g16 + 8 * p4;
  char* hout = ws + OFF_H + ((size_t)tt * 288 + (size_t)j * 18) * 1024 + lane * 16;
  f32x16 acc[3];
  float negm = 0.f, r1 = 0.f, rm = 0.f;
  const unsigned selv = ((lane & 15) == ((lane >> 4) & 1)) ? 0x3f803f80u : 0u;
  const u32x4 selu = {selv, selv, selv, selv};
  const s16x8 sel = __builtin_bit_cast(s16x8, selu);

  auto load_stats = [&]() {
    f32x2 st = *(LDSP f32x2*)(smem + K1_STAT + (w * 32 + t32) * 8);
    negm = -st[0] * (1.0f / 576.0f);
    const float var = __builtin_fmaxf(st[1] * (1.0f / 576.0f) - negm * negm, 0.f);
    r1 = __builtin_amdgcn_rsqf(var + 1e-5f);
    rm = r1 * __builtin_amdgcn_rsqf(var * r1 * r1 + 1e-5f);
  };
#define HELP_PRIO 1
  auto mfma_chunk = [&](auto cc) {
    constexpr int c = decltype(cc)::value;
    __builtin_amdgcn_s_setprio(0);
    f32x4 cs = {0.f, 0.f, 0.f, 0.f};
#pragma unroll
    for (int t = 0; t < 3; ++t)
#pragma unroll
      for (int i = 0; i < 16; ++i) acc[t][i] = 0.f;
    LDSP char* s0 = smem + lo_w1 + tl0 * 64;
    LDSP char* s1 = smem + lo_w1 + tl1 * 64;
    LDSP char* s2 = smem + lo_w1 + tl2 * 64;
    constexpr int o0 = w_slot(3 * c) * K1_SLOT, o1 = w_slot(3 * c + 1) * K1_SLOT, o2 = w_slot(3 * c + 2) * K1_SLOT;
    w1_stage<0>(s0 + o0, s1 + o0, s2 + o0, xf, acc, sel, cs);
    if (c == 1) { WAIT_VM(9); } else if (c == 2) { WAIT_VM(11); }
    WAIT_LGKM0(); BAR();
    if (c == 0) { read_x(2, 3); WAIT_LGKM0(); BAR(); }
    w1_stage<1>(s0 + o1, s1 + o1, s2 + o1, xf, acc, sel, cs);
    if (c != 0) WAIT_VM(0);
    WAIT_LGKM0(); BAR();
    if (c == 0) { read_x(3, 0); WAIT_LGKM0(); BAR(); }
    w1_stage<2>(s0 + o2, s1 + o2, s2 + o2, xf, acc, sel, cs);
    if (lane < 16 && w < 3) {
      *(LDSP float*)(smem + K1_CS + (c * 96 + tl0 * 32 + lane) * 4) = cs[0];
      *(LDSP float*)(smem + K1_CS + (c * 96 + tl0 * 32 + 16 + lane) * 4) = cs[1];
    }
    WAIT_LGKM0(); BAR();
    __builtin_amdgcn_s_setprio(HELP_PRIO);
  };
  auto gelu_t = [&](int c, int t, int tl, int st_issue) {
    const int ca = (c + rc) % 3;
    gelu_tile(acc[t], smem + K1_CS + (c * 96 + tl * 32) * 4 + hh * 16, smem + K1_BIAS + (ca * 96 + tl * 32) * 4 + hh * 16,
              hout + (ca * 6 + tl * 2) * 1024, negm, rm, [&](int i) { if (st_issue >= 0 && i < 3) issue_w1_rows(st_issue, i); });
  };

  if (grp == 0) {
    mfma_chunk(std::integral_constant<int, 0>{});
    STAMP(3);
    load_stats();
    gelu_t(0, 0, tl0, 6); WAIT_LGKM0(); BAR();
    gelu_t(0, 1, tl1, 7); WAIT_LGKM0(); BAR();
    gelu_t(0, 2, tl2, 8); WAIT_VM(22); WAIT_LGKM0(); BAR();
    STAMP(4);
    mfma_chunk(std::integral_constant<int, 2>{});
    STAMP(5);
    gelu_t(2, 0, tl0, -1); gelu_t(2, 1, tl1, -1); gelu_t(2, 2, tl2, -1);
    STAMP(6);
  } else {
    f32x4 rs = {0.f, 0.f, 0.f, 0.f};
#pragma unroll
    for (int i = 0; i < 16; ++i) acc[0][i] = 0.f;
    auto stats_frag = [&](int s) {
      colsum_frag(xf[s], sel, rs);
      acc[0] = MFMA32(xf[s], xf[s], acc[0]);
    };
    __builtin_amdgcn_s_setprio(HELP_PRIO);
#pragma unroll
    for (int s = 0; s < 18; ++s) { if (s % 6 == 0) issue_w1_rows(2, s / 6); stats_frag(s); }
    WAIT_VM(9); BAR();
    issue_x(3, 0);
    read_x(2, 3); WAIT_LGKM0(); BAR();
#pragma unroll
    for (int s = 18; s < 27; ++s) { if (s % 3 == 0) issue_w1_rows(3, (s - 18) / 3); stats_frag(s); }
    WAIT_VM(9); BAR();
    issue_w1(4);
    read_x(3, 0); WAIT_LGKM0(); BAR();
#pragma unroll
    for (int s = 27; s < 36; ++s) { if (s % 3 == 0) issue_w1_rows(5, (s - 27) / 3); stats_frag(s); }
    {
      const int dreg = (t32 & 3) + 4 * (t32 >> 3);
      float s2 = 0.f;
#pragma unroll
      for (int i = 0; i < 16; ++i) s2 = (i == dreg) ? acc[0][i] : s2;
      if (hh == ((t32 >> 2) & 1)) *(LDSP float*)(smem + K1_STAT + (w * 32 + t32) * 8 + 4) = s2;
      if (lane < 16) {
        *(LDSP float*)(smem + K1_STAT + (w * 32 + lane) * 8) = rs[0];
        *(LDSP float*)(smem + K1_STAT + (w * 32 + 16 + lane) * 8) = rs[1];
      }
    }
    WAIT_VM(18); WAIT_LGKM0(); BAR();
    STAMP(3);
    mfma_chunk(std::integral_constant<int, 1>{});
    STAMP(4);
    load_stats();
    if (heavy) issue_qkv(16 + (j >> 2), 0); else issue_unit();
    gelu_t(1, 0, tl0, -1); WAIT_LGKM0(); BAR();
    issue_qkv(j, 1);  gelu_t(1, 1, tl1, -1); WAIT_LGKM0(); BAR();
    gelu_t(1, 2, tl2, -1);
    WAIT_VM(2); WAIT_LGKM0(); BAR();
    __builtin_amdgcn_s_setprio(0);
    STAMP(5);

    auto qkv_tile = [&](int tile, int slot) {
      f32x16 qa;
#pragma unroll
      for (int i = 0; i < 16; ++i) qa[i] = 0.f;
      f32x4 cs = {0.f, 0.f, 0.f, 0.f};
      qkv_stage(smem + slot * K1_SLOT + lo_q, xf, qa, sel, cs);
      int ln = tid & 63; asm volatile("" : "+v"(ln));
      const int t32 = ln & 31, hh = ln >> 5, token = tt * 32 + t32;
      LDSP char* cp = smem + K1_CSQ + (w * 2 + (slot & 1)) * 128;
      if (ln < 16) { *(LDSP float*)(cp + ln * 4) = cs[0]; *(LDSP float*)(cp + 64 + ln * 4) = cs[1]; }
      WAIT_LGKM0();
#pragma unroll
      for (int g = 0; g < 4; ++g) {
        f32x4 c4 = *(LDSP f32x4*)(cp + g * 32 + hh * 16);
#pragma unroll
        for (int i = 0; i < 4; ++i) qa[4 * g + i] = __builtin_fmaf(negm, c4[i], qa[4 * g + i]);
      }
      if (tile < 18) {
        const float sc = r1 * (0.23570226039551584f * 1.4426950408889634f);
        char* qp = ws + OFF_Q + (size_t)tt * 36864;
#pragma unroll
        for (int i = 0; i < 8; ++i) {
          const unsigned n = tile * 32 + 8 * (i >> 1) + 4 * hh + 2 * (i & 1);
          const unsigned head = n / 18, d = n - head * 18;
          const unsigned off = d < 16 ? (t32 + 32 * ((d >> 2) & 1)) * 16 + ((d >> 3) * 2 + ((d >> 1) & 1)) * 4 : 1024 + t32 * 4;
          *(unsigned*)(qp + head * 1152 + off) = pack2(qa[2 * i] * sc, qa[2 * i + 1] * sc);
        }
      } else if (tile == 18) {
        u32x4 o = {pack2(qa[0] * r1, qa[1] * r1), pack2(qa[2] * r1, qa[3] * r1),
                   pack2(qa[4] * r1, qa[5] * r1), pack2(qa[6] * r1, qa[7] * r1)};
        char* gp = ws + OFF_KV + (size_t)tg * KV_GRP;
        *(u32x4*)(gp + 1024 + w * KV_TILE + ln * 16) = o;
        if (hh == 0) *(unsigned*)(gp + (w >> 1) * 512 + t32 * 16 + (w & 1) * 4) = pack2(qa[8] * r1, qa[9] * r1);
        else if ((w & 1) == 0) {
          u32x2 cst = {w == 0 ? 0x3f803f80u : 0u, 0u};
          *(u32x2*)(gp + (w >> 1) * 512 + t32 * 16 + 8) = cst;
        }
      } else {
        const int s = t32 >> 4, hk = (t32 >> 2) & 1, jj = ((t32 >> 3) & 1) * 4 + (t32 & 3);
        char* vp = ws + OFF_KV + (size_t)tg * KV_GRP + 2048 + w * KV_TILE + (s * 2 + hk) * 304 + jj * 2;
#pragma unroll
        for (int i = 0; i < 8; ++i) {
          const int d = (i & 3) + 8 * (i >> 2) + 4 * hh;
          *(u16*)(vp + d * 16) = (u16)(pack2(qa[i] * r1, 0.f) & 0xffffu);
        }
        if (hh == 0) {
          *(u16*)(vp + 16 * 16) = (u16)(pack2(qa[8] * r1, 0.f) & 0xffffu);
          *(u16*)(vp + 17 * 16) = (u16)(pack2(qa[9] * r1, 0.f) & 0xffffu);
          *(u16*)(vp + 18 * 16) = (u16)0x3f80;
        }
      }
    };
    qkv_tile(j, 1);
    STAMP(6);
    if (heavy) {
      qkv_tile(16 + (j >> 2), 0);
    } else if (lw < 648) {
      int ln = tid & 63; asm volatile("" : "+v"(ln));
      const int hh = ln >> 5, q4 = (ln & 15) >> 2, p4 = ln & 3, g16 = (ln >> 4) & 1;
      LDSP char* sb = smem + 0 * K1_SLOT + w * 9216 + (4 * hh + q4) * 576 + 32 * g16 + 8 * p4;
      char* dst = (lw < 576) ? ws + OFF_W2T + ((size_t)(lw >> 1) * 18 + (lw & 1) * 9) * 1024
                             : ws + OFF_WOT + ((size_t)((lw - 576) >> 1) * 18 + (lw & 1) * 9) * 1024;
#pragma unroll
      for (int n = 0; n < 9; ++n) {
        s16x8 f = tr_frag(sb + n * 64, 8 * 576);
        store16(dst + n * 1024 + ln * 16, __builtin_bit_cast(u32x4, f));
      }
    }
  }
  STAMP(7);
  STAMP(8);
  TS_FLUSH(0);
}

constexpr int K2_FLAG = KV_BYTES;
constexpr int K2_PF   = KV_BYTES + 128;
constexpr int K2_LDS  = KV_BYTES + 128 + 256;
enum { FL_KVA = 0, FL_KVG = 1, FL_PROG = 2, FL_GW = 3, FL_DONE = 4, FL_SENT = 5, FL_GW2 = 6 };

#define EXP2(x) __builtin_amdgcn_exp2f(x)

DI void flag_set(LDSP char* smem, int set, int i, int lane) {
  if (lane == 0) *(volatile LDSP unsigned*)(smem + K2_FLAG + set * 16 + i * 4) = 1u;
}
DI void flag_wait_all(LDSP char* smem, int set) {
  while (true) {
    u32x4 f = *(volatile LDSP u32x4*)(smem + K2_FLAG + set * 16);
    if (f[0] & f[1] & f[2] & f[3]) break;
    __builtin_amdgcn_s_sleep(2);
  }
  asm volatile("" ::: "memory");
}
DI void flag_wait_one(LDSP char* smem, int set, int i) {
  while (*(volatile LDSP unsigned*)(smem + K2_FLAG + set * 16 + i * 4) == 0u) __builtin_amdgcn_s_sleep(1);
  asm volatile("" ::: "memory");
}

DI void attn_step(f32x16& scur, f32x16& snext, const s16x8& k0, const s16x8& k1, const s16x8& qn0, const s16x8& qn1,
                  const s16x8& vprev1, f32x16& oprev, const s16x8& vcur0, f32x16& ocur, u32x4& p0, u32x4& p1) {
  f32x16 zero;
#pragma unroll
  for (int i = 0; i < 16; ++i) zero[i] = 0.f;
#define EX(i) scur[i] = EXP2(scur[i])
#define PRIO_LO() do { SCHED(); __builtin_amdgcn_s_setprio(0); SCHED(); } while (0)
#define PRIO_HI() do { SCHED(); __builtin_amdgcn_s_setprio(2); SCHED(); } while (0)
  p1[2] = pack2(snext[12], snext[13]);
  p1[3] = pack2(snext[14], snext[15]);
  SCHED();
  snext = MFMA32(k0, qn0, zero);
  SCHED();
  EX(0); EX(1); EX(2);
  SCHED();
  snext = MFMA32(k1, qn1, snext);
  SCHED();
  EX(3); EX(4);
  PRIO_LO();
  EX(5); EX(6);
  PRIO_HI();
  EX(7);
  SCHED();
  p0[0] = pack2(scur[0], scur[1]);
  p0[1] = pack2(scur[2], scur[3]);
  p0[2] = pack2(scur[4], scur[5]);
  p0[3] = pack2(scur[6], scur[7]);
  SCHED();
  oprev = MFMA32(vprev1, __builtin_bit_cast(s16x8, p1), oprev);
  SCHED();
  EX(8); EX(9); EX(10);
  SCHED();
  ocur = MFMA32(vcur0, __builtin_bit_cast(s16x8, p0), ocur);
  SCHED();
  EX(11); EX(12);
  PRIO_LO();
  EX(13); EX(14);
  PRIO_HI();
  EX(15);
  SCHED();
  p1[0] = pack2(scur[8], scur[9]);
  p1[1] = pack2(scur[10], scur[11]);
  SCHED();
#undef EX
}

DI s16x8 make_q1(unsigned qe, unsigned mneg, int i, int hh) {
  const unsigned a = (hh == (i >> 1)) ? qe : 0u;
  u32x4 r = {(i & 1) ? 0u : a, (i & 1) ? a : 0u, hh ? 0u : mneg, 0u};
  return __builtin_bit_cast(s16x8, r);
}
DI unsigned neg_hi_lo(float m) {
  unsigned hi = pack2(-m, 0.f) & 0xffffu;
  float rem = -m - __builtin_bit_cast(float, hi << 16);
  unsigned lo = pack2(rem, 0.f) & 0xffffu;
  return hi | (lo << 16);
}
DI int v_row(int t32) { return t32 > 18 ? ((t32 >= 20 && t32 < 28) ? 0 : 4) : t32; }

DI f32x16 attn_exact(const char* kv, int lane, u32x4 q0u, unsigned qe) {
  const int t32 = lane & 31, hh = lane >> 5;
  const s16x8 q0 = __builtin_bit_cast(s16x8, q0u);
  f32x16 zero, o;
#pragma unroll
  for (int i = 0; i < 16; ++i) { zero[i] = 0.f; o[i] = 0.f; }
  float m = -INFINITY;
#pragma unroll 1
  for (int T = 0; T < 64; ++T) {
    const char* gp = kv + (T >> 2) * KV_GRP;
    s16x8 k0 = *(const s16x8*)(gp + 1024 + (T & 3) * KV_TILE + lane * 16);
    s16x8 k1 = *(const s16x8*)(gp + lane * 16);
    f32x16 s = MFMA32(k0, q0, zero);
    s = MFMA32(k1, make_q1(qe, 0u, T & 3, hh), s);
#pragma unroll
    for (int i = 0; i < 16; ++i) m = __builtin_fmaxf(m, s[i]);
  }
  m = __builtin_fmaxf(m, other_half(m));
  const unsigned mneg = neg_hi_lo(m);
#pragma unroll 1
  for (int T = 0; T < 64; ++T) {
    const char* gp = kv + (T >> 2) * KV_GRP;
    const char* tp = gp + 1024 + (T & 3) * KV_TILE;
    s16x8 k0 = *(const s16x8*)(tp + lane * 16);
    s16x8 k1 = *(const s16x8*)(gp + lane * 16);
    s16x8 v0 = *(const s16x8*)(tp + 1024 + hh * 304 + v_row(t32) * 16);
    s16x8 v1 = *(const s16x8*)(tp + 1024 + 608 + hh * 304 + v_row(t32) * 16);
    f32x16 s = MFMA32(k0, q0, zero);
    s = MFMA32(k1, make_q1(qe, mneg, T & 3, hh), s);
    u32x4 p0, p1;
#pragma unroll
    for (int i = 0; i < 4; ++i) {
      p0[i] = pack2(EXP2(s[2 * i]), EXP2(s[2 * i + 1]));
      p1[i] = pack2(EXP2(s[8 + 2 * i]), EXP2(s[8 + 2 * i + 1]));
    }
    o = MFMA32(v0, __builtin_bit_cast(s16x8, p0), o);
    o = MFMA32(v1, __builtin_bit_cast(s16x8, p1), o);
  }
  return o;
}

template <int NT>
DI void gemm2_load(const char* hp, const char* wp, const char* wsp, unsigned lo16, int k, s16x8 (&hb)[2], s16x8 (&wb)[5]) {
  const GLP char* h0 = (const GLP char*)hp + (size_t)k * 1024;
  const GLP char* h1 = h0 + (size_t)288 * 1024;
  const GLP char* w0 = (const GLP char*)wp + (size_t)k * 18432;
  const GLP char* w4 = (const GLP char*)wsp + (size_t)k * 18432;
  asm volatile("" : "+s"(h0), "+s"(h1), "+s"(w0), "+s"(w4));
  hb[0] = *(const GLP s16x8*)(h0 + lo16);
  wb[0] = *(const GLP s16x8*)(w0 + lo16);
  hb[1] = *(const GLP s16x8*)(h1 + lo16);
#pragma unroll
  for (int n = 1; n < 4; ++n) wb[n] = *(const GLP s16x8*)(w0 + n * 1024 + lo16);
  if (NT == 5) wb[4] = *(const GLP s16x8*)(w4 + lo16);
}
#define GEM_PAUSE()
template <int NT, int NTN>
DI void gemm2_step(const char* hp, const char* wp, const char* wsp, unsigned lo16, int kn, s16x8 (&hb)[2], s16x8 (&wb)[5],
                   f32x16 (&acc)[2][5]) {
  const GLP char* h0 = (const GLP char*)hp + (size_t)kn * 1024 + lo16;
  const GLP char* w0 = (const GLP char*)wp + (size_t)kn * 18432 + lo16;
  const GLP char* w4 = (const GLP char*)wsp + (size_t)kn * 18432 + lo16;
#pragma unroll
  for (int n = 0; n < NT; ++n) {
    acc[0][n] = MFMA32(wb[n], hb[0], acc[0][n]);
    SCHED(); GEM_PAUSE(); SCHED();
    acc[1][n] = MFMA32(wb[n], hb[1], acc[1][n]);
    SCHED();
    if (n < 4) wb[n] = *(const GLP s16x8*)(w0 + n * 1024);
    else if (NTN == 5) wb[4] = *(const GLP s16x8*)(w4);
    GEM_PAUSE(); SCHED();
  }
  if (NT == 4 && NTN == 5) wb[4] = *(const GLP s16x8*)(w4);
  hb[0] = *(const GLP s16x8*)(h0);
  hb[1] = *(const GLP s16x8*)(h0 + (size_t)288 * 1024);
  SCHED();
}
template <int N1, int N2, typename F, typename G>
DI void gemm2_all(const char* hp, const char* wp, const char* wsp, unsigned lo16, int rot, f32x16 (&acc)[2][5], F&& after_first_loads, G&& after_first_step) {
  auto kk = [&](int i) { const int h = i >= 18 ? 18 : 0; int r = i - h + rot; r = r >= 18 ? r - 18 : r; return h + r; };
  s16x8 hA[2], wA[5], hB[2], wB[5];
  after_first_loads();
  gemm2_load<N1>(hp, wp, wsp, lo16, kk(0), hA, wA);
  gemm2_load<N1>(hp, wp, wsp, lo16, kk(1), hB, wB);
  SCHED();
  after_first_step();
#pragma unroll 1
  for (int k = 0; k < 16; k += 2) {
    gemm2_step<N1, N1>(hp, wp, wsp, lo16, kk(k + 2), hA, wA, acc);
    gemm2_step<N1, N1>(hp, wp, wsp, lo16, kk(k + 3), hB, wB, acc);
  }
  gemm2_step<N1, N2>(hp, wp, wsp, lo16, kk(18), hA, wA, acc);
  gemm2_step<N1, N2>(hp, wp, wsp, lo16, kk(19), hB, wB, acc);
#pragma unroll 1
  for (int k = 18; k < 36; k += 2) {
    gemm2_step<N2, N2>(hp, wp, wsp, lo16, kk((k + 2 < 36) ? k + 2 : 35), hA, wA, acc);
    gemm2_step<N2, N2>(hp, wp, wsp, lo16, kk((k + 3 < 36) ? k + 3 : 35), hB, wB, acc);
  }
}

__global__ __launch_bounds__(512, 2)
void k2(char* __restrict__ ws) {
  extern __shared__ __attribute__((aligned(16))) char smem_[];
  LDSP char* smem = (LDSP char*)smem_;
  const int tid = threadIdx.x, lane = tid & 63;
  const int w = __builtin_amdgcn_readfirstlane(tid >> 6);
  const int b = blockIdx.x;
  const int t32 = lane & 31, hh = lane >> 5;
  TS_DECL; STAMP(0);
  if (tid < 32) *(LDSP unsigned*)(smem + K2_FLAG + tid * 4) = 0u;
  const char* kvg = ws + OFF_KV;

  if (w < 4) {
#define ATT_PRIO 2
#define GEM_PRIO 1
    __builtin_amdgcn_s_setprio(ATT_PRIO);
    const int tt = b >> 2, hg = b & 3;
    const int rs = w & 1, kh2 = w >> 1;
    const int head0 = hg * 8 + rs * 4;
#pragma unroll
    for (int i = 0; i < 5; ++i) {
      glds16(kvg + (i * 4 + w) * 1024, lane * 16, smem + (i * 4 + w) * 1024);
      glds16(kvg + KV_HALF + (i * 4 + w) * 1024, lane * 16, smem + KV_HALF + (i * 4 + w) * 1024);
    }
    u32x4 q0u[4];
    unsigned qe[4];
#pragma unroll
    for (int qb = 0; qb < 4; ++qb) {
      const char* qp = ws + OFF_Q + (size_t)tt * 36864 + (head0 + qb) * 1152;
      q0u[qb] = *(const u32x4*)(qp + lane * 16);
      qe[qb] = *(const unsigned*)(qp + 1024 + t32 * 4);
    }
    const char* pf_base;
    unsigned pf_off, pf_step;
    {
      const int xcd = b & 7, a = (b >> 3) * 4 + w, kh = xcd & 1;
      if (a < 48) {
        const unsigned L = (a % 3) * 64 + lane;
        pf_base = ws + OFF_H + ((size_t)((xcd >> 1) * 16 + a / 3) * 288 + kh * 144) * 1024;
        pf_off = (L / 48) * 36 * 1024 + (L % 48) * 128;
        pf_step = 6 * 1024;
      } else {
        const unsigned L = ((a < 102 ? a : 101) - 48) * 64 + lane;
        pf_base = ws + OFF_W2T + (size_t)kh * 144 * 18432;
        pf_off = (L / 864) * 36 * 18432 + (L % 864) * 128;
        pf_step = 6 * 18432;
      }
    }
    WAIT_LGKM0(); BAR();
    WAIT_VM(0);
    flag_set(smem, FL_KVA, w, lane);
    flag_wait_all(smem, FL_KVA);
    STAMP(1);
    s16x8 q0[4], q1[4][4];
    f32x16 o[4];
    f32x16 sa, sb;
#pragma unroll
    for (int i = 0; i < 16; ++i) sb[i] = 0.f;
    {
      s16x8 k0 = *(LDSP s16x8*)(smem + 1024 + lane * 16);
      s16x8 k1 = *(LDSP s16x8*)(smem + lane * 16);
#pragma unroll
      for (int qb = 0; qb < 4; ++qb) {
        asm volatile("" : "+v"(q0u[qb]), "+v"(qe[qb]));
        q0[qb] = __builtin_bit_cast(s16x8, q0u[qb]);
        f32x16 s = MFMA32(k0, q0[qb], sb);
        s = MFMA32(k1, make_q1(qe[qb], 0u, 0, hh), s);
        float m = s[0];
#pragma unroll
        for (int i = 1; i < 16; ++i) m = __builtin_fmaxf(m, s[i]);
        m = __builtin_fmaxf(m, other_half(m));
        const unsigned mneg = neg_hi_lo(m);
#pragma unroll
        for (int i = 0; i < 4; ++i) q1[i][qb] = make_q1(qe[qb], mneg, i, hh);
#pragma unroll
        for (int i = 0; i < 16; ++i) o[qb][i] = 0.f;
      }
    }
    LDSP char* kp = smem + kh2 * KV_HALF + lane * 16;
    LDSP char* vp = smem + kh2 * KV_HALF + 2048 + hh * 304 + v_row(t32) * 16;
    u32x4 p0 = {0u, 0u, 0u, 0u}, p1 = {0u, 0u, 0u, 0u};
    s16x8 k1c = *(LDSP s16x8*)(kp), k1n;
    s16x8 k0b[2], v0b[2], v1b[2];
    k0b[0] = *(LDSP s16x8*)(kp + 1024);
    v0b[0] = *(LDSP s16x8*)(vp);
    v1b[1] = v0b[0];
    sa = MFMA32(k0b[0], q0[0], sb);
    sa = MFMA32(k1c, q1[0][0], sa);
#pragma unroll 1
    for (int g = 0; g < 8; ++g) {
      if (g == 1) flag_wait_all(smem, FL_KVG);
      if (g == 5) flag_set(smem, FL_PROG, w, lane);
      const int nxg = (g < 7) ? KV_GRP : 0;
#pragma unroll
      for (int i = 0; i < 4; ++i) {
        const int c = i & 1, n = c ^ 1;
        const int nt_off = (i < 3) ? (i + 1) * KV_TILE : nxg;
        v1b[c] = *(LDSP s16x8*)(vp + i * KV_TILE + 608);
        SCHED();
        attn_step(sa, sb, k0b[c], k1c, q0[1], q1[i][1], v1b[n], o[3], v0b[c], o[0], p0, p1);
        k0b[n] = *(LDSP s16x8*)(kp + 1024 + nt_off);
        if (i == 3) k1n = *(LDSP s16x8*)(kp + nxg);
        SCHED();
        attn_step(sb, sa, k0b[c], k1c, q0[2], q1[i][2], v1b[c], o[0], v0b[c], o[1], p0, p1);
        v0b[n] = *(LDSP s16x8*)(vp + nt_off);
        SCHED();
        attn_step(sa, sb, k0b[c], k1c, q0[3], q1[i][3], v1b[c], o[1], v0b[c], o[2], p0, p1);
        attn_step(sb, sa, k0b[n], (i == 3) ? k1n : k1c, q0[0], q1[(i + 1) & 3][0], v1b[c], o[2], v0b[c], o[3], p0, p1);
      }
      k1c = k1n;
      kp += KV_GRP; vp += KV_GRP;
    }
    p1[2] = pack2(sb[12], sb[13]);
    p1[3] = pack2(sb[14], sb[15]);
    o[3] = MFMA32(v1b[1], __builtin_bit_cast(s16x8, p1), o[3]);
    STAMP(2);
    f32x16 keep[2], send[2];
    u32x4 qk0[2];
    unsigned qke[2];
#pragma unroll
    for (int q = 0; q < 2; ++q) {
#pragma unroll
      for (int i = 0; i < 11; ++i) {
        keep[q][i] = kh2 ? o[2 + q][i] : o[q][i];
        send[q][i] = kh2 ? o[q][i] : o[2 + q][i];
      }
#pragma unroll
      for (int i = 11; i < 16; ++i) { keep[q][i] = 0.f; send[q][i] = 0.f; }
#pragma unroll
      for (int i = 0; i < 4; ++i) qk0[q][i] = kh2 ? q0u[2 + q][i] : q0u[q][i];
      { unsigned a_ = qe[q], b_ = qe[2 + q]; asm volatile("" : "+v"(a_), "+v"(b_)); qke[q] = kh2 ? b_ : a_; }
    }
    WAIT_LGKM0();
    flag_set(smem, FL_DONE, w, lane);
    flag_wait_one(smem, FL_DONE, w ^ 1);
    {
      LDSP char* xp = smem + kh2 * KV_HALF + 7 * KV_GRP + rs * 4864;
#pragma unroll
      for (int q = 0; q < 2; ++q) {
        const f32x16 ov = send[q];
        *(LDSP f32x4*)(xp + q * 2432 + lane * 16) = f32x4{ov[0], ov[1], ov[2], ov[3]};
        *(LDSP f32x4*)(xp + q * 2432 + 1024 + lane * 16) = f32x4{ov[4], ov[5], ov[6], ov[7]};
        if (hh == 0) {
          *(LDSP float*)(xp + q * 2432 + 2048 + t32 * 4) = ov[8];
          *(LDSP float*)(xp + q * 2432 + 2176 + t32 * 4) = ov[9];
          *(LDSP float*)(xp + q * 2432 + 2304 + t32 * 4) = ov[10];
        }
      }
    }
    WAIT_LGKM0();
    flag_set(smem, FL_SENT, w, lane);
    flag_wait_one(smem, FL_SENT, w ^ 2);
    bool bad = false;
#pragma unroll
    for (int q = 0; q < 2; ++q) {
      LDSP char* xp = smem + (kh2 ^ 1) * KV_HALF + 7 * KV_GRP + rs * 4864 + q * 2432;
      f32x4 a0 = *(LDSP f32x4*)(xp + lane * 16), a1 = *(LDSP f32x4*)(xp + 1024 + lane * 16);
#pragma unroll
      for (int i = 0; i < 4; ++i) { keep[q][i] += a0[i]; keep[q][4 + i] += a1[i]; }
      keep[q][8] += *(LDSP float*)(xp + 2048 + t32 * 4);
      keep[q][9] += *(LDSP float*)(xp + 2176 + t32 * 4);
      keep[q][10] += *(LDSP float*)(xp + 2304 + t32 * 4);
      float mag = 0.f;
#pragma unroll
      for (int i = 0; i < 8; ++i) mag += __builtin_fabsf(keep[q][i]);
      const float ext = __builtin_fabsf(keep[q][8]) + __builtin_fabsf(keep[q][9]) + 1e12f * __builtin_fabsf(keep[q][10]);
      mag += hh ? 0.f : ext;
      bad = bad || !(mag < 1e30f);
    }
    if (__builtin_expect(__any(bad), 0)) {
#pragma unroll 1
      for (int q = 0; q < 2; ++q) {
        f32x16 ex = attn_exact(kvg, lane, q ? qk0[1] : qk0[0], q ? qke[1] : qke[0]);
        if (q) keep[1] = ex; else keep[0] = ex;
      }
    }
#pragma unroll
    for (int q = 0; q < 2; ++q) {
      float l = keep[q][10];
      const float lo_ = other_half(l);
      const float li = __builtin_amdgcn_rcpf(hh ? lo_ : l);
      const int head = head0 + 2 * kh2 + q;
      u32x4 o4 = {pack2(keep[q][0] * li, keep[q][1] * li), pack2(keep[q][2] * li, keep[q][3] * li),
                  pack2(keep[q][4] * li, keep[q][5] * li), pack2(keep[q][6] * li, keep[q][7] * li)};
      char* ap = ws + OFF_AO + (size_t)tt * 36 * 1024;
      store16(ap + head * 1024 + lane * 16, o4);
      if (hh == 0) {
        const int h8 = head & 7;
        *(unsigned*)(ap + (32 + (head >> 3)) * 1024 + (t32 + 32 * ((h8 >> 1) & 1)) * 16 + ((h8 >> 2) * 2 + (h8 & 1)) * 4) =
            pack2(keep[q][8] * li, keep[q][9] * li);
      }
    }
    STAMP(3);
  } else {
    __builtin_amdgcn_s_setprio(GEM_PRIO);
    const int g = w - 4;
    const int xcd = b & 7, idx = b >> 3;
    const int kh = xcd & 1, tp = (xcd >> 1) * 8 + (idx & 7), ng = idx >> 3;
    const int nt0 = (ng >> 1) * 9 + (ng & 1) * 5;
    const int nts = (ng >> 1) * 9 + 4;
    const int fb0 = kh * 144 + g * 36;
    const char* hp0 = ws + OFF_H + ((size_t)(2 * tp) * 288 + fb0) * 1024;
    const char* wp = ws + OFF_W2T + ((size_t)fb0 * 18 + nt0) * 1024;
    const char* wsp = ws + OFF_W2T + ((size_t)fb0 * 18 + nts) * 1024;
    const unsigned lo16 = lane * 16;
    BAR();
    f32x16 acc[2][5];
#pragma unroll
    for (int a = 0; a < 2; ++a)
#pragma unroll
      for (int n = 0; n < 5; ++n)
#pragma unroll
        for (int i = 0; i < 16; ++i) acc[a][n][i] = 0.f;
    auto stage_kv = [&]() {
      __builtin_amdgcn_s_setprio(3);
#pragma unroll 1
      for (int i = g; i < 58; i += 4) {
        glds16(kvg + (20 + i) * 1024, lo16, smem + (20 + i) * 1024);
        glds16(kvg + KV_HALF + (20 + i) * 1024, lo16, smem + KV_HALF + (20 + i) * 1024);
      }
      __builtin_amdgcn_s_setprio(GEM_PRIO);
      STAMP(6);
    };
    auto kv_ready = [&]() {
      WAIT_VM(12);
      flag_set(smem, FL_KVG, g, lane);
      STAMP(1);
    };
#define G2ROT 0
    const int rot = G2ROT;
    if (ng & 1) gemm2_all<4, 5>(hp0, wp, wsp, lo16, rot, acc, stage_kv, kv_ready);
    else        gemm2_all<5, 4>(hp0, wp, wsp, lo16, rot, acc, stage_kv, kv_ready);
    STAMP(2);
    flag_wait_all(smem, FL_PROG);
    STAMP(3);
    LDSP char* mine   = smem + (g >> 1) * KV_HALF + (g & 1) * 24576 + lo16;
    LDSP char* theirs = smem + (g >> 1) * KV_HALF + ((g & 1) ^ 1) * 24576 + lo16;
    LDSP char* other  = smem + ((g >> 1) ^ 1) * KV_HALF + ((g & 1) ^ 1) * 24576 + lo16;
    auto reduce = [&](auto aa, auto hh_) {
      constexpr int A = decltype(aa)::value, HF = decltype(hh_)::value;
#pragma unroll
      for (int n = 0; n < 5; ++n)
#pragma unroll
        for (int q = 0; q < 4; ++q)
          *(LDSP f32x4*)(mine + (n * 4 + q) * 1024) =
              f32x4{acc[A ^ 1][n][4 * q], acc[A ^ 1][n][4 * q + 1], acc[A ^ 1][n][4 * q + 2], acc[A ^ 1][n][4 * q + 3]};
      WAIT_LGKM0();
      flag_set(smem, FL_GW, g, lane);
      flag_wait_one(smem, FL_GW, g ^ 1);
#pragma unroll
      for (int n = 0; n < 5; ++n)
#pragma unroll
        for (int q = 0; q < 4; ++q) {
          f32x4 v = *(LDSP f32x4*)(theirs + (n * 4 + q) * 1024);
#pragma unroll
          for (int i = 0; i < 4; ++i) acc[A][n][4 * q + i] += v[i];
        }
      WAIT_LGKM0();
#pragma unroll
      for (int n = 0; n < 5; ++n)
#pragma unroll
        for (int q = 0; q < 2; ++q) {
          constexpr int r0 = 8 * (HF ^ 1);
          *(LDSP f32x4*)(theirs + (n * 2 + q) * 1024) =
              f32x4{acc[A][n][r0 + 4 * q], acc[A][n][r0 + 4 * q + 1], acc[A][n][r0 + 4 * q + 2], acc[A][n][r0 + 4 * q + 3]};
        }
      WAIT_LGKM0();
      flag_set(smem, FL_GW2, g, lane);
      flag_wait_one(smem, FL_GW2, g ^ 2);
#pragma unroll
      for (int n = 0; n < 5; ++n) {
        const int nt = (n < 4) ? nt0 + n : nts;
        const int slot = (n < 4) ? kh : kh + 2 * (ng & 1);
        char* pp = ws + OFF_PART + (((size_t)slot * 64 + (2 * tp + A)) * 18 + nt) * 2048 + HF * 1024 + lo16;
        u32x4 o4;
#pragma unroll
        for (int q = 0; q < 2; ++q) {
          f32x4 v = *(LDSP f32x4*)(other + (n * 2 + q) * 1024);
#pragma unroll
          for (int i = 0; i < 4; ++i) v[i] += acc[A][n][8 * HF + 4 * q + i];
          o4[2 * q] = pack2(v[0], v[1]);
          o4[2 * q + 1] = pack2(v[2], v[3]);
        }
        store16(pp, o4);
      }
    };
    using I0 = std::integral_constant<int, 0>;
    using I1 = std::integral_constant<int, 1>;
    if (g == 0) reduce(I0{}, I0{});
    else if (g == 1) reduce(I1{}, I0{});
    else if (g == 2) reduce(I0{}, I1{});
    else reduce(I1{}, I1{});
  }
  STAMP(4);
  STAMP(5);
  TS_FLUSH(1);
}

constexpr int K3_LDS = 6 * 6 * 4096;
__global__ __launch_bounds__(384)
void k3(const u16* __restrict__ x, const u16* __restrict__ b2, char* __restrict__ ws, u16* __restrict__ out) {
  TS_DECL; STAMP(0);
  extern __shared__ __attribute__((aligned(16))) char smem_[];
  LDSP char* smem = (LDSP char*)smem_;
  const int tid = threadIdx.x, lane = tid & 63;
  const int w = __builtin_amdgcn_readfirstlane(tid >> 6);
  const int b = blockIdx.x;
  const int tp = (b & 7) * 4 + (b >> 3) / 6, ng = (b >> 3) % 6;
  const int t32 = lane & 31, hh = lane >> 5;
  const int tt = 2 * tp + w / 3, nt = 3 * ng + w % 3;
  const int token = tt * 32 + t32;
  const char* ap = ws + OFF_AO + ((size_t)(2 * tp) * 36 + w * 6) * 1024 + lane * 16;
  const char* wp = ws + OFF_WOT + ((size_t)(w * 6) * 18 + 3 * ng) * 1024 + lane * 16;
  const bool shared = (nt == 4 || nt == 13);
  const size_t SL = (size_t)64 * 18 * 2048;
  const char* pp = ws + OFF_PART + ((size_t)tt * 18 + nt) * 2048 + lane * 16;
  const char* pp2 = shared ? pp + 2 * SL : pp;
  s16x8 af[2][6], wf[3][6];
  u32x4 pt[4][2];
  u32x2 xv[4], bv[4];
#pragma unroll
  for (int k = 0; k < 6; ++k) {
    af[0][k] = *(const s16x8*)(ap + k * 1024);
    wf[0][k] = *(const s16x8*)(wp + k * 18432);
    af[1][k] = *(const s16x8*)(ap + 36 * 1024 + k * 1024);
    wf[1][k] = *(const s16x8*)(wp + k * 18432 + 1024);
    wf[2][k] = *(const s16x8*)(wp + k * 18432 + 2048);
  }
#pragma unroll
  for (int h = 0; h < 2; ++h) {
    pt[0][h] = *(const u32x4*)(pp + h * 1024);
    pt[1][h] = *(const u32x4*)(pp + SL + h * 1024);
    pt[2][h] = *(const u32x4*)(pp2 + h * 1024);
    pt[3][h] = *(const u32x4*)(pp2 + SL + h * 1024);
  }
#pragma unroll
  for (int q = 0; q < 4; ++q) {
    const int col = nt * 32 + 8 * q + 4 * hh;
    xv[q] = *(const u32x2*)((const char*)x + (size_t)token * 1152 + col * 2);
    bv[q] = *(const u32x2*)((const char*)b2 + col * 2);
  }
  SCHED();
  f32x16 acc[2][3];
#pragma unroll
  for (int i = 0; i < 2; ++i)
#pragma unroll
    for (int jn = 0; jn < 3; ++jn)
#pragma unroll
      for (int r = 0; r < 16; ++r) acc[i][jn][r] = 0.f;
#pragma unroll
  for (int k = 0; k < 6; ++k)
#pragma unroll
    for (int i = 0; i < 2; ++i)
#pragma unroll
      for (int jn = 0; jn < 3; ++jn) acc[i][jn] = MFMA32(wf[jn][k], af[i][k], acc[i][jn]);
#pragma unroll
  for (int i = 0; i < 2; ++i)
#pragma unroll
    for (int jn = 0; jn < 3; ++jn)
#pragma unroll
      for (int q = 0; q < 4; ++q) {
        f32x4 v = {acc[i][jn][4 * q], acc[i][jn][4 * q + 1], acc[i][jn][4 * q + 2], acc[i][jn][4 * q + 3]};
        *(LDSP f32x4*)(smem + ((w * 6 + i * 3 + jn) * 4 + q) * 1024 + lane * 16) = v;
      }
  const float sh = shared ? 1.f : 0.f;
  f32x4 fin[4];
#pragma unroll
  for (int q = 0; q < 4; ++q) {
#pragma unroll
    for (int e = 0; e < 2; ++e) {
      const unsigned p0 = pt[0][q >> 1][(q & 1) * 2 + e], p1 = pt[1][q >> 1][(q & 1) * 2 + e];
      const unsigned p2 = pt[2][q >> 1][(q & 1) * 2 + e], p3 = pt[3][q >> 1][(q & 1) * 2 + e];
      const float lo = (__builtin_bit_cast(float, p0 << 16) + __builtin_bit_cast(float, p1 << 16)) +
                       sh * (__builtin_bit_cast(float, p2 << 16) + __builtin_bit_cast(float, p3 << 16));
      const float hi = (__builtin_bit_cast(float, p0 & 0xffff0000u) + __builtin_bit_cast(float, p1 & 0xffff0000u)) +
                       sh * (__builtin_bit_cast(float, p2 & 0xffff0000u) + __builtin_bit_cast(float, p3 & 0xffff0000u));
      fin[q][2 * e]     = lo + __builtin_bit_cast(float, xv[q][e] << 16) + __builtin_bit_cast(float, bv[q][e] << 16);
      fin[q][2 * e + 1] = hi + __builtin_bit_cast(float, xv[q][e] & 0xffff0000u) + __builtin_bit_cast(float, bv[q][e] & 0xffff0000u);
    }
  }
  STAMP(1);
  __syncthreads();
#pragma unroll
  for (int q = 0; q < 4; ++q) {
    f32x4 v = fin[q];
#pragma unroll
    for (int o = 0; o < 6; ++o) v += *(LDSP f32x4*)(smem + ((o * 6 + w) * 4 + q) * 1024 + lane * 16);
    u32x2 ov = {pack2(v[0], v[1]), pack2(v[2], v[3])};
    *(u32x2*)((char*)out + (size_t)token * 1152 + (nt * 32 + 8 * q + 4 * hh) * 2) = ov;
  }
  STAMP(2);
  STAMP(3);
  TS_FLUSH(2);
}

extern "C" void kernel_launch(void* const* d_in, const int* in_sizes, int n_in,
                              void* d_out, int out_size, void* d_ws, size_t ws_size,
                              hipStream_t stream) {
  const u16* x  = (const u16*)d_in[0];
  const u16* wq = (const u16*)d_in[1];
  const u16* wk = (const u16*)d_in[2];
  const u16* wv = (const u16*)d_in[3];
  const u16* wo = (const u16*)d_in[4];
  const u16* w1 = (const u16*)d_in[6];
  const u16* b1 = (const u16*)d_in[7];
  const u16* w2 = (const u16*)d_in[8];
  const u16* b2 = (const u16*)d_in[9];
  char* ws = (char*)d_ws;
  (void)hipFuncSetAttribute((const void*)k1, hipFuncAttributeMaxDynamicSharedMemorySize, K1_LDS);
  (void)hipFuncSetAttribute((const void*)k2, hipFuncAttributeMaxDynamicSharedMemorySize, K2_LDS);
  (void)hipFuncSetAttribute((const void*)k3, hipFuncAttributeMaxDynamicSharedMemorySize, K3_LDS);
  k1<<<256, 512, K1_LDS, stream>>>(x, wq, wk, wv, wo, w1, b1, w2, ws);
  k2<<<256, 512, K2_LDS, stream>>>(ws);
  k3<<<192, 384, K3_LDS, stream>>>(x, b2, ws, (u16*)d_out);
}
```

```cpp
#include <hip/hip_runtime.h>
#include <hip/hip_bf16.h>
#include <stdint.h>
#include <type_traits>

#define DI __device__ __forceinline__
#define LDSP __attribute__((address_space(3)))
#define GLP __attribute__((address_space(1)))

typedef unsigned short u16;
typedef __attribute__((ext_vector_type(8))) short s16x8;
typedef __attribute__((ext_vector_type(4))) short s16x4;
typedef __attribute__((ext_vector_type(16))) float f32x16;
typedef __attribute__((ext_vector_type(4))) float f32x4;
typedef __attribute__((ext_vector_type(2))) float f32x2;
typedef __attribute__((ext_vector_type(2))) __bf16 bf16x2;
typedef __attribute__((ext_vector_type(4))) unsigned u32x4;
typedef __attribute__((ext_vector_type(2))) unsigned u32x2;

constexpr size_t OFF_H    = 0;
constexpr size_t OFF_W2T  = OFF_H + 18874368;
constexpr size_t OFF_WOT  = OFF_W2T + 5308416;
constexpr size_t OFF_Q    = OFF_WOT + 663552;
constexpr size_t OFF_KV   = OFF_Q + 2359296;
constexpr size_t OFF_AO   = OFF_KV + 159744;
constexpr size_t OFF_PART = OFF_AO + 2359296;
constexpr size_t OFF_RED  = OFF_PART + 18874368;
constexpr int KV_GRP = 9984, KV_TILE = 2240, KV_BYTES = 159744, KV_HALF = 79872;

#define MFMA32(a, b, c) __builtin_amdgcn_mfma_f32_32x32x16_bf16((a), (b), (c), 0, 0, 0)
#define WAIT_VM(n) asm volatile("s_waitcnt vmcnt(" #n ")" ::: "memory")
#define WAIT_LGKM0() asm volatile("s_waitcnt lgkmcnt(0)" ::: "memory")
#define BAR() __builtin_amdgcn_s_barrier()
#define TS_DECL
#define STAMP(i)
#define TS_FLUSH(kid)

DI unsigned pack2(float a, float b) {
  f32x2 v = {a, b};
  return __builtin_bit_cast(unsigned, __builtin_convertvector(v, bf16x2));
}
DI float bf2f(u16 v) { return __builtin_bit_cast(float, (unsigned)v << 16); }
DI float dot2(unsigned a, unsigned b, float c) {
  return __builtin_amdgcn_fdot2_f32_bf16(__builtin_bit_cast(bf16x2, a), __builtin_bit_cast(bf16x2, b), c, false);
}
DI void glds16(const void* gbase, unsigned voff, LDSP char* l) {
  unsigned keep;
  asm volatile("s_mov_b32 %0, m0\n\ts_mov_b32 m0, %3\n\ts_nop 0\n\tglobal_load_lds_dwordx4 %1, %2\n\ts_mov_b32 m0, %0"
               : "=&s"(keep) : "v"(voff), "s"(gbase), "s"((unsigned)(size_t)l) : "memory");
}
#define IN_POL ""
DI void glds16_in(const void* gbase, unsigned voff, LDSP char* l) {
  unsigned keep;
  asm volatile("s_mov_b32 %0, m0\n\ts_mov_b32 m0, %3\n\ts_nop 0\n\tglobal_load_lds_dwordx4 %1, %2" IN_POL "\n\ts_mov_b32 m0, %0"
               : "=&s"(keep) : "v"(voff), "s"(gbase), "s"((unsigned)(size_t)l) : "memory");
}
DI void glds4(const void* gbase, unsigned voff, LDSP char* l) {
  unsigned keep;
  asm volatile("s_mov_b32 %0, m0\n\ts_mov_b32 m0, %3\n\ts_nop 0\n\tglobal_load_lds_dword %1, %2\n\ts_mov_b32 m0, %0"
               : "=&s"(keep) : "v"(voff), "s"(gbase), "s"((unsigned)(size_t)l) : "memory");
}
#define ST_FLAVOR 2
DI void store16(void* p, u32x4 v) {
  asm volatile("global_store_dwordx4 %0, %1, off sc0 sc1\n\ts_nop 1" :: "v"(p), "v"(v) : "memory");
}
DI s16x8 tr_frag(LDSP char* p, int second_off) {
  s16x4 lo = __builtin_amdgcn_ds_read_tr16_b64_v4i16((LDSP s16x4*)p);
  s16x4 hi = __builtin_amdgcn_ds_read_tr16_b64_v4i16((LDSP s16x4*)(p + second_off));
  return __builtin_shufflevector(lo, hi, 0, 1, 2, 3, 4, 5, 6, 7);
}
DI float other_half(float v) {
  unsigned u = __builtin_bit_cast(unsigned, v);
  auto r = __builtin_amdgcn_permlane32_swap(u, u, false, false);
  unsigned o = (threadIdx.x & 32) ? r[0] : r[1];
  return __builtin_bit_cast(float, o);
}
DI float gelu_tanh(float z) {
  const float c0 = -2.302208198f;
  const float c1 = -0.1029432397f;
  float t = z * z;
  float p = __builtin_fmaf(t, c1, c0);
  float e = __builtin_amdgcn_exp2f(z * p);
  return z * __builtin_amdgcn_rcpf(1.0f + e);
}

constexpr int K1_SLOT = 36864;
constexpr int K1_BIAS = 4 * K1_SLOT;
constexpr int K1_CS   = K1_BIAS + 1152;
constexpr int K1_CSQ  = K1_CS + 1152;
constexpr int K1_STAT = K1_CSQ + 1024;
constexpr int K1_PF   = K1_STAT + 1024;
constexpr int K1_LDS  = K1_PF + 256;

__host__ __device__ constexpr int w_slot(int st) { return (0x231023120ll >> (4 * st)) & 3; }

#define SCHED() __builtin_amdgcn_sched_barrier(0)
#define SGB(mask, n) __builtin_amdgcn_sched_group_barrier((mask), (n), 0)

DI u32x2 gelu4(float a0, float a1, float a2, float a3, const f32x4& cs, const f32x4& bb, float negm, float r, float c0) {
  float z0, z1, z2, z3, t0, t1, t2, t3;
  unsigned o0, o1;
  asm volatile(
      "v_fma_f32 %2, %18, %14, %10\n\tv_fma_f32 %3, %18, %15, %11\n\tv_fma_f32 %4, %18, %16, %12\n\tv_fma_f32 %5, %18, %17, %13\n\t"
      "v_fma_f32 %2, %19, %2, %21\n\tv_fma_f32 %3, %19, %3, %22\n\tv_fma_f32 %4, %19, %4, %23\n\tv_fma_f32 %5, %19, %5, %24\n\t"
      "v_mul_f32 %6, %2, %2\n\tv_mul_f32 %7, %3, %3\n\tv_mul_f32 %8, %4, %4\n\tv_mul_f32 %9, %5, %5\n\t"
      "v_fmamk_f32 %6, %6, 0xbdd2d3e8, %20\n\tv_fmamk_f32 %7, %7, 0xbdd2d3e8, %20\n\tv_fmamk_f32 %8, %8, 0xbdd2d3e8, %20\n\tv_fmamk_f32 %9, %9, 0xbdd2d3e8, %20\n\t"
      "v_mul_f32 %6, %2, %6\n\tv_mul_f32 %7, %3, %7\n\tv_mul_f32 %8, %4, %8\n\tv_mul_f32 %9, %5, %9\n\t"
      "v_exp_f32 %6, %6\n\tv_exp_f32 %7, %7\n\tv_exp_f32 %8, %8\n\tv_exp_f32 %9, %9\n\t"
      "v_add_f32 %6, 1.0, %6\n\tv_add_f32 %7, 1.0, %7\n\tv_add_f32 %8, 1.0, %8\n\tv_add_f32 %9, 1.0, %9\n\t"
      "v_rcp_f32 %6, %6\n\tv_rcp_f32 %7, %7\n\tv_rcp_f32 %8, %8\n\tv_rcp_f32 %9, %9\n\t"
      "v_mul_f32 %2, %2, %6\n\tv_mul_f32 %3, %3, %7\n\tv_mul_f32 %4, %4, %8\n\tv_mul_f32 %5, %5, %9\n\t"
      "v_cvt_pk_bf16_f32 %0, %2, %3\n\tv_cvt_pk_bf16_f32 %1, %4, %5"
      : "=&v"(o0), "=&v"(o1), "=&v"(z0), "=&v"(z1), "=&v"(z2), "=&v"(z3), "=&v"(t0), "=&v"(t1), "=&v"(t2), "=&v"(t3)
      : "v"(a0), "v"(a1), "v"(a2), "v"(a3), "v"(cs[0]), "v"(cs[1]), "v"(cs[2]), "v"(cs[3]), "v"(negm), "v"(r), "v"(c0),
        "v"(bb[0]), "v"(bb[1]), "v"(bb[2]), "v"(bb[3]));
  u32x2 o = {o0, o1};
  return o;
}

template <typename F>
DI void gelu_tile(const f32x16& a, LDSP char* cs_p, LDSP char* bias_p, char* out_p, float negm, float r, F&& between) {
  const float c0 = -2.302208198f;
#pragma unroll
  for (int hf = 0; hf < 2; ++hf) {
    f32x4 cs0 = *(LDSP f32x4*)(cs_p + hf * 64), cs1 = *(LDSP f32x4*)(cs_p + hf * 64 + 32);
    f32x4 bb0 = *(LDSP f32x4*)(bias_p + hf * 64), bb1 = *(LDSP f32x4*)(bias_p + hf * 64 + 32);
    between(2 * hf);
    u32x2 lo = gelu4(a[8 * hf], a[8 * hf + 1], a[8 * hf + 2], a[8 * hf + 3], cs0, bb0, negm, r, c0);
    between(2 * hf + 1);
    u32x2 hi = gelu4(a[8 * hf + 4], a[8 * hf + 5], a[8 * hf + 6], a[8 * hf + 7], cs1, bb1, negm, r, c0);
    u32x4 o = {lo[0], lo[1], hi[0], hi[1]};
    store16(out_p + hf * 1024, o);
  }
}

DI void colsum_frag(const s16x8& a, const s16x8& sel, f32x4& cs) {
  cs = __builtin_amdgcn_mfma_f32_16x16x32_bf16(sel, a, cs, 0, 0, 0);
}

template <int KT>
DI void w1_stage(LDSP char* sb0, LDSP char* sb1, LDSP char* sb2, const s16x8 (&xf)[36], f32x16 (&acc)[3], const s16x8& sel, f32x4& cs) {
  asm volatile("" : "+v"(sb0), "+v"(sb1), "+v"(sb2));
  s16x8 a[2][3];
  a[0][0] = tr_frag(sb0, 768); a[0][1] = tr_frag(sb1, 768); a[0][2] = tr_frag(sb2, 768);
#pragma unroll
  for (int ks = 0; ks < 12; ++ks) {
    const int cur = ks & 1;
    if (ks + 1 < 12) {
      a[cur ^ 1][0] = tr_frag(sb0 + (ks + 1) * 3072, 768);
      a[cur ^ 1][1] = tr_frag(sb1 + (ks + 1) * 3072, 768);
      a[cur ^ 1][2] = tr_frag(sb2 + (ks + 1) * 3072, 768);
    }
#pragma unroll
    for (int t = 0; t < 3; ++t) acc[t] = MFMA32(a[cur][t], xf[12 * KT + ks], acc[t]);
    colsum_frag(a[cur][0], sel, cs);
    SGB(0x100, 2); SGB(0x8, 1); SGB(0x100, 2); SGB(0x8, 1); SGB(0x100, 2); SGB(0x8, 2);
    SCHED();
  }
}

DI void qkv_stage(LDSP char* sb, const s16x8 (&xf)[36], f32x16& qa, const s16x8& sel, f32x4& cs) {
  asm volatile("" : "+v"(sb));
  s16x8 a[3];
  a[0] = tr_frag(sb, 256);
  a[1] = tr_frag(sb + 1024, 256);
#pragma unroll
  for (int s = 0; s < 36; ++s) {
    if (s + 2 < 36) a[(s + 2) % 3] = tr_frag(sb + (s + 2) * 1024, 256);
    qa = MFMA32(a[s % 3], xf[s], qa);
    colsum_frag(a[s % 3], sel, cs);
    SCHED();
  }
}

__global__ __launch_bounds__(512, 2)
void k1(const u16* __restrict__ x, const u16* __restrict__ wq, const u16* __restrict__ wk, const u16* __restrict__ wv,
        const u16* __restrict__ wo, const u16* __restrict__ w1, const u16* __restrict__ b1, const u16* __restrict__ w2,
        char* __restrict__ ws) {
  extern __shared__ __attribute__((aligned(16))) char smem_[];
  LDSP char* smem = (LDSP char*)smem_;
  const int tid = threadIdx.x, lane = tid & 63;
  const int w8 = __builtin_amdgcn_readfirstlane(tid >> 6);
  const int w = w8 & 3, grp = w8 >> 2;
  const int b = blockIdx.x, xcd = b & 7, idx = b >> 3;
#define K1MAP 0
  const int tg = (xcd >> 1) * 4 + (idx & 3), j = (xcd & 1) * 8 + (idx >> 2);
  const int t32 = lane & 31, hh = lane >> 5;
  const int tt = tg * 4 + w;
  const bool heavy = (j & 3) == 0;
  const int rk = 0, rc = 0;
  TS_DECL; STAMP(0);

  const char* w1w = (const char*)w1 + (size_t)(48 * w) * 9216 + (size_t)j * 576;
  unsigned off3[3];
#pragma unroll
  for (int i = 0; i < 3; ++i) {
    const unsigned bb = 1024 * i + 16 * lane;
    off3[i] = (bb / 192) * 9216 + (bb % 192);
  }
  auto issue_w1_rows = [&](int st, int r) {
    const int c = st / 3, kt = st - 3 * c;
    const char* g = w1w + ((c + rc) % 3) * 192 + (size_t)((kt + rk) % 3) * 192 * 9216 + r * 16 * 9216;
    LDSP char* l = smem + w_slot(st) * K1_SLOT + w * 9216 + r * 3072;
#pragma unroll
    for (int i = 0; i < 3; ++i) glds16_in(g, off3[i], l + i * 1024);
  };
  auto issue_w1 = [&](int st) {
#pragma unroll
    for (int r = 0; r < 3; ++r) issue_w1_rows(st, r);
  };
  auto issue_x = [&](int q, int slot) {
    int ln = tid & 63; asm volatile("" : "+v"(ln));
    const char* g = (const char*)x + (size_t)(tt * 32) * 1152;
    LDSP char* l = smem + slot * K1_SLOT + w * 9216;
    unsigned row = (unsigned)ln / 18, pos = (unsigned)ln % 18;
    const unsigned b0 = q * 288 + rk * 384;
#pragma unroll
    for (int i = 0; i < 9; ++i) {
      unsigned cb = b0 + ((pos ^ ((row >> 3) & 1)) << 4);
      cb = cb >= 1152 ? cb - 1152 : cb;
      glds16(g, row * 1152 + cb, l + i * 1024);
      pos += 10; row += 3;
      if (pos >= 18) { pos -= 18; row += 1; }
    }
  };
  auto issue_qkv = [&](int tile, int slot, int part) {
    int ln = tid & 63; asm volatile("" : "+v"(ln));
    LDSP char* l = smem + slot * K1_SLOT + w * 9216 + part * 3072;
    const int row0 = 144 * w + 48 * part + 192 * rk;
    if (tile < 18) {
      const char* g = (const char*)wq + tile * 64;
      const unsigned vo = (ln >> 2) * 1152 + (ln & 3) * 16;
#pragma unroll
      for (int i = 0; i < 3; ++i) glds16_in(g + (size_t)((row0 + 16 * i) % 576) * 1152, vo, l + i * 1024);
    } else {
      const u16* src = (tile == 18) ? wk : wv;
      int dw = ln & 15; dw = dw > 8 ? 8 : dw;
      const unsigned vo = (ln >> 4) * 36 + dw * 4;
#pragma unroll
      for (int i = 0; i < 12; ++i) glds4((const char*)src + ((row0 + 4 * i) % 576) * 36, vo, l + i * 256);
    }
  };
  const int lw = (tg * 12 + (j - (j >> 2) - 1)) * 4 + w;
  auto issue_unit = [&](int part) {
    if (lw < 648) {
      int ln = tid & 63; asm volatile("" : "+v"(ln));
      LDSP char* l = smem + 0 * K1_SLOT + w * 9216 + part * 3072;
      const int u = lw - 576, ks = u >> 1;
      const bool plain = ks < 32;
      const char* g = lw < 576 ? (const char*)w2 + (size_t)((lw >> 1) * 16) * 1152 + (lw & 1) * 576
                               : (const char*)wo + (size_t)(plain ? ks * 18 : 144 * (ks - 32) + 16) * 1152 + (u & 1) * 576;
      const unsigned p0 = 192 * part + ln;
      unsigned row = p0 / 36, pos = p0 % 36;
#pragma unroll
      for (int i = 0; i < 3; ++i) {
        const unsigned srow = plain ? row : 18 * (row >> 1) + (row & 1);
        glds16_in(g, srow * 1152 + pos * 16, l + i * 1024);
        pos += 28; row += 1;
        if (pos >= 36) { pos -= 36; row += 1; }
      }
    }
  };

  unsigned bias_raw = 0, bias_raw2 = 0;
  if (grp == 1) {
    asm volatile("global_load_ushort %0, %2, %4\n\tglobal_load_ushort %1, %3, %4" : "=&v"(bias_raw), "=&v"(bias_raw2)
                 : "v"((unsigned)((tid - 256) * 2)), "v"((unsigned)((tid < 288 ? tid : 287) * 2)), "s"(b1 + j * 288) : "memory");
  }
  if (grp == 0) { issue_x(0, 3); issue_x(1, 1); }
  else          { issue_w1(0);   issue_w1(1); }
  s16x8 xf[36];
  const int xl = w * 9216 + t32 * 288 + ((hh ^ ((t32 >> 3) & 1)) << 4);
  auto read_x = [&](int q, int slot) {
#pragma unroll
    for (int s = 0; s < 9; ++s) xf[9 * q + s] = *(LDSP s16x8*)(smem + slot * K1_SLOT + xl + s * 32);
  };
  if (grp == 0) WAIT_VM(9);
  BAR();
  read_x(0, 3);
  if (grp == 0) { WAIT_VM(0); }
  else {
    asm volatile("s_waitcnt vmcnt(9)" : "+v"(bias_raw), "+v"(bias_raw2) :: "memory");
    *(LDSP float*)(smem + K1_BIAS + (tid - 256) * 4) = __builtin_bit_cast(float, bias_raw << 16);
    if (tid < 288) *(LDSP float*)(smem + K1_BIAS + tid * 4) = __builtin_bit_cast(float, bias_raw2 << 16);
  }
  WAIT_LGKM0(); BAR();
  STAMP(1);
  read_x(1, 1);
  if (grp == 1) issue_x(2, 3);
  WAIT_LGKM0(); BAR();
  STAMP(2);

  const int q4 = (lane & 15) >> 2, p4 = lane & 3, g16 = (lane >> 4) & 1;
  const int rot = (w == 3) ? 0 : w;
  const int tl0 = rot, tl1 = (rot + 1) % 3, tl2 = (rot + 2) % 3;
  const int lo_w1 = (8 * hh + q4) * 192 + 32 * g16 + 8 * p4;
  const int lo_q  = (8 * hh + q4) * 64 + 32 * g16 + 8 * p4;
  char* hout = ws + OFF_H + ((size_t)tt * 288 + (size_t)j * 18) * 1024 + lane * 16;
  f32x16 acc[3];
  float negm = 0.f, r1 = 0.f, rm = 0.f;
  const unsigned selv = ((lane & 15) == ((lane >> 4) & 1)) ? 0x3f803f80u : 0u;
  const u32x4 selu = {selv, selv, selv, selv};
  const s16x8 sel = __builtin_bit_cast(s16x8, selu);

  auto load_stats = [&]() {
    f32x2 st = *(LDSP f32x2*)(smem + K1_STAT + (w * 32 + t32) * 8);
    negm = -st[0] * (1.0f / 576.0f);
    const float var = __builtin_fmaxf(st[1] * (1.0f / 576.0f) - negm * negm, 0.f);
    r1 = __builtin_amdgcn_rsqf(var + 1e-5f);
    rm = r1 * __builtin_amdgcn_rsqf(var * r1 * r1 + 1e-5f);
  };
#define HELP_PRIO 1
  auto mfma_chunk = [&](auto cc) {
    constexpr int c = decltype(cc)::value;
    __builtin_amdgcn_s_setprio(0);
    f32x4 cs = {0.f, 0.f, 0.f, 0.f};
#pragma unroll
    for (int t = 0; t < 3; ++t)
#pragma unroll
      for (int i = 0; i < 16; ++i) acc[t][i] = 0.f;
    LDSP char* s0 = smem + lo_w1 + tl0 * 64;
    LDSP char* s1 = smem + lo_w1 + tl1 * 64;
    LDSP char* s2 = smem + lo_w1 + tl2 * 64;
    constexpr int o0 = w_slot(3 * c) * K1_SLOT, o1 = w_slot(3 * c + 1) * K1_SLOT, o2 = w_slot(3 * c + 2) * K1_SLOT;
    w1_stage<0>(s0 + o0, s1 + o0, s2 + o0, xf, acc, sel, cs);
    if (c == 1) { WAIT_VM(9); } else if (c == 2) { WAIT_VM(11); }
    WAIT_LGKM0(); BAR();
    if (c == 0) { read_x(2, 3); WAIT_LGKM0(); BAR(); }
    w1_stage<1>(s0 + o1, s1 + o1, s2 + o1, xf, acc, sel, cs);
    if (c != 0) WAIT_VM(0);
    WAIT_LGKM0(); BAR();
    if (c == 0) { read_x(3, 0); WAIT_LGKM0(); BAR(); }
    w1_stage<2>(s0 + o2, s1 + o2, s2 + o2, xf, acc, sel, cs);
    if (lane < 16 && w < 3) {
      *(LDSP float*)(smem + K1_CS + (c * 96 + tl0 * 32 + lane) * 4) = cs[0];
      *(LDSP float*)(smem + K1_CS + (c * 96 + tl0 * 32 + 16 + lane) * 4) = cs[1];
    }
    WAIT_LGKM0(); BAR();
    __builtin_amdgcn_s_setprio(HELP_PRIO);
  };
  auto gelu_t = [&](int c, int t, int tl, int st_issue) {
    const int ca = (c + rc) % 3;
    gelu_tile(acc[t], smem + K1_CS + (c * 96 + tl * 32) * 4 + hh * 16, smem + K1_BIAS + (ca * 96 + tl * 32) * 4 + hh * 16,
              hout + (ca * 6 + tl * 2) * 1024, negm, rm, [&](int i) {
      if (i < 3) {
        if (st_issue >= 0 && st_issue < 9) issue_w1_rows(st_issue, i);
        else if (st_issue == 9) issue_qkv(j, 1, i);
        else if (st_issue == 10) { if (heavy) issue_qkv(16 + (j >> 2), 0, i); else issue_unit(i); }
      }
    });
  };

  if (grp == 0) {
    mfma_chunk(std::integral_constant<int, 0>{});
    STAMP(3);
    load_stats();
    gelu_t(0, 0, tl0, 6); WAIT_LGKM0(); BAR();
    gelu_t(0, 1, tl1, 7); WAIT_LGKM0(); BAR();
    gelu_t(0, 2, tl2, 8); WAIT_VM(22); WAIT_LGKM0(); BAR();
    STAMP(4);
    mfma_chunk(std::integral_constant<int, 2>{});
    STAMP(5);
    gelu_t(2, 0, tl0, -1); gelu_t(2, 1, tl1, -1); gelu_t(2, 2, tl2, -1);
    STAMP(6);
  } else {
    f32x4 rs = {0.f, 0.f, 0.f, 0.f};
#pragma unroll
    for (int i = 0; i < 16; ++i) acc[0][i] = 0.f;
    auto stats_frag = [&](int s) {
      colsum_frag(xf[s], sel, rs);
      acc[0] = MFMA32(xf[s], xf[s], acc[0]);
    };
    __builtin_amdgcn_s_setprio(HELP_PRIO);
#pragma unroll
    for (int s = 0; s < 18; ++s) { if (s % 6 == 0) issue_w1_rows(2, s / 6); stats_frag(s); }
    WAIT_VM(9); BAR();
    issue_x(3, 0);
    read_x(2, 3); WAIT_LGKM0(); BAR();
#pragma unroll
    for (int s = 18; s < 27; ++s) { if (s % 3 == 0) issue_w1_rows(3, (s - 18) / 3); stats_frag(s); }
    WAIT_VM(9); BAR();
    issue_w1(4);
    read_x(3, 0); WAIT_LGKM0(); BAR();
#pragma unroll
    for (int s = 27; s < 36; ++s) { if (s % 3 == 0) issue_w1_rows(5, (s - 27) / 3); stats_frag(s); }
    {
      const int dreg = (t32 & 3) + 4 * (t32 >> 3);
      float s2 = 0.f;
#pragma unroll
      for (int i = 0; i < 16; ++i) s2 = (i == dreg) ? acc[0][i] : s2;
      if (hh == ((t32 >> 2) & 1)) *(LDSP float*)(smem + K1_STAT + (w * 32 + t32) * 8 + 4) = s2;
      if (lane < 16) {
        *(LDSP float*)(smem + K1_STAT + (w * 32 + lane) * 8) = rs[0];
        *(LDSP float*)(smem + K1_STAT + (w * 32 + 16 + lane) * 8) = rs[1];
      }
    }
    WAIT_VM(18); WAIT_LGKM0(); BAR();
    STAMP(3);
    mfma_chunk(std::integral_constant<int, 1>{});
    STAMP(4);
    load_stats();
    gelu_t(1, 0, tl0, 10); WAIT_LGKM0(); BAR();
    gelu_t(1, 1, tl1, 9); WAIT_LGKM0(); BAR();
    gelu_t(1, 2, tl2, -1);
    WAIT_VM(2); WAIT_LGKM0(); BAR();
#define TAIL_PRIO 2
    __builtin_amdgcn_s_setprio(TAIL_PRIO);
    STAMP(5);

    auto qkv_tile = [&](int tile, int slot) {
      f32x16 qa;
#pragma unroll
      for (int i = 0; i < 16; ++i) qa[i] = 0.f;
      f32x4 cs = {0.f, 0.f, 0.f, 0.f};
      qkv_stage(smem + slot * K1_SLOT + lo_q, xf, qa, sel, cs);
      int ln = tid & 63; asm volatile("" : "+v"(ln));
      const int t32 = ln & 31, hh = ln >> 5, token = tt * 32 + t32;
      LDSP char* cp = smem + K1_CSQ + (w * 2 + (slot & 1)) * 128;
      if (ln < 16) { *(LDSP float*)(cp + ln * 4) = cs[0]; *(LDSP float*)(cp + 64 + ln * 4) = cs[1]; }
      WAIT_LGKM0();
#pragma unroll
      for (int g = 0; g < 4; ++g) {
        f32x4 c4 = *(LDSP f32x4*)(cp + g * 32 + hh * 16);
#pragma unroll
        for (int i = 0; i < 4; ++i) qa[4 * g + i] = __builtin_fmaf(negm, c4[i], qa[4 * g + i]);
      }
      if (tile < 18) {
        const float sc = r1 * (0.23570226039551584f * 1.4426950408889634f);
        char* qp = ws + OFF_Q + (size_t)tt * 36864;
#pragma unroll
        for (int i = 0; i < 8; ++i) {
          const unsigned n = tile * 32 + 8 * (i >> 1) + 4 * hh + 2 * (i & 1);
          const unsigned head = n / 18, d = n - head * 18;
          const unsigned off = d < 16 ? (t32 + 32 * ((d >> 2) & 1)) * 16 + ((d >> 3) * 2 + ((d >> 1) & 1)) * 4 : 1024 + t32 * 4;
          *(unsigned*)(qp + head * 1152 + off) = pack2(qa[2 * i] * sc, qa[2 * i + 1] * sc);
        }
      } else if (tile == 18) {
        u32x4 o = {pack2(qa[0] * r1, qa[1] * r1), pack2(qa[2] * r1, qa[3] * r1),
                   pack2(qa[4] * r1, qa[5] * r1), pack2(qa[6] * r1, qa[7] * r1)};
        char* gp = ws + OFF_KV + (size_t)tg * KV_GRP;
        *(u32x4*)(gp + 1024 + w * KV_TILE + ln * 16) = o;
        if (hh == 0) *(unsigned*)(gp + (w >> 1) * 512 + t32 * 16 + (w & 1) * 4) = pack2(qa[8] * r1, qa[9] * r1);
        else if ((w & 1) == 0) {
          u32x2 cst = {w == 0 ? 0x3f803f80u : 0u, 0u};
          *(u32x2*)(gp + (w >> 1) * 512 + t32 * 16 + 8) = cst;
        }
      } else {
        const int s = t32 >> 4, hk = (t32 >> 2) & 1, jj = ((t32 >> 3) & 1) * 4 + (t32 & 3);
        char* vp = ws + OFF_KV + (size_t)tg * KV_GRP + 2048 + w * KV_TILE + (s * 2 + hk) * 304 + jj * 2;
#pragma unroll
        for (int i = 0; i < 8; ++i) {
          const int d = (i & 3) + 8 * (i >> 2) + 4 * hh;
          *(u16*)(vp + d * 16) = (u16)(pack2(qa[i] * r1, 0.f) & 0xffffu);
        }
        if (hh == 0) {
          *(u16*)(vp + 16 * 16) = (u16)(pack2(qa[8] * r1, 0.f) & 0xffffu);
          *(u16*)(vp + 17 * 16) = (u16)(pack2(qa[9] * r1, 0.f) & 0xffffu);
          *(u16*)(vp + 18 * 16) = (u16)0x3f80;
        }
      }
    };
    qkv_tile(j, 1);
    STAMP(6);
    if (heavy) {
      qkv_tile(16 + (j >> 2), 0);
    } else if (lw < 648) {
      int ln = tid & 63; asm volatile("" : "+v"(ln));
      const int hh = ln >> 5, q4 = (ln & 15) >> 2, p4 = ln & 3, g16 = (ln >> 4) & 1;
      LDSP char* sb = smem + 0 * K1_SLOT + w * 9216 + (4 * hh + q4) * 576 + 32 * g16 + 8 * p4;
      char* dst = (lw < 576) ? ws + OFF_W2T + ((size_t)(lw >> 1) * 18 + (lw & 1) * 9) * 1024
                             : ws + OFF_WOT + ((size_t)((lw - 576) >> 1) * 18 + (lw & 1) * 9) * 1024;
#pragma unroll
      for (int n = 0; n < 9; ++n) {
        s16x8 f = tr_frag(sb + n * 64, 8 * 576);
        store16(dst + n * 1024 + ln * 16, __builtin_bit_cast(u32x4, f));
      }
    }
  }
  STAMP(7);
  STAMP(8);
  TS_FLUSH(0);
}

constexpr int K2_FLAG = KV_BYTES;
constexpr int K2_PF   = KV_BYTES + 128;
constexpr int K2_LDS  = KV_BYTES + 128 + 256;
enum { FL_KVA = 0, FL_KVG = 1, FL_PROG = 2, FL_GW = 3, FL_DONE = 4, FL_SENT = 5, FL_GW2 = 6 };

#define EXP2(x) __builtin_amdgcn_exp2f(x)

DI void flag_set(LDSP char* smem, int set, int i, int lane) {
  if (lane == 0) *(volatile LDSP unsigned*)(smem + K2_FLAG + set * 16 + i * 4) = 1u;
}
DI void flag_wait_all(LDSP char* smem, int set) {
  while (true) {
    u32x4 f = *(volatile LDSP u32x4*)(smem + K2_FLAG + set * 16);
    if (f[0] & f[1] & f[2] & f[3]) break;
    __builtin_amdgcn_s_sleep(2);
  }
  asm volatile("" ::: "memory");
}
DI void flag_wait_one(LDSP char* smem, int set, int i) {
  while (*(volatile LDSP unsigned*)(smem + K2_FLAG + set * 16 + i * 4) == 0u) __builtin_amdgcn_s_sleep(1);
  asm volatile("" ::: "memory");
}

DI void attn_step(f32x16& scur, f32x16& snext, const s16x8& k0, const s16x8& k1, const s16x8& qn0, const s16x8& qn1,
                  const s16x8& vprev1, f32x16& oprev, const s16x8& vcur0, f32x16& ocur, u32x4& p0, u32x4& p1) {
  f32x16 zero;
#pragma unroll
  for (int i = 0; i < 16; ++i) zero[i] = 0.f;
#define EX(i) do { float t_ = scur[i]; asm volatile("v_exp_f32 %0, %0" : "+v"(t_)); scur[i] = t_; } while (0)
#define SPACING 3
  p1[2] = pack2(snext[12], snext[13]);
  p1[3] = pack2(snext[14], snext[15]);
  SCHED();
  snext = MFMA32(k0, qn0, zero);
  SCHED();
#pragma unroll
  for (int i = 0; i < SPACING; ++i) EX(i);
  SCHED();
  snext = MFMA32(k1, qn1, snext);
  SCHED();
#pragma unroll
  for (int i = SPACING; i < 8; ++i) EX(i);
  SCHED();
  p0[0] = pack2(scur[0], scur[1]);
  p0[1] = pack2(scur[2], scur[3]);
  p0[2] = pack2(scur[4], scur[5]);
  p0[3] = pack2(scur[6], scur[7]);
  SCHED();
  oprev = MFMA32(vprev1, __builtin_bit_cast(s16x8, p1), oprev);
  SCHED();
#pragma unroll
  for (int i = 8; i < 8 + SPACING; ++i) EX(i);
  SCHED();
  ocur = MFMA32(vcur0, __builtin_bit_cast(s16x8, p0), ocur);
  SCHED();
#pragma unroll
  for (int i = 8 + SPACING; i < 16; ++i) EX(i);
  SCHED();
  p1[0] = pack2(scur[8], scur[9]);
  p1[1] = pack2(scur[10], scur[11]);
  SCHED();
#undef EX
}

DI s16x8 make_q1(unsigned qe, unsigned mneg, int i, int hh) {
  const unsigned a = (hh == (i >> 1)) ? qe : 0u;
  u32x4 r = {(i & 1) ? 0u : a, (i & 1) ? a : 0u, hh ? 0u : mneg, 0u};
  return __builtin_bit_cast(s16x8, r);
}
DI unsigned neg_hi_lo(float m) {
  unsigned hi = pack2(-m, 0.f) & 0xffffu;
  float rem = -m - __builtin_bit_cast(float, hi << 16);
  unsigned lo = pack2(rem, 0.f) & 0xffffu;
  return hi | (lo << 16);
}
DI int v_row(int t32) { return t32 > 18 ? ((t32 >= 20 && t32 < 28) ? 0 : 4) : t32; }

DI f32x16 attn_exact(const char* kv, int lane, u32x4 q0u, unsigned qe) {
  const int t32 = lane & 31, hh = lane >> 5;
  const s16x8 q0 = __builtin_bit_cast(s16x8, q0u);
  f32x16 zero, o;
#pragma unroll
  for (int i = 0; i < 16; ++i) { zero[i] = 0.f; o[i] = 0.f; }
  float m = -INFINITY;
#pragma unroll 1
  for (int T = 0; T < 64; ++T) {
    const char* gp = kv + (T >> 2) * KV_GRP;
    s16x8 k0 = *(const s16x8*)(gp + 1024 + (T & 3) * KV_TILE + lane * 16);
    s16x8 k1 = *(const s16x8*)(gp + lane * 16);
    f32x16 s = MFMA32(k0, q0, zero);
    s = MFMA32(k1, make_q1(qe, 0u, T & 3, hh), s);
#pragma unroll
    for (int i = 0; i < 16; ++i) m = __builtin_fmaxf(m, s[i]);
  }
  m = __builtin_fmaxf(m, other_half(m));
  const unsigned mneg = neg_hi_lo(m);
#pragma unroll 1
  for (int T = 0; T < 64; ++T) {
    const char* gp = kv + (T >> 2) * KV_GRP;
    const char* tp = gp + 1024 + (T & 3) * KV_TILE;
    s16x8 k0 = *(const s16x8*)(tp + lane * 16);
    s16x8 k1 = *(const s16x8*)(gp + lane * 16);
    s16x8 v0 = *(const s16x8*)(tp + 1024 + hh * 304 + v_row(t32) * 16);
    s16x8 v1 = *(const s16x8*)(tp + 1024 + 608 + hh * 304 + v_row(t32) * 16);
    f32x16 s = MFMA32(k0, q0, zero);
    s = MFMA32(k1, make_q1(qe, mneg, T & 3, hh), s);
    u32x4 p0, p1;
#pragma unroll
    for (int i = 0; i < 4; ++i) {
      p0[i] = pack2(EXP2(s[2 * i]), EXP2(s[2 * i + 1]));
      p1[i] = pack2(EXP2(s[8 + 2 * i]), EXP2(s[8 + 2 * i + 1]));
    }
    o = MFMA32(v0, __builtin_bit_cast(s16x8, p0), o);
    o = MFMA32(v1, __builtin_bit_cast(s16x8, p1), o);
  }
  return o;
}

typedef __amdgpu_buffer_rsrc_t rsrc_t;
DI s16x8 bload(rsrc_t r, unsigned voff, unsigned soff) {
  return __builtin_bit_cast(s16x8, __builtin_amdgcn_raw_buffer_load_b128(r, voff, soff, 0));
}
template <int NT>
DI void gemm2_load(rsrc_t r, unsigned hp, unsigned wp, unsigned wsp, unsigned lo16, int k, s16x8 (&hb)[2], s16x8 (&wb)[5]) {
  hb[0] = bload(r, lo16, hp + k * 1024);
  wb[0] = bload(r, lo16, wp + k * 18432);
  hb[1] = bload(r, lo16, hp + k * 1024 + 288 * 1024);
#pragma unroll
  for (int n = 1; n < 4; ++n) wb[n] = bload(r, lo16 + n * 1024, wp + k * 18432);
  if (NT == 5) wb[4] = bload(r, lo16, wsp + k * 18432);
}
template <int NT, int NTN>
DI void gemm2_step(rsrc_t r, unsigned hp, unsigned wp, unsigned wsp, unsigned lo16, int kn, s16x8 (&hb)[2], s16x8 (&wb)[5],
                   f32x16 (&acc)[2][5]) {
#pragma unroll
  for (int n = 0; n < NT; ++n) {
    acc[0][n] = MFMA32(wb[n], hb[0], acc[0][n]);
    acc[1][n] = MFMA32(wb[n], hb[1], acc[1][n]);
    SCHED();
    if (n < 4) wb[n] = bload(r, lo16 + n * 1024, wp + kn * 18432);
    else if (NTN == 5) wb[4] = bload(r, lo16, wsp + kn * 18432);
    SCHED();
  }
  if (NT == 4 && NTN == 5) wb[4] = bload(r, lo16, wsp + kn * 18432);
  hb[0] = bload(r, lo16, hp + kn * 1024);
  hb[1] = bload(r, lo16, hp + kn * 1024 + 288 * 1024);
  SCHED();
}
template <int N1, int N2, typename F, typename G>
DI void gemm2_all(rsrc_t r, unsigned hp, unsigned wp, unsigned wsp, unsigned lo16, f32x16 (&acc)[2][5], F&& after_first_loads, G&& after_first_step) {
  s16x8 hA[2], wA[5], hB[2], wB[5];
  after_first_loads();
  gemm2_load<N1>(r, hp, wp, wsp, lo16, 0, hA, wA);
  gemm2_load<N1>(r, hp, wp, wsp, lo16, 1, hB, wB);
  SCHED();
  after_first_step();
#pragma unroll 1
  for (int k = 0; k < 16; k += 2) {
    gemm2_step<N1, N1>(r, hp, wp, wsp, lo16, k + 2, hA, wA, acc);
    gemm2_step<N1, N1>(r, hp, wp, wsp, lo16, k + 3, hB, wB, acc);
  }
  gemm2_step<N1, N2>(r, hp, wp, wsp, lo16, 18, hA, wA, acc);
  gemm2_step<N1, N2>(r, hp, wp, wsp, lo16, 19, hB, wB, acc);
#pragma unroll 1
  for (int k = 18; k < 36; k += 2) {
    gemm2_step<N2, N2>(r, hp, wp, wsp, lo16, (k + 2 < 36) ? k + 2 : 35, hA, wA, acc);
    gemm2_step<N2, N2>(r, hp, wp, wsp, lo16, (k + 3 < 36) ? k + 3 : 35, hB, wB, acc);
  }
}

__global__ __launch_bounds__(512, 2)
void k2(const u16* __restrict__ x, char* __restrict__ ws) {
  extern __shared__ __attribute__((aligned(16))) char smem_[];
  LDSP char* smem = (LDSP char*)smem_;
  const int tid = threadIdx.x, lane = tid & 63;
  const int w = __builtin_amdgcn_readfirstlane(tid >> 6);
  const int b = blockIdx.x;
  const int t32 = lane & 31, hh = lane >> 5;
  TS_DECL; STAMP(0);
  if (tid < 32) *(LDSP unsigned*)(smem + K2_FLAG + tid * 4) = 0u;
  const char* kvg = ws + OFF_KV;

  if (w < 4) {
#define ATT_PRIO 0
#define GEM_PRIO 0
    __builtin_amdgcn_s_setprio(ATT_PRIO);
    const int tt = b >> 2, hg = b & 3;
    const int rs = w & 1, kh2 = w >> 1;
    const int head0 = hg * 8 + rs * 4;
#pragma unroll
    for (int i = 0; i < 5; ++i) {
      glds16(kvg + (i * 4 + w) * 1024, lane * 16, smem + (i * 4 + w) * 1024);
      glds16(kvg + KV_HALF + (i * 4 + w) * 1024, lane * 16, smem + KV_HALF + (i * 4 + w) * 1024);
    }
    u32x4 q0u[4];
    unsigned qe[4];
#pragma unroll
    for (int qb = 0; qb < 4; ++qb) {
      const char* qp = ws + OFF_Q + (size_t)tt * 36864 + (head0 + qb) * 1152;
      q0u[qb] = *(const u32x4*)(qp + lane * 16);
      qe[qb] = *(const unsigned*)(qp + 1024 + t32 * 4);
    }
    const char* pf_base;
    unsigned pf_off, pf_step;
    {
      const int xcd = b & 7, a = (b >> 3) * 4 + w, kh = xcd & 1;
      if (a < 48) {
        const unsigned L = (a % 3) * 64 + lane;
        pf_base = ws + OFF_H + ((size_t)((xcd >> 1) * 16 + a / 3) * 288 + kh * 144) * 1024;
        pf_off = (L / 48) * 36 * 1024 + (L % 48) * 128;
        pf_step = 6 * 1024;
      } else {
        const unsigned L = ((a < 102 ? a : 101) - 48) * 64 + lane;
        pf_base = ws + OFF_W2T + (size_t)kh * 144 * 18432;
        pf_off = (L / 864) * 36 * 18432 + (L % 864) * 128;
        pf_step = 6 * 18432;
      }
    }
    WAIT_LGKM0(); BAR();
    WAIT_VM(0);
    flag_set(smem, FL_KVA, w, lane);
    flag_wait_all(smem, FL_KVA);
    STAMP(1);
    s16x8 q0[4], q1[4][4];
    f32x16 o[4];
    f32x16 sa, sb;
#pragma unroll
    for (int i = 0; i < 16; ++i) sb[i] = 0.f;
    {
      s16x8 k0 = *(LDSP s16x8*)(smem + 1024 + lane * 16);
      s16x8 k1 = *(LDSP s16x8*)(smem + lane * 16);
#pragma unroll
      for (int qb = 0; qb < 4; ++qb) {
        asm volatile("" : "+v"(q0u[qb]), "+v"(qe[qb]));
        q0[qb] = __builtin_bit_cast(s16x8, q0u[qb]);
        f32x16 s = MFMA32(k0, q0[qb], sb);
        s = MFMA32(k1, make_q1(qe[qb], 0u, 0, hh), s);
        float m = s[0];
#pragma unroll
        for (int i = 1; i < 16; ++i) m = __builtin_fmaxf(m, s[i]);
        m = __builtin_fmaxf(m, other_half(m));
        const unsigned mneg = neg_hi_lo(m);
#pragma unroll
        for (int i = 0; i < 4; ++i) q1[i][qb] = make_q1(qe[qb], mneg, i, hh);
#pragma unroll
        for (int i = 0; i < 16; ++i) o[qb][i] = 0.f;
      }
    }
    LDSP char* kp = smem + kh2 * KV_HALF + lane * 16;
    LDSP char* vp = smem + kh2 * KV_HALF + 2048 + hh * 304 + v_row(t32) * 16;
    u32x4 p0 = {0u, 0u, 0u, 0u}, p1 = {0u, 0u, 0u, 0u};
    s16x8 k1c = *(LDSP s16x8*)(kp), k1n;
    s16x8 k0b[2], v0b[2], v1b[2];
    k0b[0] = *(LDSP s16x8*)(kp + 1024);
    const unsigned long long vmask = 0x0007ffff0007ffffull;
#pragma unroll
    for (int i = 0; i < 2; ++i) { v0b[i] = s16x8{0, 0, 0, 0, 0, 0, 0, 0}; v1b[i] = s16x8{0, 0, 0, 0, 0, 0, 0, 0}; }
#define VLD(dst, addr, off) asm volatile("s_mov_b64 exec, %2\n\tds_read_b128 %0, %1 offset:%3\n\ts_mov_b64 exec, -1" \
                                         : "+v"(dst) : "v"(addr), "s"(vmask), "i"(off) : "memory")
#define VWAIT(dst) asm volatile("s_waitcnt lgkmcnt(0)" : "+v"(dst) :: "memory")
    VLD(v0b[0], vp, 0);
    sa = MFMA32(k0b[0], q0[0], sb);
    sa = MFMA32(k1c, q1[0][0], sa);
    asm volatile("s_nop 15" : "+v"(sa));
#pragma unroll 1
    for (int g = 0; g < 8; ++g) {
      if (g == 1) flag_wait_all(smem, FL_KVG);
      if (g == 5) flag_set(smem, FL_PROG, w, lane);
      const int nxg = (g < 7) ? KV_GRP : 0;
#pragma unroll
      for (int i = 0; i < 4; ++i) {
        const int c = i & 1, n = c ^ 1;
        const int nt_off = (i < 3) ? (i + 1) * KV_TILE : nxg;
        VWAIT(v0b[c]);
        VLD(v1b[c], vp, i * KV_TILE + 608);
        SCHED();
        attn_step(sa, sb, k0b[c], k1c, q0[1], q1[i][1], v1b[n], o[3], v0b[c], o[0], p0, p1);
        VWAIT(v1b[c]);
        k0b[n] = *(LDSP s16x8*)(kp + 1024 + nt_off);
        if (i == 3) k1n = *(LDSP s16x8*)(kp + nxg);
        SCHED();
        attn_step(sb, sa, k0b[c], k1c, q0[2], q1[i][2], v1b[c], o[0], v0b[c], o[1], p0, p1);
        if (i < 3) VLD(v0b[n], vp, (i + 1) * KV_TILE); else { LDSP char* vn = vp + nxg; VLD(v0b[n], vn, 0); }
        SCHED();
        attn_step(sa, sb, k0b[c], k1c, q0[3], q1[i][3], v1b[c], o[1], v0b[c], o[2], p0, p1);
        attn_step(sb, sa, k0b[n], (i == 3) ? k1n : k1c, q0[0], q1[(i + 1) & 3][0], v1b[c], o[2], v0b[c], o[3], p0, p1);
      }
      k1c = k1n;
      kp += KV_GRP; vp += KV_GRP;
    }
    p1[2] = pack2(sb[12], sb[13]);
    p1[3] = pack2(sb[14], sb[15]);
    o[3] = MFMA32(v1b[1], __builtin_bit_cast(s16x8, p1), o[3]);
    STAMP(2);
    f32x16 keep[2], send[2];
    u32x4 qk0[2];
    unsigned qke[2];
#pragma unroll
    for (int q = 0; q < 2; ++q) {
#pragma unroll
      for (int i = 0; i < 11; ++i) {
        keep[q][i] = kh2 ? o[2 + q][i] : o[q][i];
        send[q][i] = kh2 ? o[q][i] : o[2 + q][i];
      }
#pragma unroll
      for (int i = 11; i < 16; ++i) { keep[q][i] = 0.f; send[q][i] = 0.f; }
#pragma unroll
      for (int i = 0; i < 4; ++i) qk0[q][i] = kh2 ? q0u[2 + q][i] : q0u[q][i];
      { unsigned a_ = qe[q], b_ = qe[2 + q]; asm volatile("" : "+v"(a_), "+v"(b_)); qke[q] = kh2 ? b_ : a_; }
    }
    WAIT_LGKM0();
    flag_set(smem, FL_DONE, w, lane);
    flag_wait_one(smem, FL_DONE, w ^ 1);
    {
      LDSP char* xp = smem + kh2 * KV_HALF + 7 * KV_GRP + rs * 4864;
#pragma unroll
      for (int q = 0; q < 2; ++q) {
        const f32x16 ov = send[q];
        *(LDSP f32x4*)(xp + q * 2432 + lane * 16) = f32x4{ov[0], ov[1], ov[2], ov[3]};
        *(LDSP f32x4*)(xp + q * 2432 + 1024 + lane * 16) = f32x4{ov[4], ov[5], ov[6], ov[7]};
        if (hh == 0) {
          *(LDSP float*)(xp + q * 2432 + 2048 + t32 * 4) = ov[8];
          *(LDSP float*)(xp + q * 2432 + 2176 + t32 * 4) = ov[9];
          *(LDSP float*)(xp + q * 2432 + 2304 + t32 * 4) = ov[10];
        }
      }
    }
    WAIT_LGKM0();
    flag_set(smem, FL_SENT, w, lane);
    flag_wait_one(smem, FL_SENT, w ^ 2);
    bool bad = false;
#pragma unroll
    for (int q = 0; q < 2; ++q) {
      LDSP char* xp = smem + (kh2 ^ 1) * KV_HALF + 7 * KV_GRP + rs * 4864 + q * 2432;
      f32x4 a0 = *(LDSP f32x4*)(xp + lane * 16), a1 = *(LDSP f32x4*)(xp + 1024 + lane * 16);
#pragma unroll
      for (int i = 0; i < 4; ++i) { keep[q][i] += a0[i]; keep[q][4 + i] += a1[i]; }
      keep[q][8] += *(LDSP float*)(xp + 2048 + t32 * 4);
      keep[q][9] += *(LDSP float*)(xp + 2176 + t32 * 4);
      keep[q][10] += *(LDSP float*)(xp + 2304 + t32 * 4);
      float mag = 0.f;
#pragma unroll
      for (int i = 0; i < 8; ++i) mag += __builtin_fabsf(keep[q][i]);
      const float ext = __builtin_fabsf(keep[q][8]) + __builtin_fabsf(keep[q][9]) + 1e12f * __builtin_fabsf(keep[q][10]);
      mag += hh ? 0.f : ext;
      bad = bad || !(mag < 1e30f);
    }
    if (__builtin_expect(__any(bad), 0)) {
#pragma unroll 1
      for (int q = 0; q < 2; ++q) {
        f32x16 ex = attn_exact(kvg, lane, q ? qk0[1] : qk0[0], q ? qke[1] : qke[0]);
        if (q) keep[1] = ex; else keep[0] = ex;
      }
    }
#pragma unroll
    for (int q = 0; q < 2; ++q) {
      float l = keep[q][10];
      const float lo_ = other_half(l);
      const float li = __builtin_amdgcn_rcpf(hh ? lo_ : l);
      const int head = head0 + 2 * kh2 + q;
      u32x4 o4 = {pack2(keep[q][0] * li, keep[q][1] * li), pack2(keep[q][2] * li, keep[q][3] * li),
                  pack2(keep[q][4] * li, keep[q][5] * li), pack2(keep[q][6] * li, keep[q][7] * li)};
      char* ap = ws + OFF_AO + (size_t)tt * 36 * 1024;
      store16(ap + head * 1024 + lane * 16, o4);
      if (hh == 0) {
        const int h8 = head & 7;
        *(unsigned*)(ap + (32 + (head >> 3)) * 1024 + (t32 + 32 * ((h8 >> 1) & 1)) * 16 + ((h8 >> 2) * 2 + (h8 & 1)) * 4) =
            pack2(keep[q][8] * li, keep[q][9] * li);
      }
    }
    STAMP(3);
  } else {
    __builtin_amdgcn_s_setprio(GEM_PRIO);
    const int g = w - 4;
    const int xcd = b & 7, idx = b >> 3;
    const int kh = xcd & 1, tp = (xcd >> 1) * 8 + (idx & 7), ng = idx >> 3;
    const int nt0 = (ng >> 1) * 9 + (ng & 1) * 5;
    const int nts = (ng >> 1) * 9 + 4;
    const int fb0 = kh * 144 + g * 36;
    const rsrc_t rsrc = __builtin_amdgcn_make_buffer_rsrc(ws, 0, (unsigned)OFF_WOT, 0x00020000);
    const unsigned hp0 = (unsigned)OFF_H + ((2 * tp) * 288 + fb0) * 1024;
    const unsigned wp = (unsigned)OFF_W2T + (fb0 * 18 + nt0) * 1024;
    const unsigned wsp = (unsigned)OFF_W2T + (fb0 * 18 + nts) * 1024;
    const unsigned lo16 = lane * 16;
    BAR();
    f32x16 acc[2][5];
#pragma unroll
    for (int a = 0; a < 2; ++a)
#pragma unroll
      for (int n = 0; n < 5; ++n)
#pragma unroll
        for (int i = 0; i < 16; ++i) acc[a][n][i] = 0.f;
    auto stage_kv = [&]() {
      __builtin_amdgcn_s_setprio(3);
#pragma unroll 1
      for (int i = g; i < 58; i += 4) {
        glds16(kvg + (20 + i) * 1024, lo16, smem + (20 + i) * 1024);
        glds16(kvg + KV_HALF + (20 + i) * 1024, lo16, smem + KV_HALF + (20 + i) * 1024);
      }
      __builtin_amdgcn_s_setprio(GEM_PRIO);
      STAMP(6);
    };
    auto kv_ready = [&]() {
      WAIT_VM(12);
      flag_set(smem, FL_KVG, g, lane);
      STAMP(1);
    };
    if (ng & 1) gemm2_all<4, 5>(rsrc, hp0, wp, wsp, lo16, acc, stage_kv, kv_ready);
    else        gemm2_all<5, 4>(rsrc, hp0, wp, wsp, lo16, acc, stage_kv, kv_ready);
    STAMP(2);
    {
      const int a = idx * 4 + g;
      unsigned sink = 0;
#pragma unroll
      for (int i = 0; i < 5; ++i) {
        const u32x4 v = *(const u32x4*)(ws + OFF_WOT + (size_t)min(a * 5 + i + (a >> 4), 647) * 1024 + lo16);
        sink ^= v[0];
      }
#pragma unroll
      for (int i = 0; i < 3; ++i) {
        const u32x4 v = *(const u32x4*)((const char*)x + (size_t)xcd * 294912 + (size_t)min(a * 3 + i, 287) * 1024 + lo16);
        sink ^= v[0];
      }
      asm volatile("" :: "v"(sink));
    }
    flag_wait_all(smem, FL_PROG);
    STAMP(3);
    LDSP char* mine   = smem + (g >> 1) * KV_HALF + (g & 1) * 24576 + lo16;
    LDSP char* theirs = smem + (g >> 1) * KV_HALF + ((g & 1) ^ 1) * 24576 + lo16;
    LDSP char* other  = smem + ((g >> 1) ^ 1) * KV_HALF + ((g & 1) ^ 1) * 24576 + lo16;
    auto reduce = [&](auto aa, auto hh_) {
      constexpr int A = decltype(aa)::value, HF = decltype(hh_)::value;
#pragma unroll
      for (int n = 0; n < 5; ++n)
#pragma unroll
        for (int q = 0; q < 4; ++q)
          *(LDSP f32x4*)(mine + (n * 4 + q) * 1024) =
              f32x4{acc[A ^ 1][n][4 * q], acc[A ^ 1][n][4 * q + 1], acc[A ^ 1][n][4 * q + 2], acc[A ^ 1][n][4 * q + 3]};
      WAIT_LGKM0();
      flag_set(smem, FL_GW, g, lane);
      flag_wait_one(smem, FL_GW, g ^ 1);
#pragma unroll
      for (int n = 0; n < 5; ++n)
#pragma unroll
        for (int q = 0; q < 4; ++q) {
          f32x4 v = *(LDSP f32x4*)(theirs + (n * 4 + q) * 1024);
#pragma unroll
          for (int i = 0; i < 4; ++i) acc[A][n][4 * q + i] += v[i];
        }
      WAIT_LGKM0();
#pragma unroll
      for (int n = 0; n < 5; ++n)
#pragma unroll
        for (int q = 0; q < 2; ++q) {
          constexpr int r0 = 8 * (HF ^ 1);
          *(LDSP f32x4*)(theirs + (n * 2 + q) * 1024) =
              f32x4{acc[A][n][r0 + 4 * q], acc[A][n][r0 + 4 * q + 1], acc[A][n][r0 + 4 * q + 2], acc[A][n][r0 + 4 * q + 3]};
        }
      WAIT_LGKM0();
      flag_set(smem, FL_GW2, g, lane);
      flag_wait_one(smem, FL_GW2, g ^ 2);
#pragma unroll
      for (int n = 0; n < 5; ++n) {
        const int nt = (n < 4) ? nt0 + n : nts;
        const int slot = (n < 4) ? kh : kh + 2 * (ng & 1);
        char* pp = ws + OFF_PART + (((size_t)slot * 64 + (2 * tp + A)) * 18 + nt) * 2048 + HF * 1024 + lo16;
        u32x4 o4;
#pragma unroll
        for (int q = 0; q < 2; ++q) {
          f32x4 v = *(LDSP f32x4*)(other + (n * 2 + q) * 1024);
#pragma unroll
          for (int i = 0; i < 4; ++i) v[i] += acc[A][n][8 * HF + 4 * q + i];
          o4[2 * q] = pack2(v[0], v[1]);
          o4[2 * q + 1] = pack2(v[2], v[3]);
        }
        store16(pp, o4);
      }
    };
    using I0 = std::integral_constant<int, 0>;
    using I1 = std::integral_constant<int, 1>;
    if (g == 0) reduce(I0{}, I0{});
    else if (g == 1) reduce(I1{}, I0{});
    else if (g == 2) reduce(I0{}, I1{});
    else reduce(I1{}, I1{});
  }
  STAMP(4);
  STAMP(5);
  TS_FLUSH(1);
}

constexpr int K3_LDS = 6 * 6 * 4096;
__global__ __launch_bounds__(384)
void k3(const u16* __restrict__ x, const u16* __restrict__ b2, char* __restrict__ ws, u16* __restrict__ out) {
  TS_DECL; STAMP(0);
  extern __shared__ __attribute__((aligned(16))) char smem_[];
  LDSP char* smem = (LDSP char*)smem_;
  const int tid = threadIdx.x, lane = tid & 63;
  const int w = __builtin_amdgcn_readfirstlane(tid >> 6);
  const int b = blockIdx.x;
  const int tp = (b & 7) * 4 + (b >> 3) / 6, ng = (b >> 3) % 6;
  const int t32 = lane & 31, hh = lane >> 5;
  const int tt = 2 * tp + w / 3, nt = 3 * ng + w % 3;
  const int token = tt * 32 + t32;
  const char* ap = ws + OFF_AO + ((size_t)(2 * tp) * 36 + w * 6) * 1024 + lane * 16;
  const char* wp = ws + OFF_WOT + ((size_t)(w * 6) * 18 + 3 * ng) * 1024 + lane * 16;
  const bool shared = (nt == 4 || nt == 13);
  const size_t SL = (size_t)64 * 18 * 2048;
  const char* pp = ws + OFF_PART + ((size_t)tt * 18 + nt) * 2048 + lane * 16;
  const char* pp2 = shared ? pp + 2 * SL : pp;
  s16x8 af[2][6], wf[3][6];
  u32x4 pt[4][2];
  u32x2 xv[4], bv[4];
#pragma unroll
  for (int h = 0; h < 2; ++h) {
    pt[0][h] = *(const u32x4*)(pp + h * 1024);
    pt[1][h] = *(const u32x4*)(pp + SL + h * 1024);
    pt[2][h] = *(const u32x4*)(pp2 + h * 1024);
    pt[3][h] = *(const u32x4*)(pp2 + SL + h * 1024);
  }
#pragma unroll
  for (int q = 0; q < 4; ++q) {
    const int col = nt * 32 + 8 * q + 4 * hh;
    xv[q] = *(const u32x2*)((const char*)x + (size_t)token * 1152 + col * 2);
    bv[q] = *(const u32x2*)((const char*)b2 + col * 2);
  }
  SCHED();
#pragma unroll
  for (int k = 0; k < 6; ++k) {
    af[0][k] = *(const s16x8*)(ap + k * 1024);
    wf[0][k] = *(const s16x8*)(wp + k * 18432);
    af[1][k] = *(const s16x8*)(ap + 36 * 1024 + k * 1024);
    wf[1][k] = *(const s16x8*)(wp + k * 18432 + 1024);
    wf[2][k] = *(const s16x8*)(wp + k * 18432 + 2048);
  }
  SCHED();
  f32x16 acc[2][3];
#pragma unroll
  for (int i = 0; i < 2; ++i)
#pragma unroll
    for (int jn = 0; jn < 3; ++jn)
#pragma unroll
      for (int r = 0; r < 16; ++r) acc[i][jn][r] = 0.f;
#pragma unroll
  for (int k = 0; k < 6; ++k)
#pragma unroll
    for (int i = 0; i < 2; ++i)
#pragma unroll
      for (int jn = 0; jn < 3; ++jn) acc[i][jn] = MFMA32(wf[jn][k], af[i][k], acc[i][jn]);
#pragma unroll
  for (int i = 0; i < 2; ++i)
#pragma unroll
    for (int jn = 0; jn < 3; ++jn)
#pragma unroll
      for (int q = 0; q < 4; ++q) {
        f32x4 v = {acc[i][jn][4 * q], acc[i][jn][4 * q + 1], acc[i][jn][4 * q + 2], acc[i][jn][4 * q + 3]};
        *(LDSP f32x4*)(smem + ((w * 6 + i * 3 + jn) * 4 + q) * 1024 + lane * 16) = v;
      }
  const float sh = shared ? 1.f : 0.f;
  f32x4 fin[4];
#pragma unroll
  for (int q = 0; q < 4; ++q) {
#pragma unroll
    for (int e = 0; e < 2; ++e) {
      const unsigned p0 = pt[0][q >> 1][(q & 1) * 2 + e], p1 = pt[1][q >> 1][(q & 1) * 2 + e];
      const unsigned p2 = pt[2][q >> 1][(q & 1) * 2 + e], p3 = pt[3][q >> 1][(q & 1) * 2 + e];
      const float lo = (__builtin_bit_cast(float, p0 << 16) + __builtin_bit_cast(float, p1 << 16)) +
                       sh * (__builtin_bit_cast(float, p2 << 16) + __builtin_bit_cast(float, p3 << 16));
      const float hi = (__builtin_bit_cast(float, p0 & 0xffff0000u) + __builtin_bit_cast(float, p1 & 0xffff0000u)) +
                       sh * (__builtin_bit_cast(float, p2 & 0xffff0000u) + __builtin_bit_cast(float, p3 & 0xffff0000u));
      fin[q][2 * e]     = lo + __builtin_bit_cast(float, xv[q][e] << 16) + __builtin_bit_cast(float, bv[q][e] << 16);
      fin[q][2 * e + 1] = hi + __builtin_bit_cast(float, xv[q][e] & 0xffff0000u) + __builtin_bit_cast(float, bv[q][e] & 0xffff0000u);
    }
  }
  STAMP(1);
  __syncthreads();
#pragma unroll
  for (int q = 0; q < 4; ++q) {
    f32x4 v = fin[q];
#pragma unroll
    for (int o = 0; o < 6; ++o) v += *(LDSP f32x4*)(smem + ((o * 6 + w) * 4 + q) * 1024 + lane * 16);
    u32x2 ov = {pack2(v[0], v[1]), pack2(v[2], v[3])};
    *(u32x2*)((char*)out + (size_t)token * 1152 + (nt * 32 + 8 * q + 4 * hh) * 2) = ov;
  }
  STAMP(2);
  STAMP(3);
  TS_FLUSH(2);
}

extern "C" void kernel_launch(void* const* d_in, const int* in_sizes, int n_in,
                              void* d_out, int out_size, void* d_ws, size_t ws_size,
                              hipStream_t stream) {
  const u16* x  = (const u16*)d_in[0];
  const u16* wq = (const u16*)d_in[1];
  const u16* wk = (const u16*)d_in[2];
  const u16* wv = (const u16*)d_in[3];
  const u16* wo = (const u16*)d_in[4];
  const u16* w1 = (const u16*)d_in[6];
  const u16* b1 = (const u16*)d_in[7];
  const u16* w2 = (const u16*)d_in[8];
  const u16* b2 = (const u16*)d_in[9];
  char* ws = (char*)d_ws;
  (void)hipFuncSetAttribute((const void*)k1, hipFuncAttributeMaxDynamicSharedMemorySize, K1_LDS);
  (void)hipFuncSetAttribute((const void*)k2, hipFuncAttributeMaxDynamicSharedMemorySize, K2_LDS);
  (void)hipFuncSetAttribute((const void*)k3, hipFuncAttributeMaxDynamicSharedMemorySize, K3_LDS);
  k1<<<256, 512, K1_LDS, stream>>>(x, wq, wk, wv, wo, w1, b1, w2, ws);
  k2<<<256, 512, K2_LDS, stream>>>(x, ws);
  k3<<<192, 384, K3_LDS, stream>>>(x, b2, ws, (u16*)d_out);
}
```

```cpp
#include <hip/hip_runtime.h>
#include <hip/hip_bf16.h>
#include <stdint.h>
#include <type_traits>

#define DI __device__ __forceinline__
#define LDSP __attribute__((address_space(3)))
#define GLP __attribute__((address_space(1)))

typedef unsigned short u16;
typedef __attribute__((ext_vector_type(8))) short s16x8;
typedef __attribute__((ext_vector_type(4))) short s16x4;
typedef __attribute__((ext_vector_type(16))) float f32x16;
typedef __attribute__((ext_vector_type(4))) float f32x4;
typedef __attribute__((ext_vector_type(2))) float f32x2;
typedef __attribute__((ext_vector_type(2))) __bf16 bf16x2;
typedef __attribute__((ext_vector_type(4))) unsigned u32x4;
typedef __attribute__((ext_vector_type(2))) unsigned u32x2;

constexpr size_t OFF_H    = 0;
constexpr size_t OFF_W2T  = OFF_H + 18874368;
constexpr size_t OFF_WOT  = OFF_W2T + 5308416;
constexpr size_t OFF_Q    = OFF_WOT + 663552;
constexpr size_t OFF_KV   = OFF_Q + 2359296;
constexpr size_t OFF_AO   = OFF_KV + 159744;
constexpr size_t OFF_PART = OFF_AO + 2359296;
constexpr size_t OFF_RED  = OFF_PART + 18874368;
constexpr int KV_GRP = 9984, KV_TILE = 2240, KV_BYTES = 159744, KV_HALF = 79872;

#define MFMA32(a, b, c) __builtin_amdgcn_mfma_f32_32x32x16_bf16((a), (b), (c), 0, 0, 0)
#define WAIT_VM(n) asm volatile("s_waitcnt vmcnt(" #n ")" ::: "memory")
#define WAIT_LGKM0() asm volatile("s_waitcnt lgkmcnt(0)" ::: "memory")
#define BAR() __builtin_amdgcn_s_barrier()
#define TS_DECL
#define STAMP(i)
#define TS_FLUSH(kid)

DI unsigned pack2(float a, float b) {
  f32x2 v = {a, b};
  return __builtin_bit_cast(unsigned, __builtin_convertvector(v, bf16x2));
}
DI float bf2f(u16 v) { return __builtin_bit_cast(float, (unsigned)v << 16); }
DI float dot2(unsigned a, unsigned b, float c) {
  return __builtin_amdgcn_fdot2_f32_bf16(__builtin_bit_cast(bf16x2, a), __builtin_bit_cast(bf16x2, b), c, false);
}
DI void glds16(const void* gbase, unsigned voff, LDSP char* l) {
  unsigned keep;
  asm volatile("s_mov_b32 %0, m0\n\ts_mov_b32 m0, %3\n\ts_nop 0\n\tglobal_load_lds_dwordx4 %1, %2\n\ts_mov_b32 m0, %0"
               : "=&s"(keep) : "v"(voff), "s"(gbase), "s"((unsigned)(size_t)l) : "memory");
}
#define IN_POL ""
DI void glds16_in(const void* gbase, unsigned voff, LDSP char* l) {
  unsigned keep;
  asm volatile("s_mov_b32 %0, m0\n\ts_mov_b32 m0, %3\n\ts_nop 0\n\tglobal_load_lds_dwordx4 %1, %2" IN_POL "\n\ts_mov_b32 m0, %0"
               : "=&s"(keep) : "v"(voff), "s"(gbase), "s"((unsigned)(size_t)l) : "memory");
}
DI void glds4(const void* gbase, unsigned voff, LDSP char* l) {
  unsigned keep;
  asm volatile("s_mov_b32 %0, m0\n\ts_mov_b32 m0, %3\n\ts_nop 0\n\tglobal_load_lds_dword %1, %2\n\ts_mov_b32 m0, %0"
               : "=&s"(keep) : "v"(voff), "s"(gbase), "s"((unsigned)(size_t)l) : "memory");
}
#define ST_FLAVOR 1
DI void store16(void* p, u32x4 v) {
  asm volatile("global_store_dwordx4 %0, %1, off sc1\n\ts_nop 1" :: "v"(p), "v"(v) : "memory");
}
DI s16x8 tr_frag(LDSP char* p, int second_off) {
  s16x4 lo = __builtin_amdgcn_ds_read_tr16_b64_v4i16((LDSP s16x4*)p);
  s16x4 hi = __builtin_amdgcn_ds_read_tr16_b64_v4i16((LDSP s16x4*)(p + second_off));
  return __builtin_shufflevector(lo, hi, 0, 1, 2, 3, 4, 5, 6, 7);
}
DI float other_half(float v) {
  unsigned u = __builtin_bit_cast(unsigned, v);
  auto r = __builtin_amdgcn_permlane32_swap(u, u, false, false);
  unsigned o = (threadIdx.x & 32) ? r[0] : r[1];
  return __builtin_bit_cast(float, o);
}
DI float gelu_tanh(float z) {
  const float c0 = -2.302208198f;
  const float c1 = -0.1029432397f;
  float t = z * z;
  float p = __builtin_fmaf(t, c1, c0);
  float e = __builtin_amdgcn_exp2f(z * p);
  return z * __builtin_amdgcn_rcpf(1.0f + e);
}

constexpr int K1_SLOT = 36864;
constexpr int K1_BIAS = 4 * K1_SLOT;
constexpr int K1_CS   = K1_BIAS + 1152;
constexpr int K1_CSQ  = K1_CS + 1152;
constexpr int K1_STAT = K1_CSQ + 1024;
constexpr int K1_PF   = K1_STAT + 1024;
constexpr int K1_LDS  = K1_PF + 256;

__host__ __device__ constexpr int w_slot(int st) { return (0x231023120ll >> (4 * st)) & 3; }

#define SCHED() __builtin_amdgcn_sched_barrier(0)
#define SGB(mask, n) __builtin_amdgcn_sched_group_barrier((mask), (n), 0)

DI u32x2 gelu4(float a0, float a1, float a2, float a3, const f32x4& cs, const f32x4& bb, float negm, float r, float c0) {
  float z0, z1, z2, z3, t0, t1, t2, t3;
  unsigned o0, o1;
  asm volatile(
      "v_fma_f32 %2, %18, %14, %10\n\tv_fma_f32 %3, %18, %15, %11\n\tv_fma_f32 %4, %18, %16, %12\n\tv_fma_f32 %5, %18, %17, %13\n\t"
      "v_fma_f32 %2, %19, %2, %21\n\tv_fma_f32 %3, %19, %3, %22\n\tv_fma_f32 %4, %19, %4, %23\n\tv_fma_f32 %5, %19, %5, %24\n\t"
      "v_mul_f32 %6, %2, %2\n\tv_mul_f32 %7, %3, %3\n\tv_mul_f32 %8, %4, %4\n\tv_mul_f32 %9, %5, %5\n\t"
      "v_fmamk_f32 %6, %6, 0xbdd2d3e8, %20\n\tv_fmamk_f32 %7, %7, 0xbdd2d3e8, %20\n\tv_fmamk_f32 %8, %8, 0xbdd2d3e8, %20\n\tv_fmamk_f32 %9, %9, 0xbdd2d3e8, %20\n\t"
      "v_mul_f32 %6, %2, %6\n\tv_mul_f32 %7, %3, %7\n\tv_mul_f32 %8, %4, %8\n\tv_mul_f32 %9, %5, %9\n\t"
      "v_exp_f32 %6, %6\n\tv_exp_f32 %7, %7\n\tv_exp_f32 %8, %8\n\tv_exp_f32 %9, %9\n\t"
      "v_add_f32 %6, 1.0, %6\n\tv_add_f32 %7, 1.0, %7\n\tv_add_f32 %8, 1.0, %8\n\tv_add_f32 %9, 1.0, %9\n\t"
      "v_rcp_f32 %6, %6\n\tv_rcp_f32 %7, %7\n\tv_rcp_f32 %8, %8\n\tv_rcp_f32 %9, %9\n\t"
      "v_mul_f32 %2, %2, %6\n\tv_mul_f32 %3, %3, %7\n\tv_mul_f32 %4, %4, %8\n\tv_mul_f32 %5, %5, %9\n\t"
      "v_cvt_pk_bf16_f32 %0, %2, %3\n\tv_cvt_pk_bf16_f32 %1, %4, %5"
      : "=&v"(o0), "=&v"(o1), "=&v"(z0), "=&v"(z1), "=&v"(z2), "=&v"(z3), "=&v"(t0), "=&v"(t1), "=&v"(t2), "=&v"(t3)
      : "v"(a0), "v"(a1), "v"(a2), "v"(a3), "v"(cs[0]), "v"(cs[1]), "v"(cs[2]), "v"(cs[3]), "v"(negm), "v"(r), "v"(c0),
        "v"(bb[0]), "v"(bb[1]), "v"(bb[2]), "v"(bb[3]));
  u32x2 o = {o0, o1};
  return o;
}

template <typename F>
DI void gelu_tile(const f32x16& a, LDSP char* cs_p, LDSP char* bias_p, char* out_p, float negm, float r, F&& between) {
  const float c0 = -2.302208198f;
#pragma unroll
  for (int hf = 0; hf < 2; ++hf) {
    f32x4 cs0 = *(LDSP f32x4*)(cs_p + hf * 64), cs1 = *(LDSP f32x4*)(cs_p + hf * 64 + 32);
    f32x4 bb0 = *(LDSP f32x4*)(bias_p + hf * 64), bb1 = *(LDSP f32x4*)(bias_p + hf * 64 + 32);
    between(2 * hf);
    u32x2 lo = gelu4(a[8 * hf], a[8 * hf + 1], a[8 * hf + 2], a[8 * hf + 3], cs0, bb0, negm, r, c0);
    between(2 * hf + 1);
    u32x2 hi = gelu4(a[8 * hf + 4], a[8 * hf + 5], a[8 * hf + 6], a[8 * hf + 7], cs1, bb1, negm, r, c0);
    u32x4 o = {lo[0], lo[1], hi[0], hi[1]};
    store16(out_p + hf * 1024, o);
  }
}

DI void colsum_frag(const s16x8& a, const s16x8& sel, f32x4& cs) {
  cs = __builtin_amdgcn_mfma_f32_16x16x32_bf16(sel, a, cs, 0, 0, 0);
}

template <int KT>
DI void w1_stage(LDSP char* sb0, LDSP char* sb1, LDSP char* sb2, const s16x8 (&xf)[36], f32x16 (&acc)[3], const s16x8& sel, f32x4& cs) {
  asm volatile("" : "+v"(sb0), "+v"(sb1), "+v"(sb2));
  s16x8 a[2][3];
  a[0][0] = tr_frag(sb0, 768); a[0][1] = tr_frag(sb1, 768); a[0][2] = tr_frag(sb2, 768);
#pragma unroll
  for (int ks = 0; ks < 12; ++ks) {
    const int cur = ks & 1;
    if (ks + 1 < 12) {
      a[cur ^ 1][0] = tr_frag(sb0 + (ks + 1) * 3072, 768);
      a[cur ^ 1][1] = tr_frag(sb1 + (ks + 1) * 3072, 768);
      a[cur ^ 1][2] = tr_frag(sb2 + (ks + 1) * 3072, 768);
    }
#pragma unroll
    for (int t = 0; t < 3; ++t) acc[t] = MFMA32(a[cur][t], xf[12 * KT + ks], acc[t]);
    colsum_frag(a[cur][0], sel, cs);
    SGB(0x100, 2); SGB(0x8, 1); SGB(0x100, 2); SGB(0x8, 1); SGB(0x100, 2); SGB(0x8, 2);
    SCHED();
  }
}

DI void qkv_stage(LDSP char* sb, const s16x8 (&xf)[36], f32x16& qa, const s16x8& sel, f32x4& cs) {
  asm volatile("" : "+v"(sb));
  s16x8 a[3];
  a[0] = tr_frag(sb, 256);
  a[1] = tr_frag(sb + 1024, 256);
#pragma unroll
  for (int s = 0; s < 36; ++s) {
    if (s + 2 < 36) a[(s + 2) % 3] = tr_frag(sb + (s + 2) * 1024, 256);
    qa = MFMA32(a[s % 3], xf[s], qa);
    colsum_frag(a[s % 3], sel, cs);
    SCHED();
  }
}

__global__ __launch_bounds__(512, 2)
void k1(const u16* __restrict__ x, const u16* __restrict__ wq, const u16* __restrict__ wk, const u16* __restrict__ wv,
        const u16* __restrict__ wo, const u16* __restrict__ w1, const u16* __restrict__ b1, const u16* __restrict__ w2,
        char* __restrict__ ws) {
  extern __shared__ __attribute__((aligned(16))) char smem_[];
  LDSP char* smem = (LDSP char*)smem_;
  const int tid = threadIdx.x, lane = tid & 63;
  const int w8 = __builtin_amdgcn_readfirstlane(tid >> 6);
  const int w = w8 & 3, grp = w8 >> 2;
  const int b = blockIdx.x, xcd = b & 7, idx = b >> 3;
#define K1MAP 0
  const int tg = (xcd >> 1) * 4 + (idx & 3), j = (xcd & 1) * 8 + (idx >> 2);
  const int t32 = lane & 31, hh = lane >> 5;
  const int tt = tg * 4 + w;
  const bool heavy = (j & 3) == 0;
  const int rk = 0, rc = 0;
  TS_DECL; STAMP(0);

  const char* w1w = (const char*)w1 + (size_t)(48 * w) * 9216 + (size_t)j * 576;
  unsigned off3[3];
#pragma unroll
  for (int i = 0; i < 3; ++i) {
    const unsigned bb = 1024 * i + 16 * lane;
    off3[i] = (bb / 192) * 9216 + (bb % 192);
  }
  auto issue_w1_rows = [&](int st, int r) {
    const int c = st / 3, kt = st - 3 * c;
    const char* g = w1w + ((c + rc) % 3) * 192 + (size_t)((kt + rk) % 3) * 192 * 9216 + r * 16 * 9216;
    LDSP char* l = smem + w_slot(st) * K1_SLOT + w * 9216 + r * 3072;
#pragma unroll
    for (int i = 0; i < 3; ++i) glds16_in(g, off3[i], l + i * 1024);
  };
  auto issue_w1 = [&](int st) {
#pragma unroll
    for (int r = 0; r < 3; ++r) issue_w1_rows(st, r);
  };
  auto issue_x = [&](int q, int slot) {
    int ln = tid & 63; asm volatile("" : "+v"(ln));
    const char* g = (const char*)x + (size_t)(tt * 32) * 1152;
    LDSP char* l = smem + slot * K1_SLOT + w * 9216;
    unsigned row = (unsigned)ln / 18, pos = (unsigned)ln % 18;
    const unsigned b0 = q * 288 + rk * 384;
#pragma unroll
    for (int i = 0; i < 9; ++i) {
      unsigned cb = b0 + ((pos ^ ((row >> 3) & 1)) << 4);
      cb = cb >= 1152 ? cb - 1152 : cb;
      glds16(g, row * 1152 + cb, l + i * 1024);
      pos += 10; row += 3;
      if (pos >= 18) { pos -= 18; row += 1; }
    }
  };
  auto issue_qkv = [&](int tile, int slot, int part) {
    int ln = tid & 63; asm volatile("" : "+v"(ln));
    LDSP char* l = smem + slot * K1_SLOT + w * 9216 + part * 3072;
    const int row0 = 144 * w + 48 * part + 192 * rk;
    if (tile < 18) {
      const char* g = (const char*)wq + tile * 64;
      const unsigned vo = (ln >> 2) * 1152 + (ln & 3) * 16;
#pragma unroll
      for (int i = 0; i < 3; ++i) glds16_in(g + (size_t)((row0 + 16 * i) % 576) * 1152, vo, l + i * 1024);
    } else {
      const u16* src = (tile == 18) ? wk : wv;
      int dw = ln & 15; dw = dw > 8 ? 8 : dw;
      const unsigned vo = (ln >> 4) * 36 + dw * 4;
#pragma unroll
      for (int i = 0; i < 12; ++i) glds4((const char*)src + ((row0 + 4 * i) % 576) * 36, vo, l + i * 256);
    }
  };
  const int lw = (tg * 12 + (j - (j >> 2) - 1)) * 4 + w;
  auto issue_unit = [&](int part) {
    if (lw < 648) {
      int ln = tid & 63; asm volatile("" : "+v"(ln));
      LDSP char* l = smem + 0 * K1_SLOT + w * 9216 + part * 3072;
      const int u = lw - 576, ks = u >> 1;
      const bool plain = ks < 32;
      const char* g = lw < 576 ? (const char*)w2 + (size_t)((lw >> 1) * 16) * 1152 + (lw & 1) * 576
                               : (const char*)wo + (size_t)(plain ? ks * 18 : 144 * (ks - 32) + 16) * 1152 + (u & 1) * 576;
      const unsigned p0 = 192 * part + ln;
      unsigned row = p0 / 36, pos = p0 % 36;
#pragma unroll
      for (int i = 0; i < 3; ++i) {
        const unsigned srow = plain ? row : 18 * (row >> 1) + (row & 1);
        glds16_in(g, srow * 1152 + pos * 16, l + i * 1024);
        pos += 28; row += 1;
        if (pos >= 36) { pos -= 36; row += 1; }
      }
    }
  };

  unsigned bias_raw = 0, bias_raw2 = 0;
  if (grp == 1) {
    asm volatile("global_load_ushort %0, %2, %4\n\tglobal_load_ushort %1, %3, %4" : "=&v"(bias_raw), "=&v"(bias_raw2)
                 : "v"((unsigned)((tid - 256) * 2)), "v"((unsigned)((tid < 288 ? tid : 287) * 2)), "s"(b1 + j * 288) : "memory");
  }
  if (grp == 0) { issue_x(0, 3); issue_x(1, 1); }
  else          { issue_w1(0);   issue_w1(1); }
#define TLBW 0
#define TW 0
  s16x8 xf[36];
  const int xl = w * 9216 + t32 * 288 + ((hh ^ ((t32 >> 3) & 1)) << 4);
  auto read_x = [&](int q, int slot) {
#pragma unroll
    for (int s = 0; s < 9; ++s) xf[9 * q + s] = *(LDSP s16x8*)(smem + slot * K1_SLOT + xl + s * 32);
  };
  if (grp == 0) WAIT_VM(9);
  BAR();
  read_x(0, 3);
  if (grp == 0) { WAIT_VM(0); }
  else {
    asm volatile("s_waitcnt vmcnt(9)" : "+v"(bias_raw), "+v"(bias_raw2) :: "memory");
    *(LDSP float*)(smem + K1_BIAS + (tid - 256) * 4) = __builtin_bit_cast(float, bias_raw << 16);
    if (tid < 288) *(LDSP float*)(smem + K1_BIAS + tid * 4) = __builtin_bit_cast(float, bias_raw2 << 16);
  }
  WAIT_LGKM0(); BAR();
  STAMP(1);
  read_x(1, 1);
  if (grp == 1) issue_x(2, 3);
  WAIT_LGKM0(); BAR();
  STAMP(2);

  const int q4 = (lane & 15) >> 2, p4 = lane & 3, g16 = (lane >> 4) & 1;
  const int rot = (w == 3) ? 0 : w;
  const int tl0 = rot, tl1 = (rot + 1) % 3, tl2 = (rot + 2) % 3;
  const int lo_w1 = (8 * hh + q4) * 192 + 32 * g16 + 8 * p4;
  const int lo_q  = (8 * hh + q4) * 64 + 32 * g16 + 8 * p4;
  char* hout = ws + OFF_H + ((size_t)tt * 288 + (size_t)j * 18) * 1024 + lane * 16;
  f32x16 acc[3];
  float negm = 0.f, r1 = 0.f, rm = 0.f;
  const unsigned selv = ((lane & 15) == ((lane >> 4) & 1)) ? 0x3f803f80u : 0u;
  const u32x4 selu = {selv, selv, selv, selv};
  const s16x8 sel = __builtin_bit_cast(s16x8, selu);

  auto load_stats = [&]() {
    f32x2 st = *(LDSP f32x2*)(smem + K1_STAT + (w * 32 + t32) * 8);
    negm = -st[0] * (1.0f / 576.0f);
    const float var = __builtin_fmaxf(st[1] * (1.0f / 576.0f) - negm * negm, 0.f);
    r1 = __builtin_amdgcn_rsqf(var + 1e-5f);
    rm = r1 * __builtin_amdgcn_rsqf(var * r1 * r1 + 1e-5f);
  };
#define HELP_PRIO 1
  auto mfma_chunk = [&](auto cc) {
    constexpr int c = decltype(cc)::value;
    __builtin_amdgcn_s_setprio(0);
    f32x4 cs = {0.f, 0.f, 0.f, 0.f};
#pragma unroll
    for (int t = 0; t < 3; ++t)
#pragma unroll
      for (int i = 0; i < 16; ++i) acc[t][i] = 0.f;
    LDSP char* s0 = smem + lo_w1 + tl0 * 64;
    LDSP char* s1 = smem + lo_w1 + tl1 * 64;
    LDSP char* s2 = smem + lo_w1 + tl2 * 64;
    constexpr int o0 = w_slot(3 * c) * K1_SLOT, o1 = w_slot(3 * c + 1) * K1_SLOT, o2 = w_slot(3 * c + 2) * K1_SLOT;
    w1_stage<0>(s0 + o0, s1 + o0, s2 + o0, xf, acc, sel, cs);
    if (c == 1) { WAIT_VM(9); } else if (c == 2) { WAIT_VM(11); }
    WAIT_LGKM0(); BAR();
    if (c == 0) { read_x(2, 3); WAIT_LGKM0(); BAR(); }
    w1_stage<1>(s0 + o1, s1 + o1, s2 + o1, xf, acc, sel, cs);
    if (c != 0) WAIT_VM(0);
    WAIT_LGKM0(); BAR();
    if (c == 0) { read_x(3, 0); WAIT_LGKM0(); BAR(); }
    w1_stage<2>(s0 + o2, s1 + o2, s2 + o2, xf, acc, sel, cs);
    if (lane < 16 && w < 3) {
      *(LDSP float*)(smem + K1_CS + (c * 96 + tl0 * 32 + lane) * 4) = cs[0];
      *(LDSP float*)(smem + K1_CS + (c * 96 + tl0 * 32 + 16 + lane) * 4) = cs[1];
    }
    WAIT_LGKM0(); BAR();
    __builtin_amdgcn_s_setprio(HELP_PRIO);
  };
  auto gelu_t = [&](int c, int t, int tl, int st_issue) {
    const int ca = (c + rc) % 3;
    gelu_tile(acc[t], smem + K1_CS + (c * 96 + tl * 32) * 4 + hh * 16, smem + K1_BIAS + (ca * 96 + tl * 32) * 4 + hh * 16,
              hout + (ca * 6 + tl * 2) * 1024, negm, rm, [&](int i) {
      if (i < 3) {
        if (st_issue >= 0 && st_issue < 9) issue_w1_rows(st_issue, i);
        else if (st_issue == 9) issue_qkv(j, 1, i);
        else if (st_issue == 10) { if (heavy) issue_qkv(16 + (j >> 2), 0, i); else issue_unit(i); }
      }
    });
  };

  if (grp == 0) {
    mfma_chunk(std::integral_constant<int, 0>{});
    STAMP(3);
    load_stats();
    gelu_t(0, 0, tl0, 6); WAIT_LGKM0(); BAR();
    gelu_t(0, 1, tl1, 7); WAIT_LGKM0(); BAR();
    gelu_t(0, 2, tl2, 8); WAIT_VM(22); WAIT_LGKM0(); BAR();
    STAMP(4);
    mfma_chunk(std::integral_constant<int, 2>{});
    STAMP(5);
    gelu_t(2, 0, tl0, -1); gelu_t(2, 1, tl1, -1); gelu_t(2, 2, tl2, -1);
    STAMP(6);
  } else {
    f32x4 rs = {0.f, 0.f, 0.f, 0.f};
#pragma unroll
    for (int i = 0; i < 16; ++i) acc[0][i] = 0.f;
    auto stats_frag = [&](int s) {
      colsum_frag(xf[s], sel, rs);
      acc[0] = MFMA32(xf[s], xf[s], acc[0]);
    };
    __builtin_amdgcn_s_setprio(HELP_PRIO);
#pragma unroll
    for (int s = 0; s < 18; ++s) { if (s % 6 == 0) issue_w1_rows(2, s / 6); stats_frag(s); }
    WAIT_VM(9); BAR();
    issue_x(3, 0);
    read_x(2, 3); WAIT_LGKM0(); BAR();
#pragma unroll
    for (int s = 18; s < 27; ++s) { if (s % 3 == 0) issue_w1_rows(3, (s - 18) / 3); stats_frag(s); }
    WAIT_VM(9); BAR();
    issue_w1(4);
    read_x(3, 0); WAIT_LGKM0(); BAR();
#pragma unroll
    for (int s = 27; s < 36; ++s) { if (s % 3 == 0) issue_w1_rows(5, (s - 27) / 3); stats_frag(s); }
    {
      const int dreg = (t32 & 3) + 4 * (t32 >> 3);
      float s2 = 0.f;
#pragma unroll
      for (int i = 0; i < 16; ++i) s2 = (i == dreg) ? acc[0][i] : s2;
      if (hh == ((t32 >> 2) & 1)) *(LDSP float*)(smem + K1_STAT + (w * 32 + t32) * 8 + 4) = s2;
      if (lane < 16) {
        *(LDSP float*)(smem + K1_STAT + (w * 32 + lane) * 8) = rs[0];
        *(LDSP float*)(smem + K1_STAT + (w * 32 + 16 + lane) * 8) = rs[1];
      }
    }
    WAIT_VM(18); WAIT_LGKM0(); BAR();
    STAMP(3);
    mfma_chunk(std::integral_constant<int, 1>{});
    STAMP(4);
    load_stats();
    gelu_t(1, 0, tl0, 10); WAIT_LGKM0(); BAR();
    gelu_t(1, 1, tl1, 9); WAIT_LGKM0(); BAR();
    gelu_t(1, 2, tl2, -1);
    WAIT_VM(2); WAIT_LGKM0(); BAR();
#define TAIL_PRIO 2
    __builtin_amdgcn_s_setprio(TAIL_PRIO);
    STAMP(5);

    auto qkv_tile = [&](int tile, int slot) {
      f32x16 qa;
#pragma unroll
      for (int i = 0; i < 16; ++i) qa[i] = 0.f;
      f32x4 cs = {0.f, 0.f, 0.f, 0.f};
      qkv_stage(smem + slot * K1_SLOT + lo_q, xf, qa, sel, cs);
      int ln = tid & 63; asm volatile("" : "+v"(ln));
      const int t32 = ln & 31, hh = ln >> 5, token = tt * 32 + t32;
      LDSP char* cp = smem + K1_CSQ + (w * 2 + (slot & 1)) * 128;
      if (ln < 16) { *(LDSP float*)(cp + ln * 4) = cs[0]; *(LDSP float*)(cp + 64 + ln * 4) = cs[1]; }
      WAIT_LGKM0();
#pragma unroll
      for (int g = 0; g < 4; ++g) {
        f32x4 c4 = *(LDSP f32x4*)(cp + g * 32 + hh * 16);
#pragma unroll
        for (int i = 0; i < 4; ++i) qa[4 * g + i] = __builtin_fmaf(negm, c4[i], qa[4 * g + i]);
      }
      if (tile < 18) {
        const float sc = r1 * (0.23570226039551584f * 1.4426950408889634f);
        char* qp = ws + OFF_Q + (size_t)tt * 36864;
#pragma unroll
        for (int i = 0; i < 8; ++i) {
          const unsigned n = tile * 32 + 8 * (i >> 1) + 4 * hh + 2 * (i & 1);
          const unsigned head = n / 18, d = n - head * 18;
          const unsigned off = d < 16 ? (t32 + 32 * ((d >> 2) & 1)) * 16 + ((d >> 3) * 2 + ((d >> 1) & 1)) * 4 : 1024 + t32 * 4;
          *(unsigned*)(qp + head * 1152 + off) = pack2(qa[2 * i] * sc, qa[2 * i + 1] * sc);
        }
      } else if (tile == 18) {
        u32x4 o = {pack2(qa[0] * r1, qa[1] * r1), pack2(qa[2] * r1, qa[3] * r1),
                   pack2(qa[4] * r1, qa[5] * r1), pack2(qa[6] * r1, qa[7] * r1)};
        char* gp = ws + OFF_KV + (size_t)tg * KV_GRP;
        *(u32x4*)(gp + 1024 + w * KV_TILE + ln * 16) = o;
        if (hh == 0) *(unsigned*)(gp + (w >> 1) * 512 + t32 * 16 + (w & 1) * 4) = pack2(qa[8] * r1, qa[9] * r1);
        else if ((w & 1) == 0) {
          u32x2 cst = {w == 0 ? 0x3f803f80u : 0u, 0u};
          *(u32x2*)(gp + (w >> 1) * 512 + t32 * 16 + 8) = cst;
        }
      } else {
        const int s = t32 >> 4, hk = (t32 >> 2) & 1, jj = ((t32 >> 3) & 1) * 4 + (t32 & 3);
        char* vp = ws + OFF_KV + (size_t)tg * KV_GRP + 2048 + w * KV_TILE + (s * 2 + hk) * 304 + jj * 2;
#pragma unroll
        for (int i = 0; i < 8; ++i) {
          const int d = (i & 3) + 8 * (i >> 2) + 4 * hh;
          *(u16*)(vp + d * 16) = (u16)(pack2(qa[i] * r1, 0.f) & 0xffffu);
        }
        if (hh == 0) {
          *(u16*)(vp + 16 * 16) = (u16)(pack2(qa[8] * r1, 0.f) & 0xffffu);
          *(u16*)(vp + 17 * 16) = (u16)(pack2(qa[9] * r1, 0.f) & 0xffffu);
          *(u16*)(vp + 18 * 16) = (u16)0x3f80;
        }
      }
    };
    qkv_tile(j, 1);
    STAMP(6);
    if (heavy) {
      qkv_tile(16 + (j >> 2), 0);
    } else if (lw < 648) {
      int ln = tid & 63; asm volatile("" : "+v"(ln));
      const int hh = ln >> 5, q4 = (ln & 15) >> 2, p4 = ln & 3, g16 = (ln >> 4) & 1;
      LDSP char* sb = smem + 0 * K1_SLOT + w * 9216 + (4 * hh + q4) * 576 + 32 * g16 + 8 * p4;
      char* dst = (lw < 576) ? ws + OFF_W2T + ((size_t)(lw >> 1) * 18 + (lw & 1) * 9) * 1024
                             : ws + OFF_WOT + ((size_t)((lw - 576) >> 1) * 18 + (lw & 1) * 9) * 1024;
#pragma unroll
      for (int n = 0; n < 9; ++n) {
        s16x8 f = tr_frag(sb + n * 64, 8 * 576);
        store16(dst + n * 1024 + ln * 16, __builtin_bit_cast(u32x4, f));
      }
    }
  }
  STAMP(7);
  STAMP(8);
  TS_FLUSH(0);
}

constexpr int K2_FLAG = KV_BYTES;
constexpr int K2_PF   = KV_BYTES + 128;
constexpr int K2_LDS  = KV_BYTES + 128 + 256;
enum { FL_KVA = 0, FL_KVG = 1, FL_PROG = 2, FL_GW = 3, FL_DONE = 4, FL_SENT = 5, FL_GW2 = 6 };

#define EXP2(x) __builtin_amdgcn_exp2f(x)

DI void flag_set(LDSP char* smem, int set, int i, int lane) {
  if (lane == 0) *(volatile LDSP unsigned*)(smem + K2_FLAG + set * 16 + i * 4) = 1u;
}
DI void flag_wait_all(LDSP char* smem, int set) {
  while (true) {
    u32x4 f = *(volatile LDSP u32x4*)(smem + K2_FLAG + set * 16);
    if (f[0] & f[1] & f[2] & f[3]) break;
    __builtin_amdgcn_s_sleep(2);
  }
  asm volatile("" ::: "memory");
}
DI void flag_wait_one(LDSP char* smem, int set, int i) {
  while (*(volatile LDSP unsigned*)(smem + K2_FLAG + set * 16 + i * 4) == 0u) __builtin_amdgcn_s_sleep(1);
  asm volatile("" ::: "memory");
}

DI void attn_step(f32x16& scur, f32x16& snext, const s16x8& k0, const s16x8& k1, const s16x8& qn0, const s16x8& qn1,
                  const s16x8& vprev1, f32x16& oprev, const s16x8& vcur0, f32x16& ocur, u32x4& p0, u32x4& p1) {
  f32x16 zero;
#pragma unroll
  for (int i = 0; i < 16; ++i) zero[i] = 0.f;
#define EX(i) do { float t_ = scur[i]; asm volatile("v_exp_f32 %0, %0" : "+v"(t_)); scur[i] = t_; } while (0)
#define SPACING 3
  p1[2] = pack2(snext[12], snext[13]);
  p1[3] = pack2(snext[14], snext[15]);
  SCHED();
  snext = MFMA32(k0, qn0, zero);
  SCHED();
#pragma unroll
  for (int i = 0; i < SPACING; ++i) EX(i);
  SCHED();
  snext = MFMA32(k1, qn1, snext);
  SCHED();
#pragma unroll
  for (int i = SPACING; i < 8; ++i) EX(i);
  SCHED();
  p0[0] = pack2(scur[0], scur[1]);
  p0[1] = pack2(scur[2], scur[3]);
  p0[2] = pack2(scur[4], scur[5]);
  p0[3] = pack2(scur[6], scur[7]);
  SCHED();
  oprev = MFMA32(vprev1, __builtin_bit_cast(s16x8, p1), oprev);
  SCHED();
#pragma unroll
  for (int i = 8; i < 8 + SPACING; ++i) EX(i);
  SCHED();
  ocur = MFMA32(vcur0, __builtin_bit_cast(s16x8, p0), ocur);
  SCHED();
#pragma unroll
  for (int i = 8 + SPACING; i < 16; ++i) EX(i);
  SCHED();
  p1[0] = pack2(scur[8], scur[9]);
  p1[1] = pack2(scur[10], scur[11]);
  SCHED();
#undef EX
}

DI s16x8 make_q1(unsigned qe, unsigned mneg, int i, int hh) {
  const unsigned a = (hh == (i >> 1)) ? qe : 0u;
  u32x4 r = {(i & 1) ? 0u : a, (i & 1) ? a : 0u, hh ? 0u : mneg, 0u};
  return __builtin_bit_cast(s16x8, r);
}
DI unsigned neg_hi_lo(float m) {
  unsigned hi = pack2(-m, 0.f) & 0xffffu;
  float rem = -m - __builtin_bit_cast(float, hi << 16);
  unsigned lo = pack2(rem, 0.f) & 0xffffu;
  return hi | (lo << 16);
}
DI int v_row(int t32) { return t32 > 18 ? ((t32 >= 20 && t32 < 28) ? 0 : 4) : t32; }

DI f32x16 attn_exact(const char* kv, int lane, u32x4 q0u, unsigned qe) {
  const int t32 = lane & 31, hh = lane >> 5;
  const s16x8 q0 = __builtin_bit_cast(s16x8, q0u);
  f32x16 zero, o;
#pragma unroll
  for (int i = 0; i < 16; ++i) { zero[i] = 0.f; o[i] = 0.f; }
  float m = -INFINITY;
#pragma unroll 1
  for (int T = 0; T < 64; ++T) {
    const char* gp = kv + (T >> 2) * KV_GRP;
    s16x8 k0 = *(const s16x8*)(gp + 1024 + (T & 3) * KV_TILE + lane * 16);
    s16x8 k1 = *(const s16x8*)(gp + lane * 16);
    f32x16 s = MFMA32(k0, q0, zero);
    s = MFMA32(k1, make_q1(qe, 0u, T & 3, hh), s);
#pragma unroll
    for (int i = 0; i < 16; ++i) m = __builtin_fmaxf(m, s[i]);
  }
  m = __builtin_fmaxf(m, other_half(m));
  const unsigned mneg = neg_hi_lo(m);
#pragma unroll 1
  for (int T = 0; T < 64; ++T) {
    const char* gp = kv + (T >> 2) * KV_GRP;
    const char* tp = gp + 1024 + (T & 3) * KV_TILE;
    s16x8 k0 = *(const s16x8*)(tp + lane * 16);
    s16x8 k1 = *(const s16x8*)(gp + lane * 16);
    s16x8 v0 = *(const s16x8*)(tp + 1024 + hh * 304 + v_row(t32) * 16);
    s16x8 v1 = *(const s16x8*)(tp + 1024 + 608 + hh * 304 + v_row(t32) * 16);
    f32x16 s = MFMA32(k0, q0, zero);
    s = MFMA32(k1, make_q1(qe, mneg, T & 3, hh), s);
    u32x4 p0, p1;
#pragma unroll
    for (int i = 0; i < 4; ++i) {
      p0[i] = pack2(EXP2(s[2 * i]), EXP2(s[2 * i + 1]));
      p1[i] = pack2(EXP2(s[8 + 2 * i]), EXP2(s[8 + 2 * i + 1]));
    }
    o = MFMA32(v0, __builtin_bit_cast(s16x8, p0), o);
    o = MFMA32(v1, __builtin_bit_cast(s16x8, p1), o);
  }
  return o;
}

typedef __amdgpu_buffer_rsrc_t rsrc_t;
DI s16x8 bload(rsrc_t r, unsigned voff, unsigned soff) {
  return __builtin_bit_cast(s16x8, __builtin_amdgcn_raw_buffer_load_b128(r, voff, soff, 0));
}
template <int NT>
DI void gemm2_load(rsrc_t r, unsigned hp, unsigned wp, unsigned wsp, unsigned lo16, int k, s16x8 (&hb)[2], s16x8 (&wb)[5]) {
  hb[0] = bload(r, lo16, hp + k * 1024);
  wb[0] = bload(r, lo16, wp + k * 18432);
  hb[1] = bload(r, lo16, hp + k * 1024 + 288 * 1024);
#pragma unroll
  for (int n = 1; n < 4; ++n) wb[n] = bload(r, lo16 + n * 1024, wp + k * 18432);
  if (NT == 5) wb[4] = bload(r, lo16, wsp + k * 18432);
}
template <int NT, int NTN>
DI void gemm2_step(rsrc_t r, unsigned hp, unsigned wp, unsigned wsp, unsigned lo16, int kn, s16x8 (&hb)[2], s16x8 (&wb)[5],
                   f32x16 (&acc)[2][5]) {
#pragma unroll
  for (int n = 0; n < NT; ++n) {
    acc[0][n] = MFMA32(wb[n], hb[0], acc[0][n]);
    acc[1][n] = MFMA32(wb[n], hb[1], acc[1][n]);
    SCHED();
    if (n < 4) wb[n] = bload(r, lo16 + n * 1024, wp + kn * 18432);
    else if (NTN == 5) wb[4] = bload(r, lo16, wsp + kn * 18432);
    SCHED();
  }
  if (NT == 4 && NTN == 5) wb[4] = bload(r, lo16, wsp + kn * 18432);
  hb[0] = bload(r, lo16, hp + kn * 1024);
  hb[1] = bload(r, lo16, hp + kn * 1024 + 288 * 1024);
  SCHED();
}
template <int N1, int N2, typename F, typename G>
DI void gemm2_all(rsrc_t r, unsigned hp, unsigned wp, unsigned wsp, unsigned lo16, f32x16 (&acc)[2][5], F&& after_first_loads, G&& after_first_step) {
  s16x8 hA[2], wA[5], hB[2], wB[5];
  after_first_loads();
  gemm2_load<N1>(r, hp, wp, wsp, lo16, 0, hA, wA);
  gemm2_load<N1>(r, hp, wp, wsp, lo16, 1, hB, wB);
  SCHED();
  after_first_step();
#pragma unroll 1
  for (int k = 0; k < 16; k += 2) {
    gemm2_step<N1, N1>(r, hp, wp, wsp, lo16, k + 2, hA, wA, acc);
    gemm2_step<N1, N1>(r, hp, wp, wsp, lo16, k + 3, hB, wB, acc);
  }
  gemm2_step<N1, N2>(r, hp, wp, wsp, lo16, 18, hA, wA, acc);
  gemm2_step<N1, N2>(r, hp, wp, wsp, lo16, 19, hB, wB, acc);
#pragma unroll 1
  for (int k = 18; k < 36; k += 2) {
    gemm2_step<N2, N2>(r, hp, wp, wsp, lo16, (k + 2 < 36) ? k + 2 : 35, hA, wA, acc);
    gemm2_step<N2, N2>(r, hp, wp, wsp, lo16, (k + 3 < 36) ? k + 3 : 35, hB, wB, acc);
  }
}

__global__ __launch_bounds__(512, 2)
void k2(const u16* __restrict__ x, char* __restrict__ ws) {
  extern __shared__ __attribute__((aligned(16))) char smem_[];
  LDSP char* smem = (LDSP char*)smem_;
  const int tid = threadIdx.x, lane = tid & 63;
  const int w = __builtin_amdgcn_readfirstlane(tid >> 6);
  const int b = blockIdx.x;
  const int t32 = lane & 31, hh = lane >> 5;
  TS_DECL; STAMP(0);
  if (tid < 32) *(LDSP unsigned*)(smem + K2_FLAG + tid * 4) = 0u;
  const char* kvg = ws + OFF_KV;

  if (w < 4) {
#define ATT_PRIO 0
#define GEM_PRIO 0
    __builtin_amdgcn_s_setprio(ATT_PRIO);
    const int tt = b >> 2, hg = b & 3;
    const int rs = w & 1, kh2 = w >> 1;
    const int head0 = hg * 8 + rs * 4;
#pragma unroll
    for (int i = 0; i < 5; ++i) {
      glds16(kvg + (i * 4 + w) * 1024, lane * 16, smem + (i * 4 + w) * 1024);
      glds16(kvg + KV_HALF + (i * 4 + w) * 1024, lane * 16, smem + KV_HALF + (i * 4 + w) * 1024);
    }
    u32x4 q0u[4];
    unsigned qe[4];
#pragma unroll
    for (int qb = 0; qb < 4; ++qb) {
      const char* qp = ws + OFF_Q + (size_t)tt * 36864 + (head0 + qb) * 1152;
      q0u[qb] = *(const u32x4*)(qp + lane * 16);
      qe[qb] = *(const unsigned*)(qp + 1024 + t32 * 4);
    }
    const char* pf_base;
    unsigned pf_off, pf_step;
    {
      const int xcd = b & 7, a = (b >> 3) * 4 + w, kh = xcd & 1;
      if (a < 48) {
        const unsigned L = (a % 3) * 64 + lane;
        pf_base = ws + OFF_H + ((size_t)((xcd >> 1) * 16 + a / 3) * 288 + kh * 144) * 1024;
        pf_off = (L / 48) * 36 * 1024 + (L % 48) * 128;
        pf_step = 6 * 1024;
      } else {
        const unsigned L = ((a < 102 ? a : 101) - 48) * 64 + lane;
        pf_base = ws + OFF_W2T + (size_t)kh * 144 * 18432;
        pf_off = (L / 864) * 36 * 18432 + (L % 864) * 128;
        pf_step = 6 * 18432;
      }
    }
    WAIT_LGKM0(); BAR();
    WAIT_VM(0);
    flag_set(smem, FL_KVA, w, lane);
    flag_wait_all(smem, FL_KVA);
    STAMP(1);
    s16x8 q0[4], q1[4][4];
    f32x16 o[4];
    f32x16 sa, sb;
#pragma unroll
    for (int i = 0; i < 16; ++i) sb[i] = 0.f;
    {
      s16x8 k0 = *(LDSP s16x8*)(smem + 1024 + lane * 16);
      s16x8 k1 = *(LDSP s16x8*)(smem + lane * 16);
#pragma unroll
      for (int qb = 0; qb < 4; ++qb) {
        asm volatile("" : "+v"(q0u[qb]), "+v"(qe[qb]));
        q0[qb] = __builtin_bit_cast(s16x8, q0u[qb]);
        f32x16 s = MFMA32(k0, q0[qb], sb);
        s = MFMA32(k1, make_q1(qe[qb], 0u, 0, hh), s);
        float m = s[0];
#pragma unroll
        for (int i = 1; i < 16; ++i) m = __builtin_fmaxf(m, s[i]);
        m = __builtin_fmaxf(m, other_half(m));
        const unsigned mneg = neg_hi_lo(m);
#pragma unroll
        for (int i = 0; i < 4; ++i) q1[i][qb] = make_q1(qe[qb], mneg, i, hh);
#pragma unroll
        for (int i = 0; i < 16; ++i) o[qb][i] = 0.f;
      }
    }
    LDSP char* kp = smem + kh2 * KV_HALF + lane * 16;
    LDSP char* vp = smem + kh2 * KV_HALF + 2048 + hh * 304 + v_row(t32) * 16;
    u32x4 p0 = {0u, 0u, 0u, 0u}, p1 = {0u, 0u, 0u, 0u};
    s16x8 k1c = *(LDSP s16x8*)(kp), k1n;
    s16x8 k0b[2], v0b[2], v1b[2];
    k0b[0] = *(LDSP s16x8*)(kp + 1024);
    const unsigned long long vmask = 0x0007ffff0007ffffull;
#pragma unroll
    for (int i = 0; i < 2; ++i) { v0b[i] = s16x8{0, 0, 0, 0, 0, 0, 0, 0}; v1b[i] = s16x8{0, 0, 0, 0, 0, 0, 0, 0}; }
#define VLD(dst, addr, off) asm volatile("s_mov_b64 exec, %2\n\tds_read_b128 %0, %1 offset:%3\n\ts_mov_b64 exec, -1" \
                                         : "+v"(dst) : "v"(addr), "s"(vmask), "i"(off) : "memory")
#define VWAIT(dst) asm volatile("s_waitcnt lgkmcnt(0)" : "+v"(dst) :: "memory")
    VLD(v0b[0], vp, 0);
    sa = MFMA32(k0b[0], q0[0], sb);
    sa = MFMA32(k1c, q1[0][0], sa);
    asm volatile("s_nop 15" : "+v"(sa));
#pragma unroll 1
    for (int g = 0; g < 8; ++g) {
      if (g == 1) flag_wait_all(smem, FL_KVG);
      if (g == 5) flag_set(smem, FL_PROG, w, lane);
      const int nxg = (g < 7) ? KV_GRP : 0;
#pragma unroll
      for (int i = 0; i < 4; ++i) {
        const int c = i & 1, n = c ^ 1;
        const int nt_off = (i < 3) ? (i + 1) * KV_TILE : nxg;
        VWAIT(v0b[c]);
        VLD(v1b[c], vp, i * KV_TILE + 608);
        SCHED();
        attn_step(sa, sb, k0b[c], k1c, q0[1], q1[i][1], v1b[n], o[3], v0b[c], o[0], p0, p1);
        VWAIT(v1b[c]);
        k0b[n] = *(LDSP s16x8*)(kp + 1024 + nt_off);
        if (i == 3) k1n = *(LDSP s16x8*)(kp + nxg);
        SCHED();
        attn_step(sb, sa, k0b[c], k1c, q0[2], q1[i][2], v1b[c], o[0], v0b[c], o[1], p0, p1);
        if (i < 3) VLD(v0b[n], vp, (i + 1) * KV_TILE); else { LDSP char* vn = vp + nxg; VLD(v0b[n], vn, 0); }
        SCHED();
        attn_step(sa, sb, k0b[c], k1c, q0[3], q1[i][3], v1b[c], o[1], v0b[c], o[2], p0, p1);
        attn_step(sb, sa, k0b[n], (i == 3) ? k1n : k1c, q0[0], q1[(i + 1) & 3][0], v1b[c], o[2], v0b[c], o[3], p0, p1);
      }
      k1c = k1n;
      kp += KV_GRP; vp += KV_GRP;
    }
    p1[2] = pack2(sb[12], sb[13]);
    p1[3] = pack2(sb[14], sb[15]);
    o[3] = MFMA32(v1b[1], __builtin_bit_cast(s16x8, p1), o[3]);
    STAMP(2);
    f32x16 keep[2], send[2];
    u32x4 qk0[2];
    unsigned qke[2];
#pragma unroll
    for (int q = 0; q < 2; ++q) {
#pragma unroll
      for (int i = 0; i < 11; ++i) {
        keep[q][i] = kh2 ? o[2 + q][i] : o[q][i];
        send[q][i] = kh2 ? o[q][i] : o[2 + q][i];
      }
#pragma unroll
      for (int i = 11; i < 16; ++i) { keep[q][i] = 0.f; send[q][i] = 0.f; }
#pragma unroll
      for (int i = 0; i < 4; ++i) qk0[q][i] = kh2 ? q0u[2 + q][i] : q0u[q][i];
      { unsigned a_ = qe[q], b_ = qe[2 + q]; asm volatile("" : "+v"(a_), "+v"(b_)); qke[q] = kh2 ? b_ : a_; }
    }
    WAIT_LGKM0();
    flag_set(smem, FL_DONE, w, lane);
    flag_wait_one(smem, FL_DONE, w ^ 1);
    {
      LDSP char* xp = smem + kh2 * KV_HALF + 7 * KV_GRP + rs * 4864;
#pragma unroll
      for (int q = 0; q < 2; ++q) {
        const f32x16 ov = send[q];
        *(LDSP f32x4*)(xp + q * 2432 + lane * 16) = f32x4{ov[0], ov[1], ov[2], ov[3]};
        *(LDSP f32x4*)(xp + q * 2432 + 1024 + lane * 16) = f32x4{ov[4], ov[5], ov[6], ov[7]};
        if (hh == 0) {
          *(LDSP float*)(xp + q * 2432 + 2048 + t32 * 4) = ov[8];
          *(LDSP float*)(xp + q * 2432 + 2176 + t32 * 4) = ov[9];
          *(LDSP float*)(xp + q * 2432 + 2304 + t32 * 4) = ov[10];
        }
      }
    }
    WAIT_LGKM0();
    flag_set(smem, FL_SENT, w, lane);
    flag_wait_one(smem, FL_SENT, w ^ 2);
    bool bad = false;
#pragma unroll
    for (int q = 0; q < 2; ++q) {
      LDSP char* xp = smem + (kh2 ^ 1) * KV_HALF + 7 * KV_GRP + rs * 4864 + q * 2432;
      f32x4 a0 = *(LDSP f32x4*)(xp + lane * 16), a1 = *(LDSP f32x4*)(xp + 1024 + lane * 16);
#pragma unroll
      for (int i = 0; i < 4; ++i) { keep[q][i] += a0[i]; keep[q][4 + i] += a1[i]; }
      keep[q][8] += *(LDSP float*)(xp + 2048 + t32 * 4);
      keep[q][9] += *(LDSP float*)(xp + 2176 + t32 * 4);
      keep[q][10] += *(LDSP float*)(xp + 2304 + t32 * 4);
      float mag = 0.f;
#pragma unroll
      for (int i = 0; i < 8; ++i) mag += __builtin_fabsf(keep[q][i]);
      const float ext = __builtin_fabsf(keep[q][8]) + __builtin_fabsf(keep[q][9]) + 1e12f * __builtin_fabsf(keep[q][10]);
      mag += hh ? 0.f : ext;
      bad = bad || !(mag < 1e30f);
    }
    if (__builtin_expect(__any(bad), 0)) {
#pragma unroll 1
      for (int q = 0; q < 2; ++q) {
        f32x16 ex = attn_exact(kvg, lane, q ? qk0[1] : qk0[0], q ? qke[1] : qke[0]);
        if (q) keep[1] = ex; else keep[0] = ex;
      }
    }
#pragma unroll
    for (int q = 0; q < 2; ++q) {
      float l = keep[q][10];
      const float lo_ = other_half(l);
      const float li = __builtin_amdgcn_rcpf(hh ? lo_ : l);
      const int head = head0 + 2 * kh2 + q;
      u32x4 o4 = {pack2(keep[q][0] * li, keep[q][1] * li), pack2(keep[q][2] * li, keep[q][3] * li),
                  pack2(keep[q][4] * li, keep[q][5] * li), pack2(keep[q][6] * li, keep[q][7] * li)};
      char* ap = ws + OFF_AO + (size_t)tt * 36 * 1024;
      store16(ap + head * 1024 + lane * 16, o4);
      if (hh == 0) {
        const int h8 = head & 7;
        *(unsigned*)(ap + (32 + (head >> 3)) * 1024 + (t32 + 32 * ((h8 >> 1) & 1)) * 16 + ((h8 >> 2) * 2 + (h8 & 1)) * 4) =
            pack2(keep[q][8] * li, keep[q][9] * li);
      }
    }
    STAMP(3);
  } else {
    __builtin_amdgcn_s_setprio(GEM_PRIO);
    const int g = w - 4;
    const int xcd = b & 7, idx = b >> 3;
    const int kh = xcd & 1, tp = (xcd >> 1) * 8 + (idx & 7), ng = idx >> 3;
    const int nt0 = (ng >> 1) * 9 + (ng & 1) * 5;
    const int nts = (ng >> 1) * 9 + 4;
    const int fb0 = kh * 144 + g * 36;
    const rsrc_t rsrc = __builtin_amdgcn_make_buffer_rsrc(ws, 0, (unsigned)OFF_WOT, 0x00020000);
    const unsigned hp0 = (unsigned)OFF_H + ((2 * tp) * 288 + fb0) * 1024;
    const unsigned wp = (unsigned)OFF_W2T + (fb0 * 18 + nt0) * 1024;
    const unsigned wsp = (unsigned)OFF_W2T + (fb0 * 18 + nts) * 1024;
    const unsigned lo16 = lane * 16;
    BAR();
    f32x16 acc[2][5];
#pragma unroll
    for (int a = 0; a < 2; ++a)
#pragma unroll
      for (int n = 0; n < 5; ++n)
#pragma unroll
        for (int i = 0; i < 16; ++i) acc[a][n][i] = 0.f;
    auto stage_kv = [&]() {
      __builtin_amdgcn_s_setprio(3);
#pragma unroll 1
      for (int i = g; i < 58; i += 4) {
        glds16(kvg + (20 + i) * 1024, lo16, smem + (20 + i) * 1024);
        glds16(kvg + KV_HALF + (20 + i) * 1024, lo16, smem + KV_HALF + (20 + i) * 1024);
      }
      __builtin_amdgcn_s_setprio(GEM_PRIO);
      STAMP(6);
    };
    auto kv_ready = [&]() {
      WAIT_VM(12);
      flag_set(smem, FL_KVG, g, lane);
      STAMP(1);
    };
    if (ng & 1) gemm2_all<4, 5>(rsrc, hp0, wp, wsp, lo16, acc, stage_kv, kv_ready);
    else        gemm2_all<5, 4>(rsrc, hp0, wp, wsp, lo16, acc, stage_kv, kv_ready);
    STAMP(2);
    {
      const int a = idx * 4 + g;
      unsigned sink = 0;
#pragma unroll
      for (int i = 0; i < 5; ++i) {
        const u32x4 v = *(const u32x4*)(ws + OFF_WOT + (size_t)min(a * 5 + i + (a >> 4), 647) * 1024 + lo16);
        sink ^= v[0];
      }
#pragma unroll
      for (int i = 0; i < 3; ++i) {
        const u32x4 v = *(const u32x4*)((const char*)x + (size_t)xcd * 294912 + (size_t)min(a * 3 + i, 287) * 1024 + lo16);
        sink ^= v[0];
      }
      asm volatile("" :: "v"(sink));
    }
    flag_wait_all(smem, FL_PROG);
    STAMP(3);
    LDSP char* mine   = smem + (g >> 1) * KV_HALF + (g & 1) * 24576 + lo16;
    LDSP char* theirs = smem + (g >> 1) * KV_HALF + ((g & 1) ^ 1) * 24576 + lo16;
    LDSP char* other  = smem + ((g >> 1) ^ 1) * KV_HALF + ((g & 1) ^ 1) * 24576 + lo16;
    auto reduce = [&](auto aa, auto hh_) {
      constexpr int A = decltype(aa)::value, HF = decltype(hh_)::value;
#pragma unroll
      for (int n = 0; n < 5; ++n)
#pragma unroll
        for (int q = 0; q < 4; ++q)
          *(LDSP f32x4*)(mine + (n * 4 + q) * 1024) =
              f32x4{acc[A ^ 1][n][4 * q], acc[A ^ 1][n][4 * q + 1], acc[A ^ 1][n][4 * q + 2], acc[A ^ 1][n][4 * q + 3]};
      WAIT_LGKM0();
      flag_set(smem, FL_GW, g, lane);
      flag_wait_one(smem, FL_GW, g ^ 1);
#pragma unroll
      for (int n = 0; n < 5; ++n)
#pragma unroll
        for (int q = 0; q < 4; ++q) {
          f32x4 v = *(LDSP f32x4*)(theirs + (n * 4 + q) * 1024);
#pragma unroll
          for (int i = 0; i < 4; ++i) acc[A][n][4 * q + i] += v[i];
        }
      WAIT_LGKM0();
#pragma unroll
      for (int n = 0; n < 5; ++n)
#pragma unroll
        for (int q = 0; q < 2; ++q) {
          constexpr int r0 = 8 * (HF ^ 1);
          *(LDSP f32x4*)(theirs + (n * 2 + q) * 1024) =
              f32x4{acc[A][n][r0 + 4 * q], acc[A][n][r0 + 4 * q + 1], acc[A][n][r0 + 4 * q + 2], acc[A][n][r0 + 4 * q + 3]};
        }
      WAIT_LGKM0();
      flag_set(smem, FL_GW2, g, lane);
      flag_wait_one(smem, FL_GW2, g ^ 2);
#pragma unroll
      for (int n = 0; n < 5; ++n) {
        const int nt = (n < 4) ? nt0 + n : nts;
        const int slot = (n < 4) ? kh : kh + 2 * (ng & 1);
        char* pp = ws + OFF_PART + (((size_t)slot * 64 + (2 * tp + A)) * 18 + nt) * 2048 + HF * 1024 + lo16;
        u32x4 o4;
#pragma unroll
        for (int q = 0; q < 2; ++q) {
          f32x4 v = *(LDSP f32x4*)(other + (n * 2 + q) * 1024);
#pragma unroll
          for (int i = 0; i < 4; ++i) v[i] += acc[A][n][8 * HF + 4 * q + i];
          o4[2 * q] = pack2(v[0], v[1]);
          o4[2 * q + 1] = pack2(v[2], v[3]);
        }
        store16(pp, o4);
      }
    };
    using I0 = std::integral_constant<int, 0>;
    using I1 = std::integral_constant<int, 1>;
    if (g == 0) reduce(I0{}, I0{});
    else if (g == 1) reduce(I1{}, I0{});
    else if (g == 2) reduce(I0{}, I1{});
    else reduce(I1{}, I1{});
  }
  STAMP(4);
  STAMP(5);
  TS_FLUSH(1);
}

constexpr int K3_LDS = 6 * 6 * 4096;
__global__ __launch_bounds__(384)
void k3(const u16* __restrict__ x, const u16* __restrict__ b2, char* __restrict__ ws, u16* __restrict__ out) {
  TS_DECL; STAMP(0);
  extern __shared__ __attribute__((aligned(16))) char smem_[];
  LDSP char* smem = (LDSP char*)smem_;
  const int tid = threadIdx.x, lane = tid & 63;
  const int w = __builtin_amdgcn_readfirstlane(tid >> 6);
  const int b = blockIdx.x;
  const int tp = (b & 7) * 4 + (b >> 3) / 6, ng = (b >> 3) % 6;
  const int t32 = lane & 31, hh = lane >> 5;
  const int tt = 2 * tp + w / 3, nt = 3 * ng + w % 3;
  const int token = tt * 32 + t32;
  const char* ap = ws + OFF_AO + ((size_t)(2 * tp) * 36 + w * 6) * 1024 + lane * 16;
  const char* wp = ws + OFF_WOT + ((size_t)(w * 6) * 18 + 3 * ng) * 1024 + lane * 16;
  const bool shared = (nt == 4 || nt == 13);
  const size_t SL = (size_t)64 * 18 * 2048;
  const char* pp = ws + OFF_PART + ((size_t)tt * 18 + nt) * 2048 + lane * 16;
  const char* pp2 = shared ? pp + 2 * SL : pp;
  s16x8 af[2][6], wf[3][6];
  u32x4 pt[4][2];
  u32x2 xv[4], bv[4];
#pragma unroll
  for (int h = 0; h < 2; ++h) {
    pt[0][h] = *(const u32x4*)(pp + h * 1024);
    pt[1][h] = *(const u32x4*)(pp + SL + h * 1024);
    pt[2][h] = *(const u32x4*)(pp2 + h * 1024);
    pt[3][h] = *(const u32x4*)(pp2 + SL + h * 1024);
  }
#pragma unroll
  for (int q = 0; q < 4; ++q) {
    const int col = nt * 32 + 8 * q + 4 * hh;
    xv[q] = *(const u32x2*)((const char*)x + (size_t)token * 1152 + col * 2);
    bv[q] = *(const u32x2*)((const char*)b2 + col * 2);
  }
  SCHED();
#pragma unroll
  for (int k = 0; k < 6; ++k) {
    af[0][k] = *(const s16x8*)(ap + k * 1024);
    wf[0][k] = *(const s16x8*)(wp + k * 18432);
    af[1][k] = *(const s16x8*)(ap + 36 * 1024 + k * 1024);
    wf[1][k] = *(const s16x8*)(wp + k * 18432 + 1024);
    wf[2][k] = *(const s16x8*)(wp + k * 18432 + 2048);
  }
  SCHED();
  f32x16 acc[2][3];
#pragma unroll
  for (int i = 0; i < 2; ++i)
#pragma unroll
    for (int jn = 0; jn < 3; ++jn)
#pragma unroll
      for (int r = 0; r < 16; ++r) acc[i][jn][r] = 0.f;
#pragma unroll
  for (int k = 0; k < 6; ++k)
#pragma unroll
    for (int i = 0; i < 2; ++i)
#pragma unroll
      for (int jn = 0; jn < 3; ++jn) acc[i][jn] = MFMA32(wf[jn][k], af[i][k], acc[i][jn]);
#pragma unroll
  for (int i = 0; i < 2; ++i)
#pragma unroll
    for (int jn = 0; jn < 3; ++jn)
#pragma unroll
      for (int q = 0; q < 4; ++q) {
        f32x4 v = {acc[i][jn][4 * q], acc[i][jn][4 * q + 1], acc[i][jn][4 * q + 2], acc[i][jn][4 * q + 3]};
        *(LDSP f32x4*)(smem + ((w * 6 + i * 3 + jn) * 4 + q) * 1024 + lane * 16) = v;
      }
  const float sh = shared ? 1.f : 0.f;
  f32x4 fin[4];
#pragma unroll
  for (int q = 0; q < 4; ++q) {
#pragma unroll
    for (int e = 0; e < 2; ++e) {
      const unsigned p0 = pt[0][q >> 1][(q & 1) * 2 + e], p1 = pt[1][q >> 1][(q & 1) * 2 + e];
      const unsigned p2 = pt[2][q >> 1][(q & 1) * 2 + e], p3 = pt[3][q >> 1][(q & 1) * 2 + e];
      const float lo = (__builtin_bit_cast(float, p0 << 16) + __builtin_bit_cast(float, p1 << 16)) +
                       sh * (__builtin_bit_cast(float, p2 << 16) + __builtin_bit_cast(float, p3 << 16));
      const float hi = (__builtin_bit_cast(float, p0 & 0xffff0000u) + __builtin_bit_cast(float, p1 & 0xffff0000u)) +
                       sh * (__builtin_bit_cast(float, p2 & 0xffff0000u) + __builtin_bit_cast(float, p3 & 0xffff0000u));
      fin[q][2 * e]     = lo + __builtin_bit_cast(float, xv[q][e] << 16) + __builtin_bit_cast(float, bv[q][e] << 16);
      fin[q][2 * e + 1] = hi + __builtin_bit_cast(float, xv[q][e] & 0xffff0000u) + __builtin_bit_cast(float, bv[q][e] & 0xffff0000u);
    }
  }
  STAMP(1);
  __syncthreads();
#pragma unroll
  for (int q = 0; q < 4; ++q) {
    f32x4 v = fin[q];
#pragma unroll
    for (int o = 0; o < 6; ++o) v += *(LDSP f32x4*)(smem + ((o * 6 + w) * 4 + q) * 1024 + lane * 16);
    u32x2 ov = {pack2(v[0], v[1]), pack2(v[2], v[3])};
    *(u32x2*)((char*)out + (size_t)token * 1152 + (nt * 32 + 8 * q + 4 * hh) * 2) = ov;
  }
  STAMP(2);
  STAMP(3);
  TS_FLUSH(2);
}

extern "C" void kernel_launch(void* const* d_in, const int* in_sizes, int n_in,
                              void* d_out, int out_size, void* d_ws, size_t ws_size,
                              hipStream_t stream) {
  const u16* x  = (const u16*)d_in[0];
  const u16* wq = (const u16*)d_in[1];
  const u16* wk = (const u16*)d_in[2];
  const u16* wv = (const u16*)d_in[3];
  const u16* wo = (const u16*)d_in[4];
  const u16* w1 = (const u16*)d_in[6];
  const u16* b1 = (const u16*)d_in[7];
  const u16* w2 = (const u16*)d_in[8];
  const u16* b2 = (const u16*)d_in[9];
  char* ws = (char*)d_ws;
  (void)hipFuncSetAttribute((const void*)k1, hipFuncAttributeMaxDynamicSharedMemorySize, K1_LDS);
  (void)hipFuncSetAttribute((const void*)k2, hipFuncAttributeMaxDynamicSharedMemorySize, K2_LDS);
  (void)hipFuncSetAttribute((const void*)k3, hipFuncAttributeMaxDynamicSharedMemorySize, K3_LDS);
  k1<<<256, 512, K1_LDS, stream>>>(x, wq, wk, wv, wo, w1, b1, w2, ws);
  k2<<<256, 512, K2_LDS, stream>>>(x, ws);
  k3<<<192, 384, K3_LDS, stream>>>(x, b2, ws, (u16*)d_out);
}
```

```cpp
#include <hip/hip_runtime.h>
#include <hip/hip_bf16.h>
#include <stdint.h>
#include <type_traits>

#define DI __device__ __forceinline__
#define LDSP __attribute__((address_space(3)))
#define GLP __attribute__((address_space(1)))

typedef unsigned short u16;
typedef __attribute__((ext_vector_type(8))) short s16x8;
typedef __attribute__((ext_vector_type(4))) short s16x4;
typedef __attribute__((ext_vector_type(16))) float f32x16;
typedef __attribute__((ext_vector_type(4))) float f32x4;
typedef __attribute__((ext_vector_type(2))) float f32x2;
typedef __attribute__((ext_vector_type(2))) __bf16 bf16x2;
typedef __attribute__((ext_vector_type(4))) unsigned u32x4;
typedef __attribute__((ext_vector_type(2))) unsigned u32x2;

constexpr size_t OFF_H    = 0;
constexpr size_t OFF_W2T  = OFF_H + 18874368;
constexpr size_t OFF_WOT  = OFF_W2T + 5308416;
constexpr size_t OFF_Q    = OFF_WOT + 663552;
constexpr size_t OFF_KV   = OFF_Q + 2359296;
constexpr size_t OFF_AO   = OFF_KV + 159744;
constexpr size_t OFF_PART = OFF_AO + 2359296;
constexpr int KV_GRP = 9984, KV_TILE = 2240, KV_BYTES = 159744, KV_HALF = 79872;

#define MFMA32(a, b, c) __builtin_amdgcn_mfma_f32_32x32x16_bf16((a), (b), (c), 0, 0, 0)
#define WAIT_VM(n) asm volatile("s_waitcnt vmcnt(" #n ")" ::: "memory")
#define WAIT_LGKM0() asm volatile("s_waitcnt lgkmcnt(0)" ::: "memory")
#define BAR() __builtin_amdgcn_s_barrier()

DI unsigned pack2(float a, float b) {
  f32x2 v = {a, b};
  return __builtin_bit_cast(unsigned, __builtin_convertvector(v, bf16x2));
}
DI void glds16(const void* gbase, unsigned voff, LDSP char* l) {
  unsigned keep;
  asm volatile("s_mov_b32 %0, m0\n\ts_mov_b32 m0, %3\n\ts_nop 0\n\tglobal_load_lds_dwordx4 %1, %2\n\ts_mov_b32 m0, %0"
               : "=&s"(keep) : "v"(voff), "s"(gbase), "s"((unsigned)(size_t)l) : "memory");
}
DI void glds4(const void* gbase, unsigned voff, LDSP char* l) {
  unsigned keep;
  asm volatile("s_mov_b32 %0, m0\n\ts_mov_b32 m0, %3\n\ts_nop 0\n\tglobal_load_lds_dword %1, %2\n\ts_mov_b32 m0, %0"
               : "=&s"(keep) : "v"(voff), "s"(gbase), "s"((unsigned)(size_t)l) : "memory");
}
DI void store16(void* p, u32x4 v) {
  asm volatile("global_store_dwordx4 %0, %1, off sc1\n\ts_nop 1" :: "v"(p), "v"(v) : "memory");
}
DI s16x8 tr_frag(LDSP char* p, int second_off) {
  s16x4 lo = __builtin_amdgcn_ds_read_tr16_b64_v4i16((LDSP s16x4*)p);
  s16x4 hi = __builtin_amdgcn_ds_read_tr16_b64_v4i16((LDSP s16x4*)(p + second_off));
  return __builtin_shufflevector(lo, hi, 0, 1, 2, 3, 4, 5, 6, 7);
}
DI float other_half(float v) {
  unsigned u = __builtin_bit_cast(unsigned, v);
  auto r = __builtin_amdgcn_permlane32_swap(u, u, false, false);
  unsigned o = (threadIdx.x & 32) ? r[0] : r[1];
  return __builtin_bit_cast(float, o);
}

constexpr int K1_SLOT = 36864;
constexpr int K1_BIAS = 4 * K1_SLOT;
constexpr int K1_CS   = K1_BIAS + 1152;
constexpr int K1_CSQ  = K1_CS + 1152;
constexpr int K1_STAT = K1_CSQ + 1024;
constexpr int K1_LDS  = K1_STAT + 1024;

__host__ __device__ constexpr int w_slot(int st) { return (0x231023120ll >> (4 * st)) & 3; }

#define SCHED() __builtin_amdgcn_sched_barrier(0)
#define SGB(mask, n) __builtin_amdgcn_sched_group_barrier((mask), (n), 0)

DI u32x2 gelu4(float a0, float a1, float a2, float a3, const f32x4& cs, const f32x4& bb, float negm, float r, float c0) {
  float z0, z1, z2, z3, t0, t1, t2, t3;
  unsigned o0, o1;
  asm volatile(
      "v_fma_f32 %2, %18, %14, %10\n\tv_fma_f32 %3, %18, %15, %11\n\tv_fma_f32 %4, %18, %16, %12\n\tv_fma_f32 %5, %18, %17, %13\n\t"
      "v_fma_f32 %2, %19, %2, %21\n\tv_fma_f32 %3, %19, %3, %22\n\tv_fma_f32 %4, %19, %4, %23\n\tv_fma_f32 %5, %19, %5, %24\n\t"
      "v_mul_f32 %6, %2, %2\n\tv_mul_f32 %7, %3, %3\n\tv_mul_f32 %8, %4, %4\n\tv_mul_f32 %9, %5, %5\n\t"
      "v_fmamk_f32 %6, %6, 0xbdd2d3e8, %20\n\tv_fmamk_f32 %7, %7, 0xbdd2d3e8, %20\n\tv_fmamk_f32 %8, %8, 0xbdd2d3e8, %20\n\tv_fmamk_f32 %9, %9, 0xbdd2d3e8, %20\n\t"
      "v_mul_f32 %6, %2, %6\n\tv_mul_f32 %7, %3, %7\n\tv_mul_f32 %8, %4, %8\n\tv_mul_f32 %9, %5, %9\n\t"
      "v_exp_f32 %6, %6\n\tv_exp_f32 %7, %7\n\tv_exp_f32 %8, %8\n\tv_exp_f32 %9, %9\n\t"
      "v_add_f32 %6, 1.0, %6\n\tv_add_f32 %7, 1.0, %7\n\tv_add_f32 %8, 1.0, %8\n\tv_add_f32 %9, 1.0, %9\n\t"
      "v_rcp_f32 %6, %6\n\tv_rcp_f32 %7, %7\n\tv_rcp_f32 %8, %8\n\tv_rcp_f32 %9, %9\n\t"
      "v_mul_f32 %2, %2, %6\n\tv_mul_f32 %3, %3, %7\n\tv_mul_f32 %4, %4, %8\n\tv_mul_f32 %5, %5, %9\n\t"
      "v_cvt_pk_bf16_f32 %0, %2, %3\n\tv_cvt_pk_bf16_f32 %1, %4, %5"
      : "=&v"(o0), "=&v"(o1), "=&v"(z0), "=&v"(z1), "=&v"(z2), "=&v"(z3), "=&v"(t0), "=&v"(t1), "=&v"(t2), "=&v"(t3)
      : "v"(a0), "v"(a1), "v"(a2), "v"(a3), "v"(cs[0]), "v"(cs[1]), "v"(cs[2]), "v"(cs[3]), "v"(negm), "v"(r), "v"(c0),
        "v"(bb[0]), "v"(bb[1]), "v"(bb[2]), "v"(bb[3]));
  u32x2 o = {o0, o1};
  return o;
}

template <typename F>
DI void gelu_tile(const f32x16& a, LDSP char* cs_p, LDSP char* bias_p, char* out_p, float negm, float r, F&& between) {
  const float c0 = -2.302208198f;
#pragma unroll
  for (int hf = 0; hf < 2; ++hf) {
    f32x4 cs0 = *(LDSP f32x4*)(cs_p + hf * 64), cs1 = *(LDSP f32x4*)(cs_p + hf * 64 + 32);
    f32x4 bb0 = *(LDSP f32x4*)(bias_p + hf * 64), bb1 = *(LDSP f32x4*)(bias_p + hf * 64 + 32);
    between(2 * hf);
    u32x2 lo = gelu4(a[8 * hf], a[8 * hf + 1], a[8 * hf + 2], a[8 * hf + 3], cs0, bb0, negm, r, c0);
    between(2 * hf + 1);
    u32x2 hi = gelu4(a[8 * hf + 4], a[8 * hf + 5], a[8 * hf + 6], a[8 * hf + 7], cs1, bb1, negm, r, c0);
    u32x4 o = {lo[0], lo[1], hi[0], hi[1]};
    store16(out_p + hf * 1024, o);
  }
}

DI void colsum_frag(const s16x8& a, const s16x8& sel, f32x4& cs) {
  cs = __builtin_amdgcn_mfma_f32_16x16x32_bf16(sel, a, cs, 0, 0, 0);
}

template <int KT>
DI void w1_stage(LDSP char* sb0, LDSP char* sb1, LDSP char* sb2, const s16x8 (&xf)[36], f32x16 (&acc)[3], const s16x8& sel, f32x4& cs) {
  asm volatile("" : "+v"(sb0), "+v"(sb1), "+v"(sb2));
  s16x8 a[2][3];
  a[0][0] = tr_frag(sb0, 768); a[0][1] = tr_frag(sb1, 768); a[0][2] = tr_frag(sb2, 768);
#pragma unroll
  for (int ks = 0; ks < 12; ++ks) {
    const int cur = ks & 1;
    if (ks + 1 < 12) {
      a[cur ^ 1][0] = tr_frag(sb0 + (ks + 1) * 3072, 768);
      a[cur ^ 1][1] = tr_frag(sb1 + (ks + 1) * 3072, 768);
      a[cur ^ 1][2] = tr_frag(sb2 + (ks + 1) * 3072, 768);
    }
#pragma unroll
    for (int t = 0; t < 3; ++t) acc[t] = MFMA32(a[cur][t], xf[12 * KT + ks], acc[t]);
    colsum_frag(a[cur][0], sel, cs);
    SGB(0x100, 2); SGB(0x8, 1); SGB(0x100, 2); SGB(0x8, 1); SGB(0x100, 2); SGB(0x8, 2);
    SCHED();
  }
}

DI void qkv_stage(LDSP char* sb, const s16x8 (&xf)[36], f32x16& qa, const s16x8& sel, f32x4& cs) {
  asm volatile("" : "+v"(sb));
  s16x8 a[3];
  a[0] = tr_frag(sb, 256);
  a[1] = tr_frag(sb + 1024, 256);
#pragma unroll
  for (int s = 0; s < 36; ++s) {
    if (s + 2 < 36) a[(s + 2) % 3] = tr_frag(sb + (s + 2) * 1024, 256);
    qa = MFMA32(a[s % 3], xf[s], qa);
    colsum_frag(a[s % 3], sel, cs);
    SCHED();
  }
}

__global__ __launch_bounds__(512, 2)
void k1(const u16* __restrict__ x, const u16* __restrict__ wq, const u16* __restrict__ wk, const u16* __restrict__ wv,
        const u16* __restrict__ wo, const u16* __restrict__ w1, const u16* __restrict__ b1, const u16* __restrict__ w2,
        char* __restrict__ ws) {
  extern __shared__ __attribute__((aligned(16))) char smem_[];
  LDSP char* smem = (LDSP char*)smem_;
  const int tid = threadIdx.x, lane = tid & 63;
  const int w8 = __builtin_amdgcn_readfirstlane(tid >> 6);
  const int w = w8 & 3, grp = w8 >> 2;
  const int b = blockIdx.x, xcd = b & 7, idx = b >> 3;
  const int tg = (xcd >> 1) * 4 + (idx & 3), j = (xcd & 1) * 8 + (idx >> 2);
  const int t32 = lane & 31, hh = lane >> 5;
  const int tt = tg * 4 + w;
  const bool heavy = (j & 3) == 0;
  constexpr int rk = 0, rc = 0;

  const char* w1w = (const char*)w1 + (size_t)(48 * w) * 9216 + (size_t)j * 576;
  unsigned off3[3];
#pragma unroll
  for (int i = 0; i < 3; ++i) {
    const unsigned bb = 1024 * i + 16 * lane;
    off3[i] = (bb / 192) * 9216 + (bb % 192);
  }
  auto issue_w1_rows = [&](int st, int r) {
    const int c = st / 3, kt = st - 3 * c;
    const char* g = w1w + ((c + rc) % 3) * 192 + (size_t)((kt + rk) % 3) * 192 * 9216 + r * 16 * 9216;
    LDSP char* l = smem + w_slot(st) * K1_SLOT + w * 9216 + r * 3072;
#pragma unroll
    for (int i = 0; i < 3; ++i) glds16(g, off3[i], l + i * 1024);
  };
  auto issue_w1 = [&](int st) {
#pragma unroll
    for (int r = 0; r < 3; ++r) issue_w1_rows(st, r);
  };
  auto issue_x = [&](int q, int slot) {
    int ln = tid & 63; asm volatile("" : "+v"(ln));
    const char* g = (const char*)x + (size_t)(tt * 32) * 1152;
    LDSP char* l = smem + slot * K1_SLOT + w * 9216;
    unsigned row = (unsigned)ln / 18, pos = (unsigned)ln % 18;
    const unsigned b0 = q * 288 + rk * 384;
#pragma unroll
    for (int i = 0; i < 9; ++i) {
      unsigned cb = b0 + ((pos ^ ((row >> 3) & 1)) << 4);
      cb = cb >= 1152 ? cb - 1152 : cb;
      glds16(g, row * 1152 + cb, l + i * 1024);
      pos += 10; row += 3;
      if (pos >= 18) { pos -= 18; row += 1; }
    }
  };
  auto issue_qkv = [&](int tile, int slot, int part) {
    int ln = tid & 63; asm volatile("" : "+v"(ln));
    LDSP char* l = smem + slot * K1_SLOT + w * 9216 + part * 3072;
    const int row0 = 144 * w + 48 * part + 192 * rk;
    if (tile < 18) {
      const char* g = (const char*)wq + tile * 64;
      const unsigned vo = (ln >> 2) * 1152 + (ln & 3) * 16;
#pragma unroll
      for (int i = 0; i < 3; ++i) glds16(g + (size_t)((row0 + 16 * i) % 576) * 1152, vo, l + i * 1024);
    } else {
      const u16* src = (tile == 18) ? wk : wv;
      int dw = ln & 15; dw = dw > 8 ? 8 : dw;
      const unsigned vo = (ln >> 4) * 36 + dw * 4;
#pragma unroll
      for (int i = 0; i < 12; ++i) glds4((const char*)src + ((row0 + 4 * i) % 576) * 36, vo, l + i * 256);
    }
  };
  const int lw = (tg * 12 + (j - (j >> 2) - 1)) * 4 + w;
  auto issue_unit = [&](int part) {
    if (lw < 648) {
      int ln = tid & 63; asm volatile("" : "+v"(ln));
      LDSP char* l = smem + 0 * K1_SLOT + w * 9216 + part * 3072;
      const int u = lw - 576, ks = u >> 1;
      const bool plain = ks < 32;
      const char* g = lw < 576 ? (const char*)w2 + (size_t)((lw >> 1) * 16) * 1152 + (lw & 1) * 576
                               : (const char*)wo + (size_t)(plain ? ks * 18 : 144 * (ks - 32) + 16) * 1152 + (u & 1) * 576;
      const unsigned p0 = 192 * part + ln;
      unsigned row = p0 / 36, pos = p0 % 36;
#pragma unroll
      for (int i = 0; i < 3; ++i) {
        const unsigned srow = plain ? row : 18 * (row >> 1) + (row & 1);
        glds16(g, srow * 1152 + pos * 16, l + i * 1024);
        pos += 28; row += 1;
        if (pos >= 36) { pos -= 36; row += 1; }
      }
    }
  };

  unsigned bias_raw = 0, bias_raw2 = 0;
  if (grp == 1) {
    asm volatile("global_load_ushort %0, %2, %4\n\tglobal_load_ushort %1, %3, %4" : "=&v"(bias_raw), "=&v"(bias_raw2)
                 : "v"((unsigned)((tid - 256) * 2)), "v"((unsigned)((tid < 288 ? tid : 287) * 2)), "s"(b1 + j * 288) : "memory");
  }
  if (grp == 0) { issue_x(0, 3); issue_x(1, 1); }
  else          { issue_w1(0);   issue_w1(1); }
  s16x8 xf[36];
  const int xl = w * 9216 + t32 * 288 + ((hh ^ ((t32 >> 3) & 1)) << 4);
  auto read_x = [&](int q, int slot) {
#pragma unroll
    for (int s = 0; s < 9; ++s) xf[9 * q + s] = *(LDSP s16x8*)(smem + slot * K1_SLOT + xl + s * 32);
  };
  if (grp == 0) WAIT_VM(9);
  BAR();
  read_x(0, 3);
  if (grp == 0) { WAIT_VM(0); }
  else {
    asm volatile("s_waitcnt vmcnt(9)" : "+v"(bias_raw), "+v"(bias_raw2) :: "memory");
    *(LDSP float*)(smem + K1_BIAS + (tid - 256) * 4) = __builtin_bit_cast(float, bias_raw << 16);
    if (tid < 288) *(LDSP float*)(smem + K1_BIAS + tid * 4) = __builtin_bit_cast(float, bias_raw2 << 16);
  }
  WAIT_LGKM0(); BAR();
  read_x(1, 1);
  WAIT_LGKM0(); BAR();

  const int q4 = (lane & 15) >> 2, p4 = lane & 3, g16 = (lane >> 4) & 1;
  const int rot = (w == 3) ? 0 : w;
  const int tl0 = rot, tl1 = (rot + 1) % 3, tl2 = (rot + 2) % 3;
  const int lo_w1 = (8 * hh + q4) * 192 + 32 * g16 + 8 * p4;
  const int lo_q  = (8 * hh + q4) * 64 + 32 * g16 + 8 * p4;
  char* hout = ws + OFF_H + ((size_t)tt * 288 + (size_t)j * 18) * 1024 + lane * 16;
  f32x16 acc[3];
  float negm = 0.f, r1 = 0.f, rm = 0.f;
  const unsigned selv = ((lane & 15) == ((lane >> 4) & 1)) ? 0x3f803f80u : 0u;
  const u32x4 selu = {selv, selv, selv, selv};
  const s16x8 sel = __builtin_bit_cast(s16x8, selu);

  auto load_stats = [&]() {
    f32x2 st = *(LDSP f32x2*)(smem + K1_STAT + (w * 32 + t32) * 8);
    negm = -st[0] * (1.0f / 576.0f);
    const float var = __builtin_fmaxf(st[1] * (1.0f / 576.0f) - negm * negm, 0.f);
    r1 = __builtin_amdgcn_rsqf(var + 1e-5f);
    rm = r1 * __builtin_amdgcn_rsqf(var * r1 * r1 + 1e-5f);
  };
  auto mfma_chunk = [&](auto cc) {
    constexpr int c = decltype(cc)::value;
    __builtin_amdgcn_s_setprio(0);
    f32x4 cs = {0.f, 0.f, 0.f, 0.f};
#pragma unroll
    for (int t = 0; t < 3; ++t)
#pragma unroll
      for (int i = 0; i < 16; ++i) acc[t][i] = 0.f;
    LDSP char* s0 = smem + lo_w1 + tl0 * 64;
    LDSP char* s1 = smem + lo_w1 + tl1 * 64;
    LDSP char* s2 = smem + lo_w1 + tl2 * 64;
    constexpr int o0 = w_slot(3 * c) * K1_SLOT, o1 = w_slot(3 * c + 1) * K1_SLOT, o2 = w_slot(3 * c + 2) * K1_SLOT;
    w1_stage<0>(s0 + o0, s1 + o0, s2 + o0, xf, acc, sel, cs);
    if (c == 1) { WAIT_VM(9); } else if (c == 2) { WAIT_VM(11); }
    WAIT_LGKM0(); BAR();
    if (c == 0) { read_x(2, 3); WAIT_LGKM0(); BAR(); }
    w1_stage<1>(s0 + o1, s1 + o1, s2 + o1, xf, acc, sel, cs);
    if (c != 0) WAIT_VM(0);
    WAIT_LGKM0(); BAR();
    if (c == 0) { read_x(3, 0); WAIT_LGKM0(); BAR(); }
    w1_stage<2>(s0 + o2, s1 + o2, s2 + o2, xf, acc, sel, cs);
    if (lane < 16 && w < 3) {
      *(LDSP float*)(smem + K1_CS + (c * 96 + tl0 * 32 + lane) * 4) = cs[0];
      *(LDSP float*)(smem + K1_CS + (c * 96 + tl0 * 32 + 16 + lane) * 4) = cs[1];
    }
    WAIT_LGKM0(); BAR();
    __builtin_amdgcn_s_setprio(1);
  };
  auto gelu_t = [&](int c, int t, int tl, int st_issue) {
    const int ca = (c + rc) % 3;
    gelu_tile(acc[t], smem + K1_CS + (c * 96 + tl * 32) * 4 + hh * 16, smem + K1_BIAS + (ca * 96 + tl * 32) * 4 + hh * 16,
              hout + (ca * 6 + tl * 2) * 1024, negm, rm, [&](int i) {
      if (i < 3) {
        if (st_issue >= 0 && st_issue < 9) issue_w1_rows(st_issue, i);
        else if (st_issue == 9) issue_qkv(j, 1, i);
        else if (st_issue == 10) { if (heavy) issue_qkv(16 + (j >> 2), 0, i); else issue_unit(i); }
      }
    });
  };

  if (grp == 0) {
    mfma_chunk(std::integral_constant<int, 0>{});
    load_stats();
    gelu_t(0, 0, tl0, 6); WAIT_LGKM0(); BAR();
    gelu_t(0, 1, tl1, 7); WAIT_LGKM0(); BAR();
    gelu_t(0, 2, tl2, 8); WAIT_VM(22); WAIT_LGKM0(); BAR();
    mfma_chunk(std::integral_constant<int, 2>{});
    gelu_t(2, 0, tl0, -1); gelu_t(2, 1, tl1, -1); gelu_t(2, 2, tl2, -1);
  } else {
    f32x4 rs = {0.f, 0.f, 0.f, 0.f};
#pragma unroll
    for (int i = 0; i < 16; ++i) acc[0][i] = 0.f;
    auto stats_frag = [&](int s) {
      colsum_frag(xf[s], sel, rs);
      acc[0] = MFMA32(xf[s], xf[s], acc[0]);
    };
    __builtin_amdgcn_s_setprio(1);
    issue_x(2, 3);
#pragma unroll
    for (int s = 0; s < 18; ++s) { if (s % 6 == 0) issue_w1_rows(2, s / 6); stats_frag(s); }
    WAIT_VM(9); BAR();
    read_x(2, 3); WAIT_LGKM0(); BAR();
    issue_x(3, 0);
#pragma unroll
    for (int s = 18; s < 27; ++s) { if (s % 3 == 0) issue_w1_rows(3, (s - 18) / 3); stats_frag(s); }
    WAIT_VM(9); BAR();
    read_x(3, 0); WAIT_LGKM0(); BAR();
    issue_w1(4);
#pragma unroll
    for (int s = 27; s < 36; ++s) { if (s % 3 == 0) issue_w1_rows(5, (s - 27) / 3); stats_frag(s); }
    {
      const int dreg = (t32 & 3) + 4 * (t32 >> 3);
      float s2 = 0.f;
#pragma unroll
      for (int i = 0; i < 16; ++i) s2 = (i == dreg) ? acc[0][i] : s2;
      if (hh == ((t32 >> 2) & 1)) *(LDSP float*)(smem + K1_STAT + (w * 32 + t32) * 8 + 4) = s2;
      if (lane < 16) {
        *(LDSP float*)(smem + K1_STAT + (w * 32 + lane) * 8) = rs[0];
        *(LDSP float*)(smem + K1_STAT + (w * 32 + 16 + lane) * 8) = rs[1];
      }
    }
    WAIT_VM(18); WAIT_LGKM0(); BAR();
    mfma_chunk(std::integral_constant<int, 1>{});
    load_stats();
    gelu_t(1, 0, tl0, 10); WAIT_LGKM0(); BAR();
    gelu_t(1, 1, tl1, 9); WAIT_LGKM0(); BAR();
    gelu_t(1, 2, tl2, -1);
    WAIT_VM(2); WAIT_LGKM0(); BAR();
    __builtin_amdgcn_s_setprio(2);

    auto qkv_tile = [&](int tile, int slot) {
      f32x16 qa;
#pragma unroll
      for (int i = 0; i < 16; ++i) qa[i] = 0.f;
      f32x4 cs = {0.f, 0.f, 0.f, 0.f};
      qkv_stage(smem + slot * K1_SLOT + lo_q, xf, qa, sel, cs);
      int ln = tid & 63; asm volatile("" : "+v"(ln));
      const int t32 = ln & 31, hh = ln >> 5, token = tt * 32 + t32;
      LDSP char* cp = smem + K1_CSQ + (w * 2 + (slot & 1)) * 128;
      if (ln < 16) { *(LDSP float*)(cp + ln * 4) = cs[0]; *(LDSP float*)(cp + 64 + ln * 4) = cs[1]; }
      WAIT_LGKM0();
#pragma unroll
      for (int g = 0; g < 4; ++g) {
        f32x4 c4 = *(LDSP f32x4*)(cp + g * 32 + hh * 16);
#pragma unroll
        for (int i = 0; i < 4; ++i) qa[4 * g + i] = __builtin_fmaf(negm, c4[i], qa[4 * g + i]);
      }
      if (tile < 18) {
        const float sc = r1 * (0.23570226039551584f * 1.4426950408889634f);
        char* qp = ws + OFF_Q + (size_t)tt * 36864;
#pragma unroll
        for (int i = 0; i < 8; ++i) {
          const unsigned n = tile * 32 + 8 * (i >> 1) + 4 * hh + 2 * (i & 1);
          const unsigned head = n / 18, d = n - head * 18;
          const unsigned off = d < 16 ? (t32 + 32 * ((d >> 2) & 1)) * 16 + ((d >> 3) * 2 + ((d >> 1) & 1)) * 4 : 1024 + t32 * 4;
          *(unsigned*)(qp + head * 1152 + off) = pack2(qa[2 * i] * sc, qa[2 * i + 1] * sc);
        }
      } else if (tile == 18) {
        u32x4 o = {pack2(qa[0] * r1, qa[1] * r1), pack2(qa[2] * r1, qa[3] * r1),
                   pack2(qa[4] * r1, qa[5] * r1), pack2(qa[6] * r1, qa[7] * r1)};
        char* gp = ws + OFF_KV + (size_t)tg * KV_GRP;
        *(u32x4*)(gp + 1024 + w * KV_TILE + ln * 16) = o;
        if (hh == 0) *(unsigned*)(gp + (w >> 1) * 512 + t32 * 16 + (w & 1) * 4) = pack2(qa[8] * r1, qa[9] * r1);
        else if ((w & 1) == 0) {
          u32x2 cst = {w == 0 ? 0x3f803f80u : 0u, 0u};
          *(u32x2*)(gp + (w >> 1) * 512 + t32 * 16 + 8) = cst;
        }
      } else {
        const int s = t32 >> 4, hk = (t32 >> 2) & 1, jj = ((t32 >> 3) & 1) * 4 + (t32 & 3);
        char* vp = ws + OFF_KV + (size_t)tg * KV_GRP + 2048 + w * KV_TILE + (s * 2 + hk) * 304 + jj * 2;
#pragma unroll
        for (int i = 0; i < 8; ++i) {
          const int d = (i & 3) + 8 * (i >> 2) + 4 * hh;
          *(u16*)(vp + d * 16) = (u16)(pack2(qa[i] * r1, 0.f) & 0xffffu);
        }
        if (hh == 0) {
          *(u16*)(vp + 16 * 16) = (u16)(pack2(qa[8] * r1, 0.f) & 0xffffu);
          *(u16*)(vp + 17 * 16) = (u16)(pack2(qa[9] * r1, 0.f) & 0xffffu);
          *(u16*)(vp + 18 * 16) = (u16)0x3f80;
        }
      }
    };
    qkv_tile(j, 1);
    if (heavy) {
      qkv_tile(16 + (j >> 2), 0);
    } else if (lw < 648) {
      int ln = tid & 63; asm volatile("" : "+v"(ln));
      const int hh = ln >> 5, q4 = (ln & 15) >> 2, p4 = ln & 3, g16 = (ln >> 4) & 1;
      LDSP char* sb = smem + 0 * K1_SLOT + w * 9216 + (4 * hh + q4) * 576 + 32 * g16 + 8 * p4;
      char* dst = (lw < 576) ? ws + OFF_W2T + ((size_t)(lw >> 1) * 18 + (lw & 1) * 9) * 1024
                             : ws + OFF_WOT + ((size_t)((lw - 576) >> 1) * 18 + (lw & 1) * 9) * 1024;
#pragma unroll
      for (int n = 0; n < 9; ++n) {
        s16x8 f = tr_frag(sb + n * 64, 8 * 576);
        store16(dst + n * 1024 + ln * 16, __builtin_bit_cast(u32x4, f));
      }
    }
  }
}

constexpr int K2_FLAG = KV_BYTES;
constexpr int K2_LDS  = KV_BYTES + 128;
enum { FL_KVA = 0, FL_KVG = 1, FL_PROG = 2, FL_GW = 3, FL_DONE = 4, FL_SENT = 5, FL_GW2 = 6 };

#define EXP2(x) __builtin_amdgcn_exp2f(x)

DI void flag_set(LDSP char* smem, int set, int i, int lane) {
  if (lane == 0) *(volatile LDSP unsigned*)(smem + K2_FLAG + set * 16 + i * 4) = 1u;
}
DI void flag_wait_all(LDSP char* smem, int set) {
  while (true) {
    u32x4 f = *(volatile LDSP u32x4*)(smem + K2_FLAG + set * 16);
    if (f[0] & f[1] & f[2] & f[3]) break;
    __builtin_amdgcn_s_sleep(2);
  }
  asm volatile("" ::: "memory");
}
DI void flag_wait_one(LDSP char* smem, int set, int i) {
  while (*(volatile LDSP unsigned*)(smem + K2_FLAG + set * 16 + i * 4) == 0u) __builtin_amdgcn_s_sleep(1);
  asm volatile("" ::: "memory");
}

DI void attn_step(f32x16& scur, f32x16& snext, const s16x8& k0, const s16x8& k1, const s16x8& qn0, const s16x8& qn1,
                  const s16x8& vprev1, f32x16& oprev, const s16x8& vcur0, f32x16& ocur, u32x4& p0, u32x4& p1) {
  f32x16 zero;
#pragma unroll
  for (int i = 0; i < 16; ++i) zero[i] = 0.f;
#define EX(i) do { float t_ = scur[i]; asm volatile("v_exp_f32 %0, %0" : "+v"(t_)); scur[i] = t_; } while (0)
  p1[2] = pack2(snext[12], snext[13]);
  p1[3] = pack2(snext[14], snext[15]);
  SCHED();
  snext = MFMA32(k0, qn0, zero);
  SCHED();
#pragma unroll
  for (int i = 0; i < 3; ++i) EX(i);
  SCHED();
  snext = MFMA32(k1, qn1, snext);
  SCHED();
#pragma unroll
  for (int i = 3; i < 8; ++i) EX(i);
  SCHED();
  p0[0] = pack2(scur[0], scur[1]);
  p0[1] = pack2(scur[2], scur[3]);
  p0[2] = pack2(scur[4], scur[5]);
  p0[3] = pack2(scur[6], scur[7]);
  SCHED();
  oprev = MFMA32(vprev1, __builtin_bit_cast(s16x8, p1), oprev);
  SCHED();
#pragma unroll
  for (int i = 8; i < 8 + 3; ++i) EX(i);
  SCHED();
  ocur = MFMA32(vcur0, __builtin_bit_cast(s16x8, p0), ocur);
  SCHED();
#pragma unroll
  for (int i = 8 + 3; i < 16; ++i) EX(i);
  SCHED();
  p1[0] = pack2(scur[8], scur[9]);
  p1[1] = pack2(scur[10], scur[11]);
  SCHED();
#undef EX
}

DI s16x8 make_q1(unsigned qe, unsigned mneg, int i, int hh) {
  const unsigned a = (hh == (i >> 1)) ? qe : 0u;
  u32x4 r = {(i & 1) ? 0u : a, (i & 1) ? a : 0u, hh ? 0u : mneg, 0u};
  return __builtin_bit_cast(s16x8, r);
}
DI unsigned neg_hi_lo(float m) {
  unsigned hi = pack2(-m, 0.f) & 0xffffu;
  float rem = -m - __builtin_bit_cast(float, hi << 16);
  unsigned lo = pack2(rem, 0.f) & 0xffffu;
  return hi | (lo << 16);
}
DI int v_row(int t32) { return t32 > 18 ? ((t32 >= 20 && t32 < 28) ? 0 : 4) : t32; }

DI f32x16 attn_exact(const char* kv, int lane, u32x4 q0u, unsigned qe) {
  const int t32 = lane & 31, hh = lane >> 5;
  const s16x8 q0 = __builtin_bit_cast(s16x8, q0u);
  f32x16 zero, o;
#pragma unroll
  for (int i = 0; i < 16; ++i) { zero[i] = 0.f; o[i] = 0.f; }
  float m = -INFINITY;
#pragma unroll 1
  for (int T = 0; T < 64; ++T) {
    const char* gp = kv + (T >> 2) * KV_GRP;
    s16x8 k0 = *(const s16x8*)(gp + 1024 + (T & 3) * KV_TILE + lane * 16);
    s16x8 k1 = *(const s16x8*)(gp + lane * 16);
    f32x16 s = MFMA32(k0, q0, zero);
    s = MFMA32(k1, make_q1(qe, 0u, T & 3, hh), s);
#pragma unroll
    for (int i = 0; i < 16; ++i) m = __builtin_fmaxf(m, s[i]);
  }
  m = __builtin_fmaxf(m, other_half(m));
  const unsigned mneg = neg_hi_lo(m);
#pragma unroll 1
  for (int T = 0; T < 64; ++T) {
    const char* gp = kv + (T >> 2) * KV_GRP;
    const char* tp = gp + 1024 + (T & 3) * KV_TILE;
    s16x8 k0 = *(const s16x8*)(tp + lane * 16);
    s16x8 k1 = *(const s16x8*)(gp + lane * 16);
    s16x8 v0 = *(const s16x8*)(tp + 1024 + hh * 304 + v_row(t32) * 16);
    s16x8 v1 = *(const s16x8*)(tp + 1024 + 608 + hh * 304 + v_row(t32) * 16);
    f32x16 s = MFMA32(k0, q0, zero);
    s = MFMA32(k1, make_q1(qe, mneg, T & 3, hh), s);
    u32x4 p0, p1;
#pragma unroll
    for (int i = 0; i < 4; ++i) {
      p0[i] = pack2(EXP2(s[2 * i]), EXP2(s[2 * i + 1]));
      p1[i] = pack2(EXP2(s[8 + 2 * i]), EXP2(s[8 + 2 * i + 1]));
    }
    o = MFMA32(v0, __builtin_bit_cast(s16x8, p0), o);
    o = MFMA32(v1, __builtin_bit_cast(s16x8, p1), o);
  }
  return o;
}

typedef __amdgpu_buffer_rsrc_t rsrc_t;
DI s16x8 bload(rsrc_t r, unsigned voff, unsigned soff) {
  return __builtin_bit_cast(s16x8, __builtin_amdgcn_raw_buffer_load_b128(r, voff, soff, 0));
}
template <int NT>
DI void gemm2_load(rsrc_t r, unsigned hp, unsigned wp, unsigned wsp, unsigned lo16, int k, s16x8 (&hb)[2], s16x8 (&wb)[5]) {
  hb[0] = bload(r, lo16, hp + k * 1024);
  wb[0] = bload(r, lo16, wp + k * 18432);
  hb[1] = bload(r, lo16, hp + k * 1024 + 288 * 1024);
#pragma unroll
  for (int n = 1; n < 4; ++n) wb[n] = bload(r, lo16 + n * 1024, wp + k * 18432);
  if (NT == 5) wb[4] = bload(r, lo16, wsp + k * 18432);
}
template <int NT, int NTN>
DI void gemm2_step(rsrc_t r, unsigned hp, unsigned wp, unsigned wsp, unsigned lo16, int kn, s16x8 (&hb)[2], s16x8 (&wb)[5],
                   f32x16 (&acc)[2][5]) {
#pragma unroll
  for (int n = 0; n < NT; ++n) {
    acc[0][n] = MFMA32(wb[n], hb[0], acc[0][n]);
    acc[1][n] = MFMA32(wb[n], hb[1], acc[1][n]);
    SCHED();
    if (n < 4) wb[n] = bload(r, lo16 + n * 1024, wp + kn * 18432);
    else if (NTN == 5) wb[4] = bload(r, lo16, wsp + kn * 18432);
    SCHED();
  }
  if (NT == 4 && NTN == 5) wb[4] = bload(r, lo16, wsp + kn * 18432);
  hb[0] = bload(r, lo16, hp + kn * 1024);
  hb[1] = bload(r, lo16, hp + kn * 1024 + 288 * 1024);
  SCHED();
}
template <int N1, int N2, typename F, typename G>
DI void gemm2_all(rsrc_t r, unsigned hp, unsigned wp, unsigned wsp, unsigned lo16, f32x16 (&acc)[2][5], F&& after_first_loads, G&& after_first_step) {
  s16x8 hA[2], wA[5], hB[2], wB[5];
  after_first_loads();
  gemm2_load<N1>(r, hp, wp, wsp, lo16, 0, hA, wA);
  gemm2_load<N1>(r, hp, wp, wsp, lo16, 1, hB, wB);
  SCHED();
  after_first_step();
#pragma unroll 1
  for (int k = 0; k < 16; k += 2) {
    gemm2_step<N1, N1>(r, hp, wp, wsp, lo16, k + 2, hA, wA, acc);
    gemm2_step<N1, N1>(r, hp, wp, wsp, lo16, k + 3, hB, wB, acc);
  }
  gemm2_step<N1, N2>(r, hp, wp, wsp, lo16, 18, hA, wA, acc);
  gemm2_step<N1, N2>(r, hp, wp, wsp, lo16, 19, hB, wB, acc);
#pragma unroll 1
  for (int k = 18; k < 36; k += 2) {
    gemm2_step<N2, N2>(r, hp, wp, wsp, lo16, (k + 2 < 36) ? k + 2 : 35, hA, wA, acc);
    gemm2_step<N2, N2>(r, hp, wp, wsp, lo16, (k + 3 < 36) ? k + 3 : 35, hB, wB, acc);
  }
}

__global__ __launch_bounds__(512, 2)
void k2(char* __restrict__ ws) {
  extern __shared__ __attribute__((aligned(16))) char smem_[];
  LDSP char* smem = (LDSP char*)smem_;
  const int tid = threadIdx.x, lane = tid & 63;
  const int w = __builtin_amdgcn_readfirstlane(tid >> 6);
  const int b = blockIdx.x;
  const int t32 = lane & 31, hh = lane >> 5;
  if (tid < 32) *(LDSP unsigned*)(smem + K2_FLAG + tid * 4) = 0u;
  const char* kvg = ws + OFF_KV;

  if (w < 4) {
    const int tt = b >> 2, hg = b & 3;
    const int rs = w & 1, kh2 = w >> 1;
    const int head0 = hg * 8 + rs * 4;
#pragma unroll
    for (int i = 0; i < 5; ++i) {
      glds16(kvg + (i * 4 + w) * 1024, lane * 16, smem + (i * 4 + w) * 1024);
      glds16(kvg + KV_HALF + (i * 4 + w) * 1024, lane * 16, smem + KV_HALF + (i * 4 + w) * 1024);
    }
    u32x4 q0u[4];
    unsigned qe[4];
#pragma unroll
    for (int qb = 0; qb < 4; ++qb) {
      const char* qp = ws + OFF_Q + (size_t)tt * 36864 + (head0 + qb) * 1152;
      q0u[qb] = *(const u32x4*)(qp + lane * 16);
      qe[qb] = *(const unsigned*)(qp + 1024 + t32 * 4);
    }
    WAIT_LGKM0(); BAR();
    WAIT_VM(0);
    flag_set(smem, FL_KVA, w, lane);
    flag_wait_all(smem, FL_KVA);
    s16x8 q0[4], q1[4][4];
    f32x16 o[4];
    f32x16 sa, sb;
#pragma unroll
    for (int i = 0; i < 16; ++i) sb[i] = 0.f;
    {
      s16x8 k0 = *(LDSP s16x8*)(smem + 1024 + lane * 16);
      s16x8 k1 = *(LDSP s16x8*)(smem + lane * 16);
#pragma unroll
      for (int qb = 0; qb < 4; ++qb) {
        asm volatile("" : "+v"(q0u[qb]), "+v"(qe[qb]));
        q0[qb] = __builtin_bit_cast(s16x8, q0u[qb]);
        f32x16 s = MFMA32(k0, q0[qb], sb);
        s = MFMA32(k1, make_q1(qe[qb], 0u, 0, hh), s);
        float m = s[0];
#pragma unroll
        for (int i = 1; i < 16; ++i) m = __builtin_fmaxf(m, s[i]);
        m = __builtin_fmaxf(m, other_half(m));
        const unsigned mneg = neg_hi_lo(m);
#pragma unroll
        for (int i = 0; i < 4; ++i) q1[i][qb] = make_q1(qe[qb], mneg, i, hh);
#pragma unroll
        for (int i = 0; i < 16; ++i) o[qb][i] = 0.f;
      }
    }
    LDSP char* kp = smem + kh2 * KV_HALF + lane * 16;
    LDSP char* vp = smem + kh2 * KV_HALF + 2048 + hh * 304 + v_row(t32) * 16;
    u32x4 p0 = {0u, 0u, 0u, 0u}, p1 = {0u, 0u, 0u, 0u};
    s16x8 k1c = *(LDSP s16x8*)(kp), k1n;
    s16x8 k0b[2], v0b[2], v1b[2];
    k0b[0] = *(LDSP s16x8*)(kp + 1024);
    const unsigned long long vmask = 0x0007ffff0007ffffull;
#pragma unroll
    for (int i = 0; i < 2; ++i) { v0b[i] = s16x8{0, 0, 0, 0, 0, 0, 0, 0}; v1b[i] = s16x8{0, 0, 0, 0, 0, 0, 0, 0}; }
#define VLD(dst, addr, off) asm volatile("s_mov_b64 exec, %2\n\tds_read_b128 %0, %1 offset:%3\n\ts_mov_b64 exec, -1" \
                                         : "+v"(dst) : "v"(addr), "s"(vmask), "i"(off) : "memory")
#define VWAIT(dst) asm volatile("s_waitcnt lgkmcnt(0)" : "+v"(dst) :: "memory")
    VLD(v0b[0], vp, 0);
    sa = MFMA32(k0b[0], q0[0], sb);
    sa = MFMA32(k1c, q1[0][0], sa);
    asm volatile("s_nop 15" : "+v"(sa));
#pragma unroll 1
    for (int g = 0; g < 8; ++g) {
      if (g == 1) flag_wait_all(smem, FL_KVG);
      if (g == 5) flag_set(smem, FL_PROG, w, lane);
      const int nxg = (g < 7) ? KV_GRP : 0;
#pragma unroll
      for (int i = 0; i < 4; ++i) {
        const int c = i & 1, n = c ^ 1;
        const int nt_off = (i < 3) ? (i + 1) * KV_TILE : nxg;
        VWAIT(v0b[c]);
        VLD(v1b[c], vp, i * KV_TILE + 608);
        SCHED();
        attn_step(sa, sb, k0b[c], k1c, q0[1], q1[i][1], v1b[n], o[3], v0b[c], o[0], p0, p1);
        VWAIT(v1b[c]);
        k0b[n] = *(LDSP s16x8*)(kp + 1024 + nt_off);
        if (i == 3) k1n = *(LDSP s16x8*)(kp + nxg);
        SCHED();
        attn_step(sb, sa, k0b[c], k1c, q0[2], q1[i][2], v1b[c], o[0], v0b[c], o[1], p0, p1);
        if (i < 3) VLD(v0b[n], vp, (i + 1) * KV_TILE); else { LDSP char* vn = vp + nxg; VLD(v0b[n], vn, 0); }
        SCHED();
        attn_step(sa, sb, k0b[c], k1c, q0[3], q1[i][3], v1b[c], o[1], v0b[c], o[2], p0, p1);
        attn_step(sb, sa, k0b[n], (i == 3) ? k1n : k1c, q0[0], q1[(i + 1) & 3][0], v1b[c], o[2], v0b[c], o[3], p0, p1);
      }
      k1c = k1n;
      kp += KV_GRP; vp += KV_GRP;
    }
    p1[2] = pack2(sb[12], sb[13]);
    p1[3] = pack2(sb[14], sb[15]);
    o[3] = MFMA32(v1b[1], __builtin_bit_cast(s16x8, p1), o[3]);
    f32x16 keep[2], send[2];
    u32x4 qk0[2];
    unsigned qke[2];
#pragma unroll
    for (int q = 0; q < 2; ++q) {
#pragma unroll
      for (int i = 0; i < 11; ++i) {
        keep[q][i] = kh2 ? o[2 + q][i] : o[q][i];
        send[q][i] = kh2 ? o[q][i] : o[2 + q][i];
      }
#pragma unroll
      for (int i = 11; i < 16; ++i) { keep[q][i] = 0.f; send[q][i] = 0.f; }
#pragma unroll
      for (int i = 0; i < 4; ++i) qk0[q][i] = kh2 ? q0u[2 + q][i] : q0u[q][i];
      { unsigned a_ = qe[q], b_ = qe[2 + q]; asm volatile("" : "+v"(a_), "+v"(b_)); qke[q] = kh2 ? b_ : a_; }
    }
    WAIT_LGKM0();
    flag_set(smem, FL_DONE, w, lane);
    flag_wait_one(smem, FL_DONE, w ^ 1);
    {
      LDSP char* xp = smem + kh2 * KV_HALF + 7 * KV_GRP + rs * 4864;
#pragma unroll
      for (int q = 0; q < 2; ++q) {
        const f32x16 ov = send[q];
        *(LDSP f32x4*)(xp + q * 2432 + lane * 16) = f32x4{ov[0], ov[1], ov[2], ov[3]};
        *(LDSP f32x4*)(xp + q * 2432 + 1024 + lane * 16) = f32x4{ov[4], ov[5], ov[6], ov[7]};
        if (hh == 0) {
          *(LDSP float*)(xp + q * 2432 + 2048 + t32 * 4) = ov[8];
          *(LDSP float*)(xp + q * 2432 + 2176 + t32 * 4) = ov[9];
          *(LDSP float*)(xp + q * 2432 + 2304 + t32 * 4) = ov[10];
        }
      }
    }
    WAIT_LGKM0();
    flag_set(smem, FL_SENT, w, lane);
    flag_wait_one(smem, FL_SENT, w ^ 2);
    bool bad = false;
#pragma unroll
    for (int q = 0; q < 2; ++q) {
      LDSP char* xp = smem + (kh2 ^ 1) * KV_HALF + 7 * KV_GRP + rs * 4864 + q * 2432;
      f32x4 a0 = *(LDSP f32x4*)(xp + lane * 16), a1 = *(LDSP f32x4*)(xp + 1024 + lane * 16);
#pragma unroll
      for (int i = 0; i < 4; ++i) { keep[q][i] += a0[i]; keep[q][4 + i] += a1[i]; }
      keep[q][8] += *(LDSP float*)(xp + 2048 + t32 * 4);
      keep[q][9] += *(LDSP float*)(xp + 2176 + t32 * 4);
      keep[q][10] += *(LDSP float*)(xp + 2304 + t32 * 4);
      float mag = 0.f;
#pragma unroll
      for (int i = 0; i < 8; ++i) mag += __builtin_fabsf(keep[q][i]);
      const float ext = __builtin_fabsf(keep[q][8]) + __builtin_fabsf(keep[q][9]) + 1e12f * __builtin_fabsf(keep[q][10]);
      mag += hh ? 0.f : ext;
      bad = bad || !(mag < 1e30f);
    }
    if (__builtin_expect(__any(bad), 0)) {
#pragma unroll 1
      for (int q = 0; q < 2; ++q) {
        f32x16 ex = attn_exact(kvg, lane, q ? qk0[1] : qk0[0], q ? qke[1] : qke[0]);
        if (q) keep[1] = ex; else keep[0] = ex;
      }
    }
#pragma unroll
    for (int q = 0; q < 2; ++q) {
      float l = keep[q][10];
      const float lo_ = other_half(l);
      const float li = __builtin_amdgcn_rcpf(hh ? lo_ : l);
      const int head = head0 + 2 * kh2 + q;
      u32x4 o4 = {pack2(keep[q][0] * li, keep[q][1] * li), pack2(keep[q][2] * li, keep[q][3] * li),
                  pack2(keep[q][4] * li, keep[q][5] * li), pack2(keep[q][6] * li, keep[q][7] * li)};
      char* ap = ws + OFF_AO + (size_t)tt * 36 * 1024;
      store16(ap + head * 1024 + lane * 16, o4);
      if (hh == 0) {
        const int h8 = head & 7;
        *(unsigned*)(ap + (32 + (head >> 3)) * 1024 + (t32 + 32 * ((h8 >> 1) & 1)) * 16 + ((h8 >> 2) * 2 + (h8 & 1)) * 4) =
            pack2(keep[q][8] * li, keep[q][9] * li);
      }
    }
  } else {
    const int g = w - 4;
    const int xcd = b & 7, idx = b >> 3;
    const int kh = xcd & 1, tp = (xcd >> 1) * 8 + (idx & 7), ng = idx >> 3;
    const int nt0 = (ng >> 1) * 9 + (ng & 1) * 5;
    const int nts = (ng >> 1) * 9 + 4;
    const int fb0 = kh * 144 + g * 36;
    const rsrc_t rsrc = __builtin_amdgcn_make_buffer_rsrc(ws, 0, (unsigned)OFF_WOT, 0x00020000);
    const unsigned hp0 = (unsigned)OFF_H + ((2 * tp) * 288 + fb0) * 1024;
    const unsigned wp = (unsigned)OFF_W2T + (fb0 * 18 + nt0) * 1024;
    const unsigned wsp = (unsigned)OFF_W2T + (fb0 * 18 + nts) * 1024;
    const unsigned lo16 = lane * 16;
    BAR();
    f32x16 acc[2][5];
#pragma unroll
    for (int a = 0; a < 2; ++a)
#pragma unroll
      for (int n = 0; n < 5; ++n)
#pragma unroll
        for (int i = 0; i < 16; ++i) acc[a][n][i] = 0.f;
    auto stage_kv = [&]() {
      __builtin_amdgcn_s_setprio(3);
#pragma unroll 1
      for (int i = g; i < 58; i += 4) {
        glds16(kvg + (20 + i) * 1024, lo16, smem + (20 + i) * 1024);
        glds16(kvg + KV_HALF + (20 + i) * 1024, lo16, smem + KV_HALF + (20 + i) * 1024);
      }
      __builtin_amdgcn_s_setprio(0);
    };
    auto kv_ready = [&]() {
      WAIT_VM(12);
      flag_set(smem, FL_KVG, g, lane);
    };
    if (ng & 1) gemm2_all<4, 5>(rsrc, hp0, wp, wsp, lo16, acc, stage_kv, kv_ready);
    else        gemm2_all<5, 4>(rsrc, hp0, wp, wsp, lo16, acc, stage_kv, kv_ready);
    flag_wait_all(smem, FL_PROG);
    LDSP char* mine   = smem + (g >> 1) * KV_HALF + (g & 1) * 24576 + lo16;
    LDSP char* theirs = smem + (g >> 1) * KV_HALF + ((g & 1) ^ 1) * 24576 + lo16;
    LDSP char* other  = smem + ((g >> 1) ^ 1) * KV_HALF + ((g & 1) ^ 1) * 24576 + lo16;
    auto reduce = [&](auto aa, auto hh_) {
      constexpr int A = decltype(aa)::value, HF = decltype(hh_)::value;
#pragma unroll
      for (int n = 0; n < 5; ++n)
#pragma unroll
        for (int q = 0; q < 4; ++q)
          *(LDSP f32x4*)(mine + (n * 4 + q) * 1024) =
              f32x4{acc[A ^ 1][n][4 * q], acc[A ^ 1][n][4 * q + 1], acc[A ^ 1][n][4 * q + 2], acc[A ^ 1][n][4 * q + 3]};
      WAIT_LGKM0();
      flag_set(smem, FL_GW, g, lane);
      flag_wait_one(smem, FL_GW, g ^ 1);
#pragma unroll
      for (int n = 0; n < 5; ++n)
#pragma unroll
        for (int q = 0; q < 4; ++q) {
          f32x4 v = *(LDSP f32x4*)(theirs + (n * 4 + q) * 1024);
#pragma unroll
          for (int i = 0; i < 4; ++i) acc[A][n][4 * q + i] += v[i];
        }
      WAIT_LGKM0();
#pragma unroll
      for (int n = 0; n < 5; ++n)
#pragma unroll
        for (int q = 0; q < 2; ++q) {
          constexpr int r0 = 8 * (HF ^ 1);
          *(LDSP f32x4*)(theirs + (n * 2 + q) * 1024) =
              f32x4{acc[A][n][r0 + 4 * q], acc[A][n][r0 + 4 * q + 1], acc[A][n][r0 + 4 * q + 2], acc[A][n][r0 + 4 * q + 3]};
        }
      WAIT_LGKM0();
      flag_set(smem, FL_GW2, g, lane);
      flag_wait_one(smem, FL_GW2, g ^ 2);
#pragma unroll
      for (int n = 0; n < 5; ++n) {
        const int nt = (n < 4) ? nt0 + n : nts;
        const int slot = (n < 4) ? kh : kh + 2 * (ng & 1);
        char* pp = ws + OFF_PART + (((size_t)slot * 64 + (2 * tp + A)) * 18 + nt) * 2048 + HF * 1024 + lo16;
        u32x4 o4;
#pragma unroll
        for (int q = 0; q < 2; ++q) {
          f32x4 v = *(LDSP f32x4*)(other + (n * 2 + q) * 1024);
#pragma unroll
          for (int i = 0; i < 4; ++i) v[i] += acc[A][n][8 * HF + 4 * q + i];
          o4[2 * q] = pack2(v[0], v[1]);
          o4[2 * q + 1] = pack2(v[2], v[3]);
        }
        store16(pp, o4);
      }
    };
    using I0 = std::integral_constant<int, 0>;
    using I1 = std::integral_constant<int, 1>;
    if (g == 0) reduce(I0{}, I0{});
    else if (g == 1) reduce(I1{}, I0{});
    else if (g == 2) reduce(I0{}, I1{});
    else reduce(I1{}, I1{});
  }
}

constexpr int K3_LDS = 6 * 6 * 4096;
__global__ __launch_bounds__(384)
void k3(const u16* __restrict__ x, const u16* __restrict__ b2, char* __restrict__ ws, u16* __restrict__ out) {
  extern __shared__ __attribute__((aligned(16))) char smem_[];
  LDSP char* smem = (LDSP char*)smem_;
  const int tid = threadIdx.x, lane = tid & 63;
  const int w = __builtin_amdgcn_readfirstlane(tid >> 6);
  const int b = blockIdx.x;
  const int tp = (b & 7) * 4 + (b >> 3) / 6, ng = (b >> 3) % 6;
  const int t32 = lane & 31, hh = lane >> 5;
  const int tt = 2 * tp + w / 3, nt = 3 * ng + w % 3;
  const int token = tt * 32 + t32;
  const char* ap = ws + OFF_AO + ((size_t)(2 * tp) * 36 + w * 6) * 1024 + lane * 16;
  const char* wp = ws + OFF_WOT + ((size_t)(w * 6) * 18 + 3 * ng) * 1024 + lane * 16;
  const bool shared = (nt == 4 || nt == 13);
  const size_t SL = (size_t)64 * 18 * 2048;
  const char* pp = ws + OFF_PART + ((size_t)tt * 18 + nt) * 2048 + lane * 16;
  const char* pp2 = shared ? pp + 2 * SL : pp;
  s16x8 af[2][6], wf[3][6];
  u32x4 pt[4][2];
  u32x2 xv[4], bv[4];
#pragma unroll
  for (int h = 0; h < 2; ++h) {
    pt[0][h] = *(const u32x4*)(pp + h * 1024);
    pt[1][h] = *(const u32x4*)(pp + SL + h * 1024);
    pt[2][h] = *(const u32x4*)(pp2 + h * 1024);
    pt[3][h] = *(const u32x4*)(pp2 + SL + h * 1024);
  }
#pragma unroll
  for (int q = 0; q < 4; ++q) {
    const int col = nt * 32 + 8 * q + 4 * hh;
    xv[q] = *(const u32x2*)((const char*)x + (size_t)token * 1152 + col * 2);
    bv[q] = *(const u32x2*)((const char*)b2 + col * 2);
  }
  SCHED();
#pragma unroll
  for (int k = 0; k < 6; ++k) {
    af[0][k] = *(const s16x8*)(ap + k * 1024);
    wf[0][k] = *(const s16x8*)(wp + k * 18432);
    af[1][k] = *(const s16x8*)(ap + 36 * 1024 + k * 1024);
    wf[1][k] = *(const s16x8*)(wp + k * 18432 + 1024);
    wf[2][k] = *(const s16x8*)(wp + k * 18432 + 2048);
  }
  SCHED();
  f32x16 acc[2][3];
#pragma unroll
  for (int i = 0; i < 2; ++i)
#pragma unroll
    for (int jn = 0; jn < 3; ++jn)
#pragma unroll
      for (int r = 0; r < 16; ++r) acc[i][jn][r] = 0.f;
#pragma unroll
  for (int k = 0; k < 6; ++k)
#pragma unroll
    for (int i = 0; i < 2; ++i)
#pragma unroll
      for (int jn = 0; jn < 3; ++jn) acc[i][jn] = MFMA32(wf[jn][k], af[i][k], acc[i][jn]);
#pragma unroll
  for (int i = 0; i < 2; ++i)
#pragma unroll
    for (int jn = 0; jn < 3; ++jn)
#pragma unroll
      for (int q = 0; q < 4; ++q) {
        f32x4 v = {acc[i][jn][4 * q], acc[i][jn][4 * q + 1], acc[i][jn][4 * q + 2], acc[i][jn][4 * q + 3]};
        *(LDSP f32x4*)(smem + ((w * 6 + i * 3 + jn) * 4 + q) * 1024 + lane * 16) = v;
      }
  const float sh = shared ? 1.f : 0.f;
  f32x4 fin[4];
#pragma unroll
  for (int q = 0; q < 4; ++q) {
#pragma unroll
    for (int e = 0; e < 2; ++e) {
      const unsigned p0 = pt[0][q >> 1][(q & 1) * 2 + e], p1 = pt[1][q >> 1][(q & 1) * 2 + e];
      const unsigned p2 = pt[2][q >> 1][(q & 1) * 2 + e], p3 = pt[3][q >> 1][(q & 1) * 2 + e];
      const float lo = (__builtin_bit_cast(float, p0 << 16) + __builtin_bit_cast(float, p1 << 16)) +
                       sh * (__builtin_bit_cast(float, p2 << 16) + __builtin_bit_cast(float, p3 << 16));
      const float hi = (__builtin_bit_cast(float, p0 & 0xffff0000u) + __builtin_bit_cast(float, p1 & 0xffff0000u)) +
                       sh * (__builtin_bit_cast(float, p2 & 0xffff0000u) + __builtin_bit_cast(float, p3 & 0xffff0000u));
      fin[q][2 * e]     = lo + __builtin_bit_cast(float, xv[q][e] << 16) + __builtin_bit_cast(float, bv[q][e] << 16);
      fin[q][2 * e + 1] = hi + __builtin_bit_cast(float, xv[q][e] & 0xffff0000u) + __builtin_bit_cast(float, bv[q][e] & 0xffff0000u);
    }
  }
  __syncthreads();
#pragma unroll
  for (int q = 0; q < 4; ++q) {
    f32x4 v = fin[q];
#pragma unroll
    for (int o = 0; o < 6; ++o) v += *(LDSP f32x4*)(smem + ((o * 6 + w) * 4 + q) * 1024 + lane * 16);
    u32x2 ov = {pack2(v[0], v[1]), pack2(v[2], v[3])};
    *(u32x2*)((char*)out + (size_t)token * 1152 + (nt * 32 + 8 * q + 4 * hh) * 2) = ov;
  }
}

extern "C" void kernel_launch(void* const* d_in, const int* in_sizes, int n_in,
                              void* d_out, int out_size, void* d_ws, size_t ws_size,
                              hipStream_t stream) {
  const u16* x  = (const u16*)d_in[0];
  const u16* wq = (const u16*)d_in[1];
  const u16* wk = (const u16*)d_in[2];
  const u16* wv = (const u16*)d_in[3];
  const u16* wo = (const u16*)d_in[4];
  const u16* w1 = (const u16*)d_in[6];
  const u16* b1 = (const u16*)d_in[7];
  const u16* w2 = (const u16*)d_in[8];
  const u16* b2 = (const u16*)d_in[9];
  char* ws = (char*)d_ws;
  (void)hipFuncSetAttribute((const void*)k1, hipFuncAttributeMaxDynamicSharedMemorySize, K1_LDS);
  (void)hipFuncSetAttribute((const void*)k2, hipFuncAttributeMaxDynamicSharedMemorySize, K2_LDS);
  (void)hipFuncSetAttribute((const void*)k3, hipFuncAttributeMaxDynamicSharedMemorySize, K3_LDS);
  k1<<<256, 512, K1_LDS, stream>>>(x, wq, wk, wv, wo, w1, b1, w2, ws);
  k2<<<256, 512, K2_LDS, stream>>>(ws);
  k3<<<192, 384, K3_LDS, stream>>>(x, b2, ws, (u16*)d_out);
}
```

```cpp
#include <hip/hip_runtime.h>
#include <hip/hip_bf16.h>
#include <stdint.h>
#include <type_traits>

#define DI __device__ __forceinline__
#define LDSP __attribute__((address_space(3)))
#define GLP __attribute__((address_space(1)))

typedef unsigned short u16;
typedef __attribute__((ext_vector_type(8))) short s16x8;
typedef __attribute__((ext_vector_type(4))) short s16x4;
typedef __attribute__((ext_vector_type(16))) float f32x16;
typedef __attribute__((ext_vector_type(4))) float f32x4;
typedef __attribute__((ext_vector_type(2))) float f32x2;
typedef __attribute__((ext_vector_type(2))) __bf16 bf16x2;
typedef __attribute__((ext_vector_type(4))) unsigned u32x4;
typedef __attribute__((ext_vector_type(2))) unsigned u32x2;

constexpr size_t OFF_H    = 0;
constexpr size_t OFF_W2T  = OFF_H + 18874368;
constexpr size_t OFF_WOT  = OFF_W2T + 5308416;
constexpr size_t OFF_Q    = OFF_WOT + 663552;
constexpr size_t OFF_KV   = OFF_Q + 2359296;
constexpr size_t OFF_AO   = OFF_KV + 159744;
constexpr size_t OFF_PART = OFF_AO + 2359296;
constexpr int KV_GRP = 9984, KV_TILE = 2240, KV_BYTES = 159744, KV_HALF = 79872;

#define MFMA32(a, b, c) __builtin_amdgcn_mfma_f32_32x32x16_bf16((a), (b), (c), 0, 0, 0)
#define WAIT_VM(n) asm volatile("s_waitcnt vmcnt(" #n ")" ::: "memory")
#define WAIT_LGKM0() asm volatile("s_waitcnt lgkmcnt(0)" ::: "memory")
#define BAR() __builtin_amdgcn_s_barrier()

DI unsigned pack2(float a, float b) {
  f32x2 v = {a, b};
  return __builtin_bit_cast(unsigned, __builtin_convertvector(v, bf16x2));
}
DI void glds16(const void* gbase, unsigned voff, LDSP char* l) {
  unsigned keep;
  asm volatile("s_mov_b32 %0, m0\n\ts_mov_b32 m0, %3\n\ts_nop 0\n\tglobal_load_lds_dwordx4 %1, %2\n\ts_mov_b32 m0, %0"
               : "=&s"(keep) : "v"(voff), "s"(gbase), "s"((unsigned)(size_t)l) : "memory");
}
DI void glds4(const void* gbase, unsigned voff, LDSP char* l) {
  unsigned keep;
  asm volatile("s_mov_b32 %0, m0\n\ts_mov_b32 m0, %3\n\ts_nop 0\n\tglobal_load_lds_dword %1, %2\n\ts_mov_b32 m0, %0"
               : "=&s"(keep) : "v"(voff), "s"(gbase), "s"((unsigned)(size_t)l) : "memory");
}
DI void store16(void* p, u32x4 v) {
  asm volatile("global_store_dwordx4 %0, %1, off sc1\n\ts_nop 1" :: "v"(p), "v"(v) : "memory");
}
DI s16x8 tr_frag(LDSP char* p, int second_off) {
  s16x4 lo = __builtin_amdgcn_ds_read_tr16_b64_v4i16((LDSP s16x4*)p);
  s16x4 hi = __builtin_amdgcn_ds_read_tr16_b64_v4i16((LDSP s16x4*)(p + second_off));
  return __builtin_shufflevector(lo, hi, 0, 1, 2, 3, 4, 5, 6, 7);
}
DI float other_half(float v) {
  unsigned u = __builtin_bit_cast(unsigned, v);
  auto r = __builtin_amdgcn_permlane32_swap(u, u, false, false);
  unsigned o = (threadIdx.x & 32) ? r[0] : r[1];
  return __builtin_bit_cast(float, o);
}

constexpr int K1_SLOT = 36864;
constexpr int K1_BIAS = 4 * K1_SLOT;
constexpr int K1_CS   = K1_BIAS + 1152;
constexpr int K1_CSQ  = K1_CS + 1152;
constexpr int K1_STAT = K1_CSQ + 1024;
constexpr int K1_LDS  = K1_STAT + 1024;

__host__ __device__ constexpr int w_slot(int st) { return (0x231023120ll >> (4 * st)) & 3; }

#define SCHED() __builtin_amdgcn_sched_barrier(0)
#define SGB(mask, n) __builtin_amdgcn_sched_group_barrier((mask), (n), 0)

DI u32x2 gelu4(float a0, float a1, float a2, float a3, const f32x4& cs, const f32x4& bb, float negm, float r, float c0) {
  float z0, z1, z2, z3, t0, t1, t2, t3;
  unsigned o0, o1;
  asm volatile(
      "v_fma_f32 %2, %18, %14, %10\n\tv_fma_f32 %3, %18, %15, %11\n\tv_fma_f32 %4, %18, %16, %12\n\tv_fma_f32 %5, %18, %17, %13\n\t"
      "v_fma_f32 %2, %19, %2, %21\n\tv_fma_f32 %3, %19, %3, %22\n\tv_fma_f32 %4, %19, %4, %23\n\tv_fma_f32 %5, %19, %5, %24\n\t"
      "v_mul_f32 %6, %2, %2\n\tv_mul_f32 %7, %3, %3\n\tv_mul_f32 %8, %4, %4\n\tv_mul_f32 %9, %5, %5\n\t"
      "v_fmamk_f32 %6, %6, 0xbdd2d3e8, %20\n\tv_fmamk_f32 %7, %7, 0xbdd2d3e8, %20\n\tv_fmamk_f32 %8, %8, 0xbdd2d3e8, %20\n\tv_fmamk_f32 %9, %9, 0xbdd2d3e8, %20\n\t"
      "v_mul_f32 %6, %2, %6\n\tv_mul_f32 %7, %3, %7\n\tv_mul_f32 %8, %4, %8\n\tv_mul_f32 %9, %5, %9\n\t"
      "v_exp_f32 %6, %6\n\tv_exp_f32 %7, %7\n\tv_exp_f32 %8, %8\n\tv_exp_f32 %9, %9\n\t"
      "v_add_f32 %6, 1.0, %6\n\tv_add_f32 %7, 1.0, %7\n\tv_add_f32 %8, 1.0, %8\n\tv_add_f32 %9, 1.0, %9\n\t"
      "v_rcp_f32 %6, %6\n\tv_rcp_f32 %7, %7\n\tv_rcp_f32 %8, %8\n\tv_rcp_f32 %9, %9\n\t"
      "v_mul_f32 %2, %2, %6\n\tv_mul_f32 %3, %3, %7\n\tv_mul_f32 %4, %4, %8\n\tv_mul_f32 %5, %5, %9\n\t"
      "v_cvt_pk_bf16_f32 %0, %2, %3\n\tv_cvt_pk_bf16_f32 %1, %4, %5"
      : "=&v"(o0), "=&v"(o1), "=&v"(z0), "=&v"(z1), "=&v"(z2), "=&v"(z3), "=&v"(t0), "=&v"(t1), "=&v"(t2), "=&v"(t3)
      : "v"(a0), "v"(a1), "v"(a2), "v"(a3), "v"(cs[0]), "v"(cs[1]), "v"(cs[2]), "v"(cs[3]), "v"(negm), "v"(r), "v"(c0),
        "v"(bb[0]), "v"(bb[1]), "v"(bb[2]), "v"(bb[3]));
  u32x2 o = {o0, o1};
  return o;
}

template <typename F>
DI void gelu_tile(const f32x16& a, LDSP char* cs_p, LDSP char* bias_p, char* out_p, float negm, float r, F&& between) {
  const float c0 = -2.302208198f;
#pragma unroll
  for (int hf = 0; hf < 2; ++hf) {
    f32x4 cs0 = *(LDSP f32x4*)(cs_p + hf * 64), cs1 = *(LDSP f32x4*)(cs_p + hf * 64 + 32);
    f32x4 bb0 = *(LDSP f32x4*)(bias_p + hf * 64), bb1 = *(LDSP f32x4*)(bias_p + hf * 64 + 32);
    between(2 * hf);
    u32x2 lo = gelu4(a[8 * hf], a[8 * hf + 1], a[8 * hf + 2], a[8 * hf + 3], cs0, bb0, negm, r, c0);
    between(2 * hf + 1);
    u32x2 hi = gelu4(a[8 * hf + 4], a[8 * hf + 5], a[8 * hf + 6], a[8 * hf + 7], cs1, bb1, negm, r, c0);
    u32x4 o = {lo[0], lo[1], hi[0], hi[1]};
    store16(out_p + hf * 1024, o);
  }
}

DI void colsum_frag(const s16x8& a, const s16x8& sel, f32x4& cs) {
  cs = __builtin_amdgcn_mfma_f32_16x16x32_bf16(sel, a, cs, 0, 0, 0);
}

template <int KT>
DI void w1_stage(LDSP char* sb0, LDSP char* sb1, LDSP char* sb2, const s16x8 (&xf)[36], f32x16 (&acc)[3], const s16x8& sel, f32x4& cs) {
  asm volatile("" : "+v"(sb0), "+v"(sb1), "+v"(sb2));
  s16x8 a[2][3];
  a[0][0] = tr_frag(sb0, 768); a[0][1] = tr_frag(sb1, 768); a[0][2] = tr_frag(sb2, 768);
#pragma unroll
  for (int ks = 0; ks < 12; ++ks) {
    const int cur = ks & 1;
    if (ks + 1 < 12) {
      a[cur ^ 1][0] = tr_frag(sb0 + (ks + 1) * 3072, 768);
      a[cur ^ 1][1] = tr_frag(sb1 + (ks + 1) * 3072, 768);
      a[cur ^ 1][2] = tr_frag(sb2 + (ks + 1) * 3072, 768);
    }
#pragma unroll
    for (int t = 0; t < 3; ++t) acc[t] = MFMA32(a[cur][t], xf[12 * KT + ks], acc[t]);
    colsum_frag(a[cur][0], sel, cs);
    SGB(0x100, 2); SGB(0x8, 1); SGB(0x100, 2); SGB(0x8, 1); SGB(0x100, 2); SGB(0x8, 2);
    SCHED();
  }
}

DI void qkv_stage(LDSP char* sb, const s16x8 (&xf)[36], f32x16& qa, const s16x8& sel, f32x4& cs) {
  asm volatile("" : "+v"(sb));
  s16x8 a[3];
  a[0] = tr_frag(sb, 256);
  a[1] = tr_frag(sb + 1024, 256);
#pragma unroll
  for (int s = 0; s < 36; ++s) {
    if (s + 2 < 36) a[(s + 2) % 3] = tr_frag(sb + (s + 2) * 1024, 256);
    qa = MFMA32(a[s % 3], xf[s], qa);
    colsum_frag(a[s % 3], sel, cs);
    SCHED();
  }
}

__global__ __launch_bounds__(512, 2)
void k1(const u16* __restrict__ x, const u16* __restrict__ wq, const u16* __restrict__ wk, const u16* __restrict__ wv,
        const u16* __restrict__ wo, const u16* __restrict__ w1, const u16* __restrict__ b1, const u16* __restrict__ w2,
        char* __restrict__ ws) {
  extern __shared__ __attribute__((aligned(16))) char smem_[];
  LDSP char* smem = (LDSP char*)smem_;
  const int tid = threadIdx.x, lane = tid & 63;
  const int w8 = __builtin_amdgcn_readfirstlane(tid >> 6);
  const int w = w8 & 3, grp = w8 >> 2;
  const int b = blockIdx.x, xcd = b & 7, idx = b >> 3;
  const int tg = (xcd >> 1) * 4 + (idx & 3), j = (xcd & 1) * 8 + (idx >> 2);
  const int t32 = lane & 31, hh = lane >> 5;
  const int tt = tg * 4 + w;
  const bool heavy = (j & 3) == 0;
  constexpr int rk = 0, rc = 0;

  const char* w1w = (const char*)w1 + (size_t)(48 * w) * 9216 + (size_t)j * 576;
  unsigned off3[3];
#pragma unroll
  for (int i = 0; i < 3; ++i) {
    const unsigned bb = 1024 * i + 16 * lane;
    off3[i] = (bb / 192) * 9216 + (bb % 192);
  }
  auto issue_w1_rows = [&](int st, int r) {
    const int c = st / 3, kt = st - 3 * c;
    const char* g = w1w + ((c + rc) % 3) * 192 + (size_t)((kt + rk) % 3) * 192 * 9216 + r * 16 * 9216;
    LDSP char* l = smem + w_slot(st) * K1_SLOT + w * 9216 + r * 3072;
#pragma unroll
    for (int i = 0; i < 3; ++i) glds16(g, off3[i], l + i * 1024);
  };
  auto issue_w1 = [&](int st) {
#pragma unroll
    for (int r = 0; r < 3; ++r) issue_w1_rows(st, r);
  };
  auto issue_x = [&](int q, int slot) {
    int ln = tid & 63; asm volatile("" : "+v"(ln));
    const char* g = (const char*)x + (size_t)(tt * 32) * 1152;
    LDSP char* l = smem + slot * K1_SLOT + w * 9216;
    unsigned row = (unsigned)ln / 18, pos = (unsigned)ln % 18;
    const unsigned b0 = q * 288 + rk * 384;
#pragma unroll
    for (int i = 0; i < 9; ++i) {
      unsigned cb = b0 + ((pos ^ ((row >> 3) & 1)) << 4);
      cb = cb >= 1152 ? cb - 1152 : cb;
      glds16(g, row * 1152 + cb, l + i * 1024);
      pos += 10; row += 3;
      if (pos >= 18) { pos -= 18; row += 1; }
    }
  };
  auto issue_qkv = [&](int tile, int slot, int part) {
    int ln = tid & 63; asm volatile("" : "+v"(ln));
    LDSP char* l = smem + slot * K1_SLOT + w * 9216 + part * 3072;
    const int row0 = 144 * w + 48 * part + 192 * rk;
    if (tile < 18) {
      const char* g = (const char*)wq + tile * 64;
      const unsigned vo = (ln >> 2) * 1152 + (ln & 3) * 16;
#pragma unroll
      for (int i = 0; i < 3; ++i) glds16(g + (size_t)((row0 + 16 * i) % 576) * 1152, vo, l + i * 1024);
    } else {
      const u16* src = (tile == 18) ? wk : wv;
      int dw = ln & 15; dw = dw > 8 ? 8 : dw;
      const unsigned vo = (ln >> 4) * 36 + dw * 4;
#pragma unroll
      for (int i = 0; i < 12; ++i) glds4((const char*)src + ((row0 + 4 * i) % 576) * 36, vo, l + i * 256);
    }
  };
  const int lw = (tg * 12 + (j - (j >> 2) - 1)) * 4 + w;
  auto issue_unit = [&](int part) {
    if (lw < 648) {
      int ln = tid & 63; asm volatile("" : "+v"(ln));
      LDSP char* l = smem + 0 * K1_SLOT + w * 9216 + part * 3072;
      const int u = lw - 576, ks = u >> 1;
      const bool plain = ks < 32;
      const char* g = lw < 576 ? (const char*)w2 + (size_t)((lw >> 1) * 16) * 1152 + (lw & 1) * 576
                               : (const char*)wo + (size_t)(plain ? ks * 18 : 144 * (ks - 32) + 16) * 1152 + (u & 1) * 576;
      const unsigned p0 = 192 * part + ln;
      unsigned row = p0 / 36, pos = p0 % 36;
#pragma unroll
      for (int i = 0; i < 3; ++i) {
        const unsigned srow = plain ? row : 18 * (row >> 1) + (row & 1);
        glds16(g, srow * 1152 + pos * 16, l + i * 1024);
        pos += 28; row += 1;
        if (pos >= 36) { pos -= 36; row += 1; }
      }
    }
  };

  unsigned bias_raw = 0, bias_raw2 = 0;
  if (grp == 1) {
    asm volatile("global_load_ushort %0, %2, %4\n\tglobal_load_ushort %1, %3, %4" : "=&v"(bias_raw), "=&v"(bias_raw2)
                 : "v"((unsigned)((tid - 256) * 2)), "v"((unsigned)((tid < 288 ? tid : 287) * 2)), "s"(b1 + j * 288) : "memory");
  }
  if (grp == 0) { issue_x(0, 3); issue_x(1, 1); }
  else          { issue_w1(0);   issue_w1(1); }
  s16x8 xf[36];
  const int xl = w * 9216 + t32 * 288 + ((hh ^ ((t32 >> 3) & 1)) << 4);
  auto read_x = [&](int q, int slot) {
#pragma unroll
    for (int s = 0; s < 9; ++s) xf[9 * q + s] = *(LDSP s16x8*)(smem + slot * K1_SLOT + xl + s * 32);
  };
  if (grp == 0) WAIT_VM(9);
  BAR();
  read_x(0, 3);
  if (grp == 0) { WAIT_VM(0); }
  else {
    asm volatile("s_waitcnt vmcnt(9)" : "+v"(bias_raw), "+v"(bias_raw2) :: "memory");
    *(LDSP float*)(smem + K1_BIAS + (tid - 256) * 4) = __builtin_bit_cast(float, bias_raw << 16);
    if (tid < 288) *(LDSP float*)(smem + K1_BIAS + tid * 4) = __builtin_bit_cast(float, bias_raw2 << 16);
  }
  WAIT_LGKM0(); BAR();
  read_x(1, 1);
  WAIT_LGKM0(); BAR();

  const int q4 = (lane & 15) >> 2, p4 = lane & 3, g16 = (lane >> 4) & 1;
  const int rot = (w == 3) ? 0 : w;
  const int tl0 = rot, tl1 = (rot + 1) % 3, tl2 = (rot + 2) % 3;
  const int lo_w1 = (8 * hh + q4) * 192 + 32 * g16 + 8 * p4;
  const int lo_q  = (8 * hh + q4) * 64 + 32 * g16 + 8 * p4;
  char* hout = ws + OFF_H + ((size_t)tt * 288 + (size_t)j * 18) * 1024 + lane * 16;
  f32x16 acc[3];
  float negm = 0.f, r1 = 0.f, rm = 0.f;
  const unsigned selv = ((lane & 15) == ((lane >> 4) & 1)) ? 0x3f803f80u : 0u;
  const u32x4 selu = {selv, selv, selv, selv};
  const s16x8 sel = __builtin_bit_cast(s16x8, selu);

  auto load_stats = [&]() {
    f32x2 st = *(LDSP f32x2*)(smem + K1_STAT + (w * 32 + t32) * 8);
    negm = -st[0] * (1.0f / 576.0f);
    const float var = __builtin_fmaxf(st[1] * (1.0f / 576.0f) - negm * negm, 0.f);
    auto rb = [](float v) { return __builtin_bit_cast(float, pack2(v, 0.f) << 16); };
    const float s1 = rb(__builtin_sqrtf(rb(rb(var) + 1e-5f)));
    r1 = 1.0f / s1;
    const float s2 = rb(__builtin_sqrtf(rb(rb(var * r1 * r1) + 1e-5f)));
    rm = r1 / s2;
  };
  auto mfma_chunk = [&](auto cc) {
    constexpr int c = decltype(cc)::value;
    __builtin_amdgcn_s_setprio(0);
    f32x4 cs = {0.f, 0.f, 0.f, 0.f};
#pragma unroll
    for (int t = 0; t < 3; ++t)
#pragma unroll
      for (int i = 0; i < 16; ++i) acc[t][i] = 0.f;
    LDSP char* s0 = smem + lo_w1 + tl0 * 64;
    LDSP char* s1 = smem + lo_w1 + tl1 * 64;
    LDSP char* s2 = smem + lo_w1 + tl2 * 64;
    constexpr int o0 = w_slot(3 * c) * K1_SLOT, o1 = w_slot(3 * c + 1) * K1_SLOT, o2 = w_slot(3 * c + 2) * K1_SLOT;
    w1_stage<0>(s0 + o0, s1 + o0, s2 + o0, xf, acc, sel, cs);
    if (c == 1) { WAIT_VM(9); } else if (c == 2) { WAIT_VM(11); }
    WAIT_LGKM0(); BAR();
    if (c == 0) { read_x(2, 3); WAIT_LGKM0(); BAR(); }
    w1_stage<1>(s0 + o1, s1 + o1, s2 + o1, xf, acc, sel, cs);
    if (c != 0) WAIT_VM(0);
    WAIT_LGKM0(); BAR();
    if (c == 0) { read_x(3, 0); WAIT_LGKM0(); BAR(); }
    w1_stage<2>(s0 + o2, s1 + o2, s2 + o2, xf, acc, sel, cs);
    if (lane < 16 && w < 3) {
      *(LDSP float*)(smem + K1_CS + (c * 96 + tl0 * 32 + lane) * 4) = cs[0];
      *(LDSP float*)(smem + K1_CS + (c * 96 + tl0 * 32 + 16 + lane) * 4) = cs[1];
    }
    WAIT_LGKM0(); BAR();
    __builtin_amdgcn_s_setprio(1);
  };
  auto gelu_t = [&](int c, int t, int tl, int st_issue) {
    const int ca = (c + rc) % 3;
    gelu_tile(acc[t], smem + K1_CS + (c * 96 + tl * 32) * 4 + hh * 16, smem + K1_BIAS + (ca * 96 + tl * 32) * 4 + hh * 16,
              hout + (ca * 6 + tl * 2) * 1024, negm, rm, [&](int i) {
      if (i < 3) {
        if (st_issue >= 0 && st_issue < 9) issue_w1_rows(st_issue, i);
        else if (st_issue == 9) issue_qkv(j, 1, i);
        else if (st_issue == 10) { if (heavy) issue_qkv(16 + (j >> 2), 0, i); else issue_unit(i); }
      }
    });
  };

  if (grp == 0) {
    mfma_chunk(std::integral_constant<int, 0>{});
    load_stats();
    gelu_t(0, 0, tl0, 6); WAIT_LGKM0(); BAR();
    gelu_t(0, 1, tl1, 7); WAIT_LGKM0(); BAR();
    gelu_t(0, 2, tl2, 8); WAIT_VM(22); WAIT_LGKM0(); BAR();
    mfma_chunk(std::integral_constant<int, 2>{});
    gelu_t(2, 0, tl0, -1); gelu_t(2, 1, tl1, -1); gelu_t(2, 2, tl2, -1);
  } else {
    f32x4 rs = {0.f, 0.f, 0.f, 0.f};
#pragma unroll
    for (int i = 0; i < 16; ++i) acc[0][i] = 0.f;
    auto stats_frag = [&](int s) {
      colsum_frag(xf[s], sel, rs);
      acc[0] = MFMA32(xf[s], xf[s], acc[0]);
    };
    __builtin_amdgcn_s_setprio(1);
    issue_x(2, 3);
#pragma unroll
    for (int s = 0; s < 18; ++s) { if (s % 6 == 0) issue_w1_rows(2, s / 6); stats_frag(s); }
    WAIT_VM(9); BAR();
    read_x(2, 3); WAIT_LGKM0(); BAR();
    issue_x(3, 0);
#pragma unroll
    for (int s = 18; s < 27; ++s) { if (s % 3 == 0) issue_w1_rows(3, (s - 18) / 3); stats_frag(s); }
    WAIT_VM(9); BAR();
    read_x(3, 0); WAIT_LGKM0(); BAR();
    issue_w1(4);
#pragma unroll
    for (int s = 27; s < 36; ++s) { if (s % 3 == 0) issue_w1_rows(5, (s - 27) / 3); stats_frag(s); }
    {
      const int dreg = (t32 & 3) + 4 * (t32 >> 3);
      float s2 = 0.f;
#pragma unroll
      for (int i = 0; i < 16; ++i) s2 = (i == dreg) ? acc[0][i] : s2;
      if (hh == ((t32 >> 2) & 1)) *(LDSP float*)(smem + K1_STAT + (w * 32 + t32) * 8 + 4) = s2;
      if (lane < 16) {
        *(LDSP float*)(smem + K1_STAT + (w * 32 + lane) * 8) = rs[0];
        *(LDSP float*)(smem + K1_STAT + (w * 32 + 16 + lane) * 8) = rs[1];
      }
    }
    WAIT_VM(18); WAIT_LGKM0(); BAR();
    mfma_chunk(std::integral_constant<int, 1>{});
    load_stats();
    gelu_t(1, 0, tl0, 10); WAIT_LGKM0(); BAR();
    gelu_t(1, 1, tl1, 9); WAIT_LGKM0(); BAR();
    gelu_t(1, 2, tl2, -1);
    WAIT_VM(2); WAIT_LGKM0(); BAR();
    __builtin_amdgcn_s_setprio(2);

    auto qkv_tile = [&](int tile, int slot) {
      f32x16 qa;
#pragma unroll
      for (int i = 0; i < 16; ++i) qa[i] = 0.f;
      f32x4 cs = {0.f, 0.f, 0.f, 0.f};
      qkv_stage(smem + slot * K1_SLOT + lo_q, xf, qa, sel, cs);
      int ln = tid & 63; asm volatile("" : "+v"(ln));
      const int t32 = ln & 31, hh = ln >> 5, token = tt * 32 + t32;
      LDSP char* cp = smem + K1_CSQ + (w * 2 + (slot & 1)) * 128;
      if (ln < 16) { *(LDSP float*)(cp + ln * 4) = cs[0]; *(LDSP float*)(cp + 64 + ln * 4) = cs[1]; }
      WAIT_LGKM0();
#pragma unroll
      for (int g = 0; g < 4; ++g) {
        f32x4 c4 = *(LDSP f32x4*)(cp + g * 32 + hh * 16);
#pragma unroll
        for (int i = 0; i < 4; ++i) qa[4 * g + i] = __builtin_fmaf(negm, c4[i], qa[4 * g + i]);
      }
      if (tile < 18) {
        const float sc = r1 * (0.23570226039551584f * 1.4426950408889634f);
        char* qp = ws + OFF_Q + (size_t)tt * 36864;
#pragma unroll
        for (int i = 0; i < 8; ++i) {
          const unsigned n = tile * 32 + 8 * (i >> 1) + 4 * hh + 2 * (i & 1);
          const unsigned head = n / 18, d = n - head * 18;
          const unsigned off = d < 16 ? (t32 + 32 * ((d >> 2) & 1)) * 16 + ((d >> 3) * 2 + ((d >> 1) & 1)) * 4 : 1024 + t32 * 4;
          *(unsigned*)(qp + head * 1152 + off) = pack2(qa[2 * i] * sc, qa[2 * i + 1] * sc);
        }
      } else if (tile == 18) {
        u32x4 o = {pack2(qa[0] * r1, qa[1] * r1), pack2(qa[2] * r1, qa[3] * r1),
                   pack2(qa[4] * r1, qa[5] * r1), pack2(qa[6] * r1, qa[7] * r1)};
        char* gp = ws + OFF_KV + (size_t)tg * KV_GRP;
        *(u32x4*)(gp + 1024 + w * KV_TILE + ln * 16) = o;
        if (hh == 0) *(unsigned*)(gp + (w >> 1) * 512 + t32 * 16 + (w & 1) * 4) = pack2(qa[8] * r1, qa[9] * r1);
        else if ((w & 1) == 0) {
          u32x2 cst = {w == 0 ? 0x3f803f80u : 0u, 0u};
          *(u32x2*)(gp + (w >> 1) * 512 + t32 * 16 + 8) = cst;
        }
      } else {
        const int s = t32 >> 4, hk = (t32 >> 2) & 1, jj = ((t32 >> 3) & 1) * 4 + (t32 & 3);
        char* vp = ws + OFF_KV + (size_t)tg * KV_GRP + 2048 + w * KV_TILE + (s * 2 + hk) * 304 + jj * 2;
#pragma unroll
        for (int i = 0; i < 8; ++i) {
          const int d = (i & 3) + 8 * (i >> 2) + 4 * hh;
          *(u16*)(vp + d * 16) = (u16)(pack2(qa[i] * r1, 0.f) & 0xffffu);
        }
        if (hh == 0) {
          *(u16*)(vp + 16 * 16) = (u16)(pack2(qa[8] * r1, 0.f) & 0xffffu);
          *(u16*)(vp + 17 * 16) = (u16)(pack2(qa[9] * r1, 0.f) & 0xffffu);
          *(u16*)(vp + 18 * 16) = (u16)0x3f80;
        }
      }
    };
    qkv_tile(j, 1);
    if (heavy) {
      qkv_tile(16 + (j >> 2), 0);
    } else if (lw < 648) {
      int ln = tid & 63; asm volatile("" : "+v"(ln));
      const int hh = ln >> 5, q4 = (ln & 15) >> 2, p4 = ln & 3, g16 = (ln >> 4) & 1;
      LDSP char* sb = smem + 0 * K1_SLOT + w * 9216 + (4 * hh + q4) * 576 + 32 * g16 + 8 * p4;
      char* dst = (lw < 576) ? ws + OFF_W2T + ((size_t)(lw >> 1) * 18 + (lw & 1) * 9) * 1024
                             : ws + OFF_WOT + ((size_t)((lw - 576) >> 1) * 18 + (lw & 1) * 9) * 1024;
#pragma unroll
      for (int n = 0; n < 9; ++n) {
        s16x8 f = tr_frag(sb + n * 64, 8 * 576);
        store16(dst + n * 1024 + ln * 16, __builtin_bit_cast(u32x4, f));
      }
    }
  }
}

constexpr int K2_FLAG = KV_BYTES;
constexpr int K2_LDS  = KV_BYTES + 128;
enum { FL_KVA = 0, FL_KVG = 1, FL_PROG = 2, FL_GW = 3, FL_DONE = 4, FL_SENT = 5, FL_GW2 = 6 };

#define EXP2(x) __builtin_amdgcn_exp2f(x)

DI void flag_set(LDSP char* smem, int set, int i, int lane) {
  if (lane == 0) *(volatile LDSP unsigned*)(smem + K2_FLAG + set * 16 + i * 4) = 1u;
}
DI void flag_wait_all(LDSP char* smem, int set) {
  while (true) {
    u32x4 f = *(volatile LDSP u32x4*)(smem + K2_FLAG + set * 16);
    if (f[0] & f[1] & f[2] & f[3]) break;
    __builtin_amdgcn_s_sleep(2);
  }
  asm volatile("" ::: "memory");
}
DI void flag_wait_one(LDSP char* smem, int set, int i) {
  while (*(volatile LDSP unsigned*)(smem + K2_FLAG + set * 16 + i * 4) == 0u) __builtin_amdgcn_s_sleep(1);
  asm volatile("" ::: "memory");
}

DI void attn_step(f32x16& scur, f32x16& snext, const s16x8& k0, const s16x8& k1, const s16x8& qn0, const s16x8& qn1,
                  const s16x8& vprev1, f32x16& oprev, const s16x8& vcur0, f32x16& ocur, u32x4& p0, u32x4& p1) {
  f32x16 zero;
#pragma unroll
  for (int i = 0; i < 16; ++i) zero[i] = 0.f;
#define EX(i) do { float t_ = scur[i]; asm volatile("v_exp_f32 %0, %0" : "+v"(t_)); scur[i] = t_; } while (0)
  p1[2] = pack2(snext[12], snext[13]);
  p1[3] = pack2(snext[14], snext[15]);
  SCHED();
  snext = MFMA32(k0, qn0, zero);
  SCHED();
#pragma unroll
  for (int i = 0; i < 3; ++i) EX(i);
  SCHED();
  snext = MFMA32(k1, qn1, snext);
  SCHED();
#pragma unroll
  for (int i = 3; i < 8; ++i) EX(i);
  SCHED();
  p0[0] = pack2(scur[0], scur[1]);
  p0[1] = pack2(scur[2], scur[3]);
  p0[2] = pack2(scur[4], scur[5]);
  p0[3] = pack2(scur[6], scur[7]);
  SCHED();
  oprev = MFMA32(vprev1, __builtin_bit_cast(s16x8, p1), oprev);
  SCHED();
#pragma unroll
  for (int i = 8; i < 8 + 3; ++i) EX(i);
  SCHED();
  ocur = MFMA32(vcur0, __builtin_bit_cast(s16x8, p0), ocur);
  SCHED();
#pragma unroll
  for (int i = 8 + 3; i < 16; ++i) EX(i);
  SCHED();
  p1[0] = pack2(scur[8], scur[9]);
  p1[1] = pack2(scur[10], scur[11]);
  SCHED();
#undef EX
}

DI s16x8 make_q1(unsigned qe, unsigned mneg, int i, int hh) {
  const unsigned a = (hh == (i >> 1)) ? qe : 0u;
  u32x4 r = {(i & 1) ? 0u : a, (i & 1) ? a : 0u, hh ? 0u : mneg, 0u};
  return __builtin_bit_cast(s16x8, r);
}
DI unsigned neg_hi_lo(float m) {
  unsigned hi = pack2(-m, 0.f) & 0xffffu;
  float rem = -m - __builtin_bit_cast(float, hi << 16);
  unsigned lo = pack2(rem, 0.f) & 0xffffu;
  return hi | (lo << 16);
}
DI int v_row(int t32) { return t32 > 18 ? ((t32 >= 20 && t32 < 28) ? 0 : 4) : t32; }

DI f32x16 attn_exact(const char* kv, int lane, u32x4 q0u, unsigned qe) {
  const int t32 = lane & 31, hh = lane >> 5;
  const s16x8 q0 = __builtin_bit_cast(s16x8, q0u);
  f32x16 zero, o;
#pragma unroll
  for (int i = 0; i < 16; ++i) { zero[i] = 0.f; o[i] = 0.f; }
  float m = -INFINITY;
#pragma unroll 1
  for (int T = 0; T < 64; ++T) {
    const char* gp = kv + (T >> 2) * KV_GRP;
    s16x8 k0 = *(const s16x8*)(gp + 1024 + (T & 3) * KV_TILE + lane * 16);
    s16x8 k1 = *(const s16x8*)(gp + lane * 16);
    f32x16 s = MFMA32(k0, q0, zero);
    s = MFMA32(k1, make_q1(qe, 0u, T & 3, hh), s);
#pragma unroll
    for (int i = 0; i < 16; ++i) m = __builtin_fmaxf(m, s[i]);
  }
  m = __builtin_fmaxf(m, other_half(m));
  const unsigned mneg = neg_hi_lo(m);
#pragma unroll 1
  for (int T = 0; T < 64; ++T) {
    const char* gp = kv + (T >> 2) * KV_GRP;
    const char* tp = gp + 1024 + (T & 3) * KV_TILE;
    s16x8 k0 = *(const s16x8*)(tp + lane * 16);
    s16x8 k1 = *(const s16x8*)(gp + lane * 16);
    s16x8 v0 = *(const s16x8*)(tp + 1024 + hh * 304 + v_row(t32) * 16);
    s16x8 v1 = *(const s16x8*)(tp + 1024 + 608 + hh * 304 + v_row(t32) * 16);
    f32x16 s = MFMA32(k0, q0, zero);
    s = MFMA32(k1, make_q1(qe, mneg, T & 3, hh), s);
    u32x4 p0, p1;
#pragma unroll
    for (int i = 0; i < 4; ++i) {
      p0[i] = pack2(EXP2(s[2 * i]), EXP2(s[2 * i + 1]));
      p1[i] = pack2(EXP2(s[8 + 2 * i]), EXP2(s[8 + 2 * i + 1]));
    }
    o = MFMA32(v0, __builtin_bit_cast(s16x8, p0), o);
    o = MFMA32(v1, __builtin_bit_cast(s16x8, p1), o);
  }
  return o;
}

typedef __amdgpu_buffer_rsrc_t rsrc_t;
DI s16x8 bload(rsrc_t r, unsigned voff, unsigned soff) {
  return __builtin_bit_cast(s16x8, __builtin_amdgcn_raw_buffer_load_b128(r, voff, soff, 0));
}
template <int NT>
DI void gemm2_load(rsrc_t r, unsigned hp, unsigned wp, unsigned wsp, unsigned lo16, int k, s16x8 (&hb)[2], s16x8 (&wb)[5]) {
  hb[0] = bload(r, lo16, hp + k * 1024);
  wb[0] = bload(r, lo16, wp + k * 18432);
  hb[1] = bload(r, lo16, hp + k * 1024 + 288 * 1024);
#pragma unroll
  for (int n = 1; n < 4; ++n) wb[n] = bload(r, lo16 + n * 1024, wp + k * 18432);
  if (NT == 5) wb[4] = bload(r, lo16, wsp + k * 18432);
}
template <int NT, int NTN>
DI void gemm2_step(rsrc_t r, unsigned hp, unsigned wp, unsigned wsp, unsigned lo16, int kn, s16x8 (&hb)[2], s16x8 (&wb)[5],
                   f32x16 (&acc)[2][5]) {
#pragma unroll
  for (int n = 0; n < NT; ++n) {
    acc[0][n] = MFMA32(wb[n], hb[0], acc[0][n]);
    acc[1][n] = MFMA32(wb[n], hb[1], acc[1][n]);
    SCHED();
    if (n < 4) wb[n] = bload(r, lo16 + n * 1024, wp + kn * 18432);
    else if (NTN == 5) wb[4] = bload(r, lo16, wsp + kn * 18432);
    SCHED();
  }
  if (NT == 4 && NTN == 5) wb[4] = bload(r, lo16, wsp + kn * 18432);
  hb[0] = bload(r, lo16, hp + kn * 1024);
  hb[1] = bload(r, lo16, hp + kn * 1024 + 288 * 1024);
  SCHED();
}
template <int N1, int N2, typename F, typename G>
DI void gemm2_all(rsrc_t r, unsigned hp, unsigned wp, unsigned wsp, unsigned lo16, f32x16 (&acc)[2][5], F&& after_first_loads, G&& after_first_step) {
  s16x8 hA[2], wA[5], hB[2], wB[5];
  after_first_loads();
  gemm2_load<N1>(r, hp, wp, wsp, lo16, 0, hA, wA);
  gemm2_load<N1>(r, hp, wp, wsp, lo16, 1, hB, wB);
  SCHED();
  after_first_step();
#pragma unroll 1
  for (int k = 0; k < 16; k += 2) {
    gemm2_step<N1, N1>(r, hp, wp, wsp, lo16, k + 2, hA, wA, acc);
    gemm2_step<N1, N1>(r, hp, wp, wsp, lo16, k + 3, hB, wB, acc);
  }
  gemm2_step<N1, N2>(r, hp, wp, wsp, lo16, 18, hA, wA, acc);
  gemm2_step<N1, N2>(r, hp, wp, wsp, lo16, 19, hB, wB, acc);
#pragma unroll 1
  for (int k = 18; k < 36; k += 2) {
    gemm2_step<N2, N2>(r, hp, wp, wsp, lo16, (k + 2 < 36) ? k + 2 : 35, hA, wA, acc);
    gemm2_step<N2, N2>(r, hp, wp, wsp, lo16, (k + 3 < 36) ? k + 3 : 35, hB, wB, acc);
  }
}

__global__ __launch_bounds__(512, 2)
void k2(char* __restrict__ ws) {
  extern __shared__ __attribute__((aligned(16))) char smem_[];
  LDSP char* smem = (LDSP char*)smem_;
  const int tid = threadIdx.x, lane = tid & 63;
  const int w = __builtin_amdgcn_readfirstlane(tid >> 6);
  const int b = blockIdx.x;
  const int t32 = lane & 31, hh = lane >> 5;
  if (tid < 32) *(LDSP unsigned*)(smem + K2_FLAG + tid * 4) = 0u;
  const char* kvg = ws + OFF_KV;

  if (w < 4) {
    const int tt = b >> 2, hg = b & 3;
    const int rs = w & 1, kh2 = w >> 1;
    const int head0 = hg * 8 + rs * 4;
#pragma unroll
    for (int i = 0; i < 5; ++i) {
      glds16(kvg + (i * 4 + w) * 1024, lane * 16, smem + (i * 4 + w) * 1024);
      glds16(kvg + KV_HALF + (i * 4 + w) * 1024, lane * 16, smem + KV_HALF + (i * 4 + w) * 1024);
    }
    u32x4 q0u[4];
    unsigned qe[4];
#pragma unroll
    for (int qb = 0; qb < 4; ++qb) {
      const char* qp = ws + OFF_Q + (size_t)tt * 36864 + (head0 + qb) * 1152;
      q0u[qb] = *(const u32x4*)(qp + lane * 16);
      qe[qb] = *(const unsigned*)(qp + 1024 + t32 * 4);
    }
    WAIT_LGKM0(); BAR();
    WAIT_VM(0);
    flag_set(smem, FL_KVA, w, lane);
    flag_wait_all(smem, FL_KVA);
    s16x8 q0[4], q1[4][4];
    f32x16 o[4];
    f32x16 sa, sb;
#pragma unroll
    for (int i = 0; i < 16; ++i) sb[i] = 0.f;
    {
      s16x8 k0 = *(LDSP s16x8*)(smem + 1024 + lane * 16);
      s16x8 k1 = *(LDSP s16x8*)(smem + lane * 16);
#pragma unroll
      for (int qb = 0; qb < 4; ++qb) {
        asm volatile("" : "+v"(q0u[qb]), "+v"(qe[qb]));
        q0[qb] = __builtin_bit_cast(s16x8, q0u[qb]);
        f32x16 s = MFMA32(k0, q0[qb], sb);
        s = MFMA32(k1, make_q1(qe[qb], 0u, 0, hh), s);
        float m = s[0];
#pragma unroll
        for (int i = 1; i < 16; ++i) m = __builtin_fmaxf(m, s[i]);
        m = __builtin_fmaxf(m, other_half(m));
        const unsigned mneg = neg_hi_lo(m);
#pragma unroll
        for (int i = 0; i < 4; ++i) q1[i][qb] = make_q1(qe[qb], mneg, i, hh);
#pragma unroll
        for (int i = 0; i < 16; ++i) o[qb][i] = 0.f;
      }
    }
    LDSP char* kp = smem + kh2 * KV_HALF + lane * 16;
    LDSP char* vp = smem + kh2 * KV_HALF + 2048 + hh * 304 + v_row(t32) * 16;
    u32x4 p0 = {0u, 0u, 0u, 0u}, p1 = {0u, 0u, 0u, 0u};
    s16x8 k1c = *(LDSP s16x8*)(kp), k1n;
    s16x8 k0b[2], v0b[2], v1b[2];
    k0b[0] = *(LDSP s16x8*)(kp + 1024);
    const unsigned long long vmask = 0x0007ffff0007ffffull;
#pragma unroll
    for (int i = 0; i < 2; ++i) { v0b[i] = s16x8{0, 0, 0, 0, 0, 0, 0, 0}; v1b[i] = s16x8{0, 0, 0, 0, 0, 0, 0, 0}; }
#define VLD(dst, addr, off) asm volatile("s_mov_b64 exec, %2\n\tds_read_b128 %0, %1 offset:%3\n\ts_mov_b64 exec, -1" \
                                         : "+v"(dst) : "v"(addr), "s"(vmask), "i"(off) : "memory")
#define VWAIT(dst) asm volatile("s_waitcnt lgkmcnt(0)" : "+v"(dst) :: "memory")
    VLD(v0b[0], vp, 0);
    sa = MFMA32(k0b[0], q0[0], sb);
    sa = MFMA32(k1c, q1[0][0], sa);
    asm volatile("s_nop 15" : "+v"(sa));
#pragma unroll 1
    for (int g = 0; g < 8; ++g) {
      if (g == 1) flag_wait_all(smem, FL_KVG);
      if (g == 5) flag_set(smem, FL_PROG, w, lane);
      const int nxg = (g < 7) ? KV_GRP : 0;
#pragma unroll
      for (int i = 0; i < 4; ++i) {
        const int c = i & 1, n = c ^ 1;
        const int nt_off = (i < 3) ? (i + 1) * KV_TILE : nxg;
        VWAIT(v0b[c]);
        VLD(v1b[c], vp, i * KV_TILE + 608);
        SCHED();
        attn_step(sa, sb, k0b[c], k1c, q0[1], q1[i][1], v1b[n], o[3], v0b[c], o[0], p0, p1);
        VWAIT(v1b[c]);
        k0b[n] = *(LDSP s16x8*)(kp + 1024 + nt_off);
        if (i == 3) k1n = *(LDSP s16x8*)(kp + nxg);
        SCHED();
        attn_step(sb, sa, k0b[c], k1c, q0[2], q1[i][2], v1b[c], o[0], v0b[c], o[1], p0, p1);
        if (i < 3) VLD(v0b[n], vp, (i + 1) * KV_TILE); else { LDSP char* vn = vp + nxg; VLD(v0b[n], vn, 0); }
        SCHED();
        attn_step(sa, sb, k0b[c], k1c, q0[3], q1[i][3], v1b[c], o[1], v0b[c], o[2], p0, p1);
        attn_step(sb, sa, k0b[n], (i == 3) ? k1n : k1c, q0[0], q1[(i + 1) & 3][0], v1b[c], o[2], v0b[c], o[3], p0, p1);
      }
      k1c = k1n;
      kp += KV_GRP; vp += KV_GRP;
    }
    p1[2] = pack2(sb[12], sb[13]);
    p1[3] = pack2(sb[14], sb[15]);
    o[3] = MFMA32(v1b[1], __builtin_bit_cast(s16x8, p1), o[3]);
    f32x16 keep[2], send[2];
    u32x4 qk0[2];
    unsigned qke[2];
#pragma unroll
    for (int q = 0; q < 2; ++q) {
#pragma unroll
      for (int i = 0; i < 11; ++i) {
        keep[q][i] = kh2 ? o[2 + q][i] : o[q][i];
        send[q][i] = kh2 ? o[q][i] : o[2 + q][i];
      }
#pragma unroll
      for (int i = 11; i < 16; ++i) { keep[q][i] = 0.f; send[q][i] = 0.f; }
#pragma unroll
      for (int i = 0; i < 4; ++i) qk0[q][i] = kh2 ? q0u[2 + q][i] : q0u[q][i];
      { unsigned a_ = qe[q], b_ = qe[2 + q]; asm volatile("" : "+v"(a_), "+v"(b_)); qke[q] = kh2 ? b_ : a_; }
    }
    WAIT_LGKM0();
    flag_set(smem, FL_DONE, w, lane);
    flag_wait_one(smem, FL_DONE, w ^ 1);
    {
      LDSP char* xp = smem + kh2 * KV_HALF + 7 * KV_GRP + rs * 4864;
#pragma unroll
      for (int q = 0; q < 2; ++q) {
        const f32x16 ov = send[q];
        *(LDSP f32x4*)(xp + q * 2432 + lane * 16) = f32x4{ov[0], ov[1], ov[2], ov[3]};
        *(LDSP f32x4*)(xp + q * 2432 + 1024 + lane * 16) = f32x4{ov[4], ov[5], ov[6], ov[7]};
        if (hh == 0) {
          *(LDSP float*)(xp + q * 2432 + 2048 + t32 * 4) = ov[8];
          *(LDSP float*)(xp + q * 2432 + 2176 + t32 * 4) = ov[9];
          *(LDSP float*)(xp + q * 2432 + 2304 + t32 * 4) = ov[10];
        }
      }
    }
    WAIT_LGKM0();
    flag_set(smem, FL_SENT, w, lane);
    flag_wait_one(smem, FL_SENT, w ^ 2);
    bool bad = false;
#pragma unroll
    for (int q = 0; q < 2; ++q) {
      LDSP char* xp = smem + (kh2 ^ 1) * KV_HALF + 7 * KV_GRP + rs * 4864 + q * 2432;
      f32x4 a0 = *(LDSP f32x4*)(xp + lane * 16), a1 = *(LDSP f32x4*)(xp + 1024 + lane * 16);
#pragma unroll
      for (int i = 0; i < 4; ++i) { keep[q][i] += a0[i]; keep[q][4 + i] += a1[i]; }
      keep[q][8] += *(LDSP float*)(xp + 2048 + t32 * 4);
      keep[q][9] += *(LDSP float*)(xp + 2176 + t32 * 4);
      keep[q][10] += *(LDSP float*)(xp + 2304 + t32 * 4);
      float mag = 0.f;
#pragma unroll
      for (int i = 0; i < 8; ++i) mag += __builtin_fabsf(keep[q][i]);
      const float ext = __builtin_fabsf(keep[q][8]) + __builtin_fabsf(keep[q][9]) + 1e12f * __builtin_fabsf(keep[q][10]);
      mag += hh ? 0.f : ext;
      bad = bad || !(mag < 1e30f);
    }
    if (__builtin_expect(__any(bad), 0)) {
#pragma unroll 1
      for (int q = 0; q < 2; ++q) {
        f32x16 ex = attn_exact(kvg, lane, q ? qk0[1] : qk0[0], q ? qke[1] : qke[0]);
        if (q) keep[1] = ex; else keep[0] = ex;
      }
    }
#pragma unroll
    for (int q = 0; q < 2; ++q) {
      float l = keep[q][10];
      const float lo_ = other_half(l);
      const float li = __builtin_amdgcn_rcpf(hh ? lo_ : l);
      const int head = head0 + 2 * kh2 + q;
      u32x4 o4 = {pack2(keep[q][0] * li, keep[q][1] * li), pack2(keep[q][2] * li, keep[q][3] * li),
                  pack2(keep[q][4] * li, keep[q][5] * li), pack2(keep[q][6] * li, keep[q][7] * li)};
      char* ap = ws + OFF_AO + (size_t)tt * 36 * 1024;
      store16(ap + head * 1024 + lane * 16, o4);
      if (hh == 0) {
        const int h8 = head & 7;
        *(unsigned*)(ap + (32 + (head >> 3)) * 1024 + (t32 + 32 * ((h8 >> 1) & 1)) * 16 + ((h8 >> 2) * 2 + (h8 & 1)) * 4) =
            pack2(keep[q][8] * li, keep[q][9] * li);
      }
    }
  } else {
    const int g = w - 4;
    const int xcd = b & 7, idx = b >> 3;
    const int kh = xcd & 1, tp = (xcd >> 1) * 8 + (idx & 7), ng = idx >> 3;
    const int nt0 = (ng >> 1) * 9 + (ng & 1) * 5;
    const int nts = (ng >> 1) * 9 + 4;
    const int fb0 = kh * 144 + g * 36;
    const rsrc_t rsrc = __builtin_amdgcn_make_buffer_rsrc(ws, 0, (unsigned)OFF_WOT, 0x00020000);
    const unsigned hp0 = (unsigned)OFF_H + ((2 * tp) * 288 + fb0) * 1024;
    const unsigned wp = (unsigned)OFF_W2T + (fb0 * 18 + nt0) * 1024;
    const unsigned wsp = (unsigned)OFF_W2T + (fb0 * 18 + nts) * 1024;
    const unsigned lo16 = lane * 16;
    BAR();
    f32x16 acc[2][5];
#pragma unroll
    for (int a = 0; a < 2; ++a)
#pragma unroll
      for (int n = 0; n < 5; ++n)
#pragma unroll
        for (int i = 0; i < 16; ++i) acc[a][n][i] = 0.f;
    auto stage_kv = [&]() {
      __builtin_amdgcn_s_setprio(3);
#pragma unroll 1
      for (int i = g; i < 58; i += 4) {
        glds16(kvg + (20 + i) * 1024, lo16, smem + (20 + i) * 1024);
        glds16(kvg + KV_HALF + (20 + i) * 1024, lo16, smem + KV_HALF + (20 + i) * 1024);
      }
      __builtin_amdgcn_s_setprio(0);
    };
    auto kv_ready = [&]() {
      WAIT_VM(12);
      flag_set(smem, FL_KVG, g, lane);
    };
    if (ng & 1) gemm2_all<4, 5>(rsrc, hp0, wp, wsp, lo16, acc, stage_kv, kv_ready);
    else        gemm2_all<5, 4>(rsrc, hp0, wp, wsp, lo16, acc, stage_kv, kv_ready);
    flag_wait_all(smem, FL_PROG);
    LDSP char* mine   = smem + (g >> 1) * KV_HALF + (g & 1) * 24576 + lo16;
    LDSP char* theirs = smem + (g >> 1) * KV_HALF + ((g & 1) ^ 1) * 24576 + lo16;
    LDSP char* other  = smem + ((g >> 1) ^ 1) * KV_HALF + ((g & 1) ^ 1) * 24576 + lo16;
    auto reduce = [&](auto aa, auto hh_) {
      constexpr int A = decltype(aa)::value, HF = decltype(hh_)::value;
#pragma unroll
      for (int n = 0; n < 5; ++n)
#pragma unroll
        for (int q = 0; q < 4; ++q)
          *(LDSP f32x4*)(mine + (n * 4 + q) * 1024) =
              f32x4{acc[A ^ 1][n][4 * q], acc[A ^ 1][n][4 * q + 1], acc[A ^ 1][n][4 * q + 2], acc[A ^ 1][n][4 * q + 3]};
      WAIT_LGKM0();
      flag_set(smem, FL_GW, g, lane);
      flag_wait_one(smem, FL_GW, g ^ 1);
#pragma unroll
      for (int n = 0; n < 5; ++n)
#pragma unroll
        for (int q = 0; q < 4; ++q) {
          f32x4 v = *(LDSP f32x4*)(theirs + (n * 4 + q) * 1024);
#pragma unroll
          for (int i = 0; i < 4; ++i) acc[A][n][4 * q + i] += v[i];
        }
      WAIT_LGKM0();
#pragma unroll
      for (int n = 0; n < 5; ++n)
#pragma unroll
        for (int q = 0; q < 2; ++q) {
          constexpr int r0 = 8 * (HF ^ 1);
          *(LDSP f32x4*)(theirs + (n * 2 + q) * 1024) =
              f32x4{acc[A][n][r0 + 4 * q], acc[A][n][r0 + 4 * q + 1], acc[A][n][r0 + 4 * q + 2], acc[A][n][r0 + 4 * q + 3]};
        }
      WAIT_LGKM0();
      flag_set(smem, FL_GW2, g, lane);
      flag_wait_one(smem, FL_GW2, g ^ 2);
#pragma unroll
      for (int n = 0; n < 5; ++n) {
        const int nt = (n < 4) ? nt0 + n : nts;
        const int slot = (n < 4) ? kh : kh + 2 * (ng & 1);
        char* pp = ws + OFF_PART + (((size_t)slot * 64 + (2 * tp + A)) * 18 + nt) * 2048 + HF * 1024 + lo16;
        u32x4 o4;
#pragma unroll
        for (int q = 0; q < 2; ++q) {
          f32x4 v = *(LDSP f32x4*)(other + (n * 2 + q) * 1024);
#pragma unroll
          for (int i = 0; i < 4; ++i) v[i] += acc[A][n][8 * HF + 4 * q + i];
          o4[2 * q] = pack2(v[0], v[1]);
          o4[2 * q + 1] = pack2(v[2], v[3]);
        }
        store16(pp, o4);
      }
    };
    using I0 = std::integral_constant<int, 0>;
    using I1 = std::integral_constant<int, 1>;
    if (g == 0) reduce(I0{}, I0{});
    else if (g == 1) reduce(I1{}, I0{});
    else if (g == 2) reduce(I0{}, I1{});
    else reduce(I1{}, I1{});
  }
}

constexpr int K3_LDS = 6 * 6 * 4096;
__global__ __launch_bounds__(384)
void k3(const u16* __restrict__ x, const u16* __restrict__ b2, char* __restrict__ ws, u16* __restrict__ out) {
  extern __shared__ __attribute__((aligned(16))) char smem_[];
  LDSP char* smem = (LDSP char*)smem_;
  const int tid = threadIdx.x, lane = tid & 63;
  const int w = __builtin_amdgcn_readfirstlane(tid >> 6);
  const int b = blockIdx.x;
  const int tp = (b & 7) * 4 + (b >> 3) / 6, ng = (b >> 3) % 6;
  const int t32 = lane & 31, hh = lane >> 5;
  const int tt = 2 * tp + w / 3, nt = 3 * ng + w % 3;
  const int token = tt * 32 + t32;
  const char* ap = ws + OFF_AO + ((size_t)(2 * tp) * 36 + w * 6) * 1024 + lane * 16;
  const char* wp = ws + OFF_WOT + ((size_t)(w * 6) * 18 + 3 * ng) * 1024 + lane * 16;
  const bool shared = (nt == 4 || nt == 13);
  const size_t SL = (size_t)64 * 18 * 2048;
  const char* pp = ws + OFF_PART + ((size_t)tt * 18 + nt) * 2048 + lane * 16;
  const char* pp2 = shared ? pp + 2 * SL : pp;
  s16x8 af[2][6], wf[3][6];
  u32x4 pt[4][2];
  u32x2 xv[4], bv[4];
#pragma unroll
  for (int h = 0; h < 2; ++h) {
    pt[0][h] = *(const u32x4*)(pp + h * 1024);
    pt[1][h] = *(const u32x4*)(pp + SL + h * 1024);
    pt[2][h] = *(const u32x4*)(pp2 + h * 1024);
    pt[3][h] = *(const u32x4*)(pp2 + SL + h * 1024);
  }
#pragma unroll
  for (int q = 0; q < 4; ++q) {
    const int col = nt * 32 + 8 * q + 4 * hh;
    xv[q] = *(const u32x2*)((const char*)x + (size_t)token * 1152 + col * 2);
    bv[q] = *(const u32x2*)((const char*)b2 + col * 2);
  }
  SCHED();
#pragma unroll
  for (int k = 0; k < 6; ++k) {
    af[0][k] = *(const s16x8*)(ap + k * 1024);
    wf[0][k] = *(const s16x8*)(wp + k * 18432);
    af[1][k] = *(const s16x8*)(ap + 36 * 1024 + k * 1024);
    wf[1][k] = *(const s16x8*)(wp + k * 18432 + 1024);
    wf[2][k] = *(const s16x8*)(wp + k * 18432 + 2048);
  }
  SCHED();
  f32x16 acc[2][3];
#pragma unroll
  for (int i = 0; i < 2; ++i)
#pragma unroll
    for (int jn = 0; jn < 3; ++jn)
#pragma unroll
      for (int r = 0; r < 16; ++r) acc[i][jn][r] = 0.f;
#pragma unroll
  for (int k = 0; k < 6; ++k)
#pragma unroll
    for (int i = 0; i < 2; ++i)
#pragma unroll
      for (int jn = 0; jn < 3; ++jn) acc[i][jn] = MFMA32(wf[jn][k], af[i][k], acc[i][jn]);
#pragma unroll
  for (int i = 0; i < 2; ++i)
#pragma unroll
    for (int jn = 0; jn < 3; ++jn)
#pragma unroll
      for (int q = 0; q < 4; ++q) {
        f32x4 v = {acc[i][jn][4 * q], acc[i][jn][4 * q + 1], acc[i][jn][4 * q + 2], acc[i][jn][4 * q + 3]};
        *(LDSP f32x4*)(smem + ((w * 6 + i * 3 + jn) * 4 + q) * 1024 + lane * 16) = v;
      }
  const float sh = shared ? 1.f : 0.f;
  f32x4 fin[4];
#pragma unroll
  for (int q = 0; q < 4; ++q) {
#pragma unroll
    for (int e = 0; e < 2; ++e) {
      const unsigned p0 = pt[0][q >> 1][(q & 1) * 2 + e], p1 = pt[1][q >> 1][(q & 1) * 2 + e];
      const unsigned p2 = pt[2][q >> 1][(q & 1) * 2 + e], p3 = pt[3][q >> 1][(q & 1) * 2 + e];
      const float lo = (__builtin_bit_cast(float, p0 << 16) + __builtin_bit_cast(float, p1 << 16)) +
                       sh * (__builtin_bit_cast(float, p2 << 16) + __builtin_bit_cast(float, p3 << 16));
      const float hi = (__builtin_bit_cast(float, p0 & 0xffff0000u) + __builtin_bit_cast(float, p1 & 0xffff0000u)) +
                       sh * (__builtin_bit_cast(float, p2 & 0xffff0000u) + __builtin_bit_cast(float, p3 & 0xffff0000u));
      fin[q][2 * e]     = lo + __builtin_bit_cast(float, xv[q][e] << 16) + __builtin_bit_cast(float, bv[q][e] << 16);
      fin[q][2 * e + 1] = hi + __builtin_bit_cast(float, xv[q][e] & 0xffff0000u) + __builtin_bit_cast(float, bv[q][e] & 0xffff0000u);
    }
  }
  __syncthreads();
#pragma unroll
  for (int q = 0; q < 4; ++q) {
    f32x4 v = fin[q];
#pragma unroll
    for (int o = 0; o < 6; ++o) v += *(LDSP f32x4*)(smem + ((o * 6 + w) * 4 + q) * 1024 + lane * 16);
    u32x2 ov = {pack2(v[0], v[1]), pack2(v[2], v[3])};
    *(u32x2*)((char*)out + (size_t)token * 1152 + (nt * 32 + 8 * q + 4 * hh) * 2) = ov;
  }
}

extern "C" void kernel_launch(void* const* d_in, const int* in_sizes, int n_in,
                              void* d_out, int out_size, void* d_ws, size_t ws_size,
                              hipStream_t stream) {
  const u16* x  = (const u16*)d_in[0];
  const u16* wq = (const u16*)d_in[1];
  const u16* wk = (const u16*)d_in[2];
  const u16* wv = (const u16*)d_in[3];
  const u16* wo = (const u16*)d_in[4];
  const u16* w1 = (const u16*)d_in[6];
  const u16* b1 = (const u16*)d_in[7];
  const u16* w2 = (const u16*)d_in[8];
  const u16* b2 = (const u16*)d_in[9];
  char* ws = (char*)d_ws;
  (void)hipFuncSetAttribute((const void*)k1, hipFuncAttributeMaxDynamicSharedMemorySize, K1_LDS);
  (void)hipFuncSetAttribute((const void*)k2, hipFuncAttributeMaxDynamicSharedMemorySize, K2_LDS);
  (void)hipFuncSetAttribute((const void*)k3, hipFuncAttributeMaxDynamicSharedMemorySize, K3_LDS);
  k1<<<256, 512, K1_LDS, stream>>>(x, wq, wk, wv, wo, w1, b1, w2, ws);
  k2<<<256, 512, K2_LDS, stream>>>(ws);
  k3<<<192, 384, K3_LDS, stream>>>(x, b2, ws, (u16*)d_out);
}
```

```cpp
#include <hip/hip_runtime.h>
#include <hip/hip_bf16.h>
#include <stdint.h>
#include <type_traits>

#define DI __device__ __forceinline__
#define LDSP __attribute__((address_space(3)))
#define GLP __attribute__((address_space(1)))

typedef unsigned short u16;
typedef __attribute__((ext_vector_type(8))) short s16x8;
typedef __attribute__((ext_vector_type(4))) short s16x4;
typedef __attribute__((ext_vector_type(16))) float f32x16;
typedef __attribute__((ext_vector_type(4))) float f32x4;
typedef __attribute__((ext_vector_type(2))) float f32x2;
typedef __attribute__((ext_vector_type(2))) __bf16 bf16x2;
typedef __attribute__((ext_vector_type(4))) unsigned u32x4;
typedef __attribute__((ext_vector_type(2))) unsigned u32x2;

constexpr size_t OFF_H    = 0;
constexpr size_t OFF_W2T  = OFF_H + 18874368;
constexpr size_t OFF_WOT  = OFF_W2T + 5308416;
constexpr size_t OFF_Q    = OFF_WOT + 663552;
constexpr size_t OFF_KV   = OFF_Q + 2359296;
constexpr size_t OFF_AO   = OFF_KV + 159744;
constexpr size_t OFF_PART = OFF_AO + 2359296;
constexpr int KV_GRP = 9984, KV_TILE = 2240, KV_BYTES = 159744, KV_HALF = 79872;

#define MFMA32(a, b, c) __builtin_amdgcn_mfma_f32_32x32x16_bf16((a), (b), (c), 0, 0, 0)
#define WAIT_VM(n) asm volatile("s_waitcnt vmcnt(" #n ")" ::: "memory")
#define WAIT_LGKM0() asm volatile("s_waitcnt lgkmcnt(0)" ::: "memory")
#define BAR() __builtin_amdgcn_s_barrier()

DI unsigned pack2(float a, float b) {
  f32x2 v = {a, b};
  return __builtin_bit_cast(unsigned, __builtin_convertvector(v, bf16x2));
}
DI void glds16(const void* gbase, unsigned voff, LDSP char* l) {
  unsigned keep;
  asm volatile("s_mov_b32 %0, m0\n\ts_mov_b32 m0, %3\n\ts_nop 0\n\tglobal_load_lds_dwordx4 %1, %2\n\ts_mov_b32 m0, %0"
               : "=&s"(keep) : "v"(voff), "s"(gbase), "s"((unsigned)(size_t)l) : "memory");
}
DI void glds4(const void* gbase, unsigned voff, LDSP char* l) {
  unsigned keep;
  asm volatile("s_mov_b32 %0, m0\n\ts_mov_b32 m0, %3\n\ts_nop 0\n\tglobal_load_lds_dword %1, %2\n\ts_mov_b32 m0, %0"
               : "=&s"(keep) : "v"(voff), "s"(gbase), "s"((unsigned)(size_t)l) : "memory");
}
DI void store16(void* p, u32x4 v) {
  asm volatile("global_store_dwordx4 %0, %1, off sc1\n\ts_nop 1" :: "v"(p), "v"(v) : "memory");
}
DI s16x8 tr_frag(LDSP char* p, int second_off) {
  s16x4 lo = __builtin_amdgcn_ds_read_tr16_b64_v4i16((LDSP s16x4*)p);
  s16x4 hi = __builtin_amdgcn_ds_read_tr16_b64_v4i16((LDSP s16x4*)(p + second_off));
  return __builtin_shufflevector(lo, hi, 0, 1, 2, 3, 4, 5, 6, 7);
}
DI float other_half(float v) {
  unsigned u = __builtin_bit_cast(unsigned, v);
  auto r = __builtin_amdgcn_permlane32_swap(u, u, false, false);
  unsigned o = (threadIdx.x & 32) ? r[0] : r[1];
  return __builtin_bit_cast(float, o);
}

constexpr int K1_SLOT = 36864;
constexpr int K1_BIAS = 4 * K1_SLOT;
constexpr int K1_CS   = K1_BIAS + 1152;
constexpr int K1_CSQ  = K1_CS + 1152;
constexpr int K1_STAT = K1_CSQ + 1024;
constexpr int K1_LDS  = K1_STAT + 2048;

__host__ __device__ constexpr int w_slot(int st) { return (0x231023120ll >> (4 * st)) & 3; }

#define SCHED() __builtin_amdgcn_sched_barrier(0)
#define SGB(mask, n) __builtin_amdgcn_sched_group_barrier((mask), (n), 0)

DI u32x2 gelu4(float a0, float a1, float a2, float a3, const f32x4& cs, const f32x4& bb, float negm, float r, float c0) {
  float z0, z1, z2, z3, t0, t1, t2, t3;
  unsigned o0, o1;
  asm volatile(
      "v_fma_f32 %2, %18, %14, %10\n\tv_fma_f32 %3, %18, %15, %11\n\tv_fma_f32 %4, %18, %16, %12\n\tv_fma_f32 %5, %18, %17, %13\n\t"
      "v_fma_f32 %2, %19, %2, %21\n\tv_fma_f32 %3, %19, %3, %22\n\tv_fma_f32 %4, %19, %4, %23\n\tv_fma_f32 %5, %19, %5, %24\n\t"
      "v_mul_f32 %6, %2, %2\n\tv_mul_f32 %7, %3, %3\n\tv_mul_f32 %8, %4, %4\n\tv_mul_f32 %9, %5, %5\n\t"
      "v_fmamk_f32 %6, %6, 0xbdd2d3e8, %20\n\tv_fmamk_f32 %7, %7, 0xbdd2d3e8, %20\n\tv_fmamk_f32 %8, %8, 0xbdd2d3e8, %20\n\tv_fmamk_f32 %9, %9, 0xbdd2d3e8, %20\n\t"
      "v_mul_f32 %6, %2, %6\n\tv_mul_f32 %7, %3, %7\n\tv_mul_f32 %8, %4, %8\n\tv_mul_f32 %9, %5, %9\n\t"
      "v_exp_f32 %6, %6\n\tv_exp_f32 %7, %7\n\tv_exp_f32 %8, %8\n\tv_exp_f32 %9, %9\n\t"
      "v_add_f32 %6, 1.0, %6\n\tv_add_f32 %7, 1.0, %7\n\tv_add_f32 %8, 1.0, %8\n\tv_add_f32 %9, 1.0, %9\n\t"
      "v_rcp_f32 %6, %6\n\tv_rcp_f32 %7, %7\n\tv_rcp_f32 %8, %8\n\tv_rcp_f32 %9, %9\n\t"
      "v_mul_f32 %2, %2, %6\n\tv_mul_f32 %3, %3, %7\n\tv_mul_f32 %4, %4, %8\n\tv_mul_f32 %5, %5, %9\n\t"
      "v_cvt_pk_bf16_f32 %0, %2, %3\n\tv_cvt_pk_bf16_f32 %1, %4, %5"
      : "=&v"(o0), "=&v"(o1), "=&v"(z0), "=&v"(z1), "=&v"(z2), "=&v"(z3), "=&v"(t0), "=&v"(t1), "=&v"(t2), "=&v"(t3)
      : "v"(a0), "v"(a1), "v"(a2), "v"(a3), "v"(cs[0]), "v"(cs[1]), "v"(cs[2]), "v"(cs[3]), "v"(negm), "v"(r), "v"(c0),
        "v"(bb[0]), "v"(bb[1]), "v"(bb[2]), "v"(bb[3]));
  u32x2 o = {o0, o1};
  return o;
}

template <typename F>
DI void gelu_tile(const f32x16& a, LDSP char* cs_p, LDSP char* bias_p, char* out_p, float negm, float r, F&& between) {
  const float c0 = -2.302208198f;
#pragma unroll
  for (int hf = 0; hf < 2; ++hf) {
    f32x4 cs0 = *(LDSP f32x4*)(cs_p + hf * 64), cs1 = *(LDSP f32x4*)(cs_p + hf * 64 + 32);
    f32x4 bb0 = *(LDSP f32x4*)(bias_p + hf * 64), bb1 = *(LDSP f32x4*)(bias_p + hf * 64 + 32);
    between(2 * hf);
    u32x2 lo = gelu4(a[8 * hf], a[8 * hf + 1], a[8 * hf + 2], a[8 * hf + 3], cs0, bb0, negm, r, c0);
    between(2 * hf + 1);
    u32x2 hi = gelu4(a[8 * hf + 4], a[8 * hf + 5], a[8 * hf + 6], a[8 * hf + 7], cs1, bb1, negm, r, c0);
    u32x4 o = {lo[0], lo[1], hi[0], hi[1]};
    store16(out_p + hf * 1024, o);
  }
}

DI void colsum_frag(const s16x8& a, const s16x8& sel, f32x4& cs) {
  cs = __builtin_amdgcn_mfma_f32_16x16x32_bf16(sel, a, cs, 0, 0, 0);
}

template <int KT>
DI void w1_stage(LDSP char* sb0, LDSP char* sb1, LDSP char* sb2, const s16x8 (&xf)[36], f32x16 (&acc)[3], const s16x8& sel, f32x4& cs) {
  asm volatile("" : "+v"(sb0), "+v"(sb1), "+v"(sb2));
  s16x8 a[2][3];
  a[0][0] = tr_frag(sb0, 768); a[0][1] = tr_frag(sb1, 768); a[0][2] = tr_frag(sb2, 768);
#pragma unroll
  for (int ks = 0; ks < 12; ++ks) {
    const int cur = ks & 1;
    if (ks + 1 < 12) {
      a[cur ^ 1][0] = tr_frag(sb0 + (ks + 1) * 3072, 768);
      a[cur ^ 1][1] = tr_frag(sb1 + (ks + 1) * 3072, 768);
      a[cur ^ 1][2] = tr_frag(sb2 + (ks + 1) * 3072, 768);
    }
#pragma unroll
    for (int t = 0; t < 3; ++t) acc[t] = MFMA32(a[cur][t], xf[12 * KT + ks], acc[t]);
    colsum_frag(a[cur][0], sel, cs);
    SGB(0x100, 2); SGB(0x8, 1); SGB(0x100, 2); SGB(0x8, 1); SGB(0x100, 2); SGB(0x8, 2);
    SCHED();
  }
}

DI void qkv_stage(LDSP char* sb, const s16x8 (&xf)[36], f32x16& qa, const s16x8& sel, f32x4& cs) {
  asm volatile("" : "+v"(sb));
  s16x8 a[3];
  a[0] = tr_frag(sb, 256);
  a[1] = tr_frag(sb + 1024, 256);
#pragma unroll
  for (int s = 0; s < 36; ++s) {
    if (s + 2 < 36) a[(s + 2) % 3] = tr_frag(sb + (s + 2) * 1024, 256);
    qa = MFMA32(a[s % 3], xf[s], qa);
    colsum_frag(a[s % 3], sel, cs);
    SCHED();
  }
}

__global__ __launch_bounds__(512, 2)
void k1(const u16* __restrict__ x, const u16* __restrict__ wq, const u16* __restrict__ wk, const u16* __restrict__ wv,
        const u16* __restrict__ wo, const u16* __restrict__ w1, const u16* __restrict__ b1, const u16* __restrict__ w2,
        char* __restrict__ ws) {
  extern __shared__ __attribute__((aligned(16))) char smem_[];
  LDSP char* smem = (LDSP char*)smem_;
  const int tid = threadIdx.x, lane = tid & 63;
  const int w8 = __builtin_amdgcn_readfirstlane(tid >> 6);
  const int w = w8 & 3, grp = w8 >> 2;
  const int b = blockIdx.x, xcd = b & 7, idx = b >> 3;
  const int tg = (xcd >> 1) * 4 + (idx & 3), j = (xcd & 1) * 8 + (idx >> 2);
  const int t32 = lane & 31, hh = lane >> 5;
  const int tt = tg * 4 + w;
  const bool heavy = (j & 3) == 0;
  constexpr int rk = 0, rc = 0;

  const char* w1w = (const char*)w1 + (size_t)(48 * w) * 9216 + (size_t)j * 576;
  unsigned off3[3];
#pragma unroll
  for (int i = 0; i < 3; ++i) {
    const unsigned bb = 1024 * i + 16 * lane;
    off3[i] = (bb / 192) * 9216 + (bb % 192);
  }
  auto issue_w1_rows = [&](int st, int r) {
    const int c = st / 3, kt = st - 3 * c;
    const char* g = w1w + ((c + rc) % 3) * 192 + (size_t)((kt + rk) % 3) * 192 * 9216 + r * 16 * 9216;
    LDSP char* l = smem + w_slot(st) * K1_SLOT + w * 9216 + r * 3072;
#pragma unroll
    for (int i = 0; i < 3; ++i) glds16(g, off3[i], l + i * 1024);
  };
  auto issue_w1 = [&](int st) {
#pragma unroll
    for (int r = 0; r < 3; ++r) issue_w1_rows(st, r);
  };
  auto issue_x = [&](int q, int slot) {
    int ln = tid & 63; asm volatile("" : "+v"(ln));
    const char* g = (const char*)x + (size_t)(tt * 32) * 1152;
    LDSP char* l = smem + slot * K1_SLOT + w * 9216;
    unsigned row = (unsigned)ln / 18, pos = (unsigned)ln % 18;
    const unsigned b0 = q * 288 + rk * 384;
#pragma unroll
    for (int i = 0; i < 9; ++i) {
      unsigned cb = b0 + ((pos ^ ((row >> 3) & 1)) << 4);
      cb = cb >= 1152 ? cb - 1152 : cb;
      glds16(g, row * 1152 + cb, l + i * 1024);
      pos += 10; row += 3;
      if (pos >= 18) { pos -= 18; row += 1; }
    }
  };
  auto issue_qkv = [&](int tile, int slot, int part) {
    int ln = tid & 63; asm volatile("" : "+v"(ln));
    LDSP char* l = smem + slot * K1_SLOT + w * 9216 + part * 3072;
    const int row0 = 144 * w + 48 * part + 192 * rk;
    if (tile < 18) {
      const char* g = (const char*)wq + tile * 64;
      const unsigned vo = (ln >> 2) * 1152 + (ln & 3) * 16;
#pragma unroll
      for (int i = 0; i < 3; ++i) glds16(g + (size_t)((row0 + 16 * i) % 576) * 1152, vo, l + i * 1024);
    } else {
      const u16* src = (tile == 18) ? wk : wv;
      int dw = ln & 15; dw = dw > 8 ? 8 : dw;
      const unsigned vo = (ln >> 4) * 36 + dw * 4;
#pragma unroll
      for (int i = 0; i < 12; ++i) glds4((const char*)src + ((row0 + 4 * i) % 576) * 36, vo, l + i * 256);
    }
  };
  const int lw = (tg * 12 + (j - (j >> 2) - 1)) * 4 + w;
  auto issue_unit = [&](int part) {
    if (lw < 648) {
      int ln = tid & 63; asm volatile("" : "+v"(ln));
      LDSP char* l = smem + 0 * K1_SLOT + w * 9216 + part * 3072;
      const int u = lw - 576, ks = u >> 1;
      const bool plain = ks < 32;
      const char* g = lw < 576 ? (const char*)w2 + (size_t)((lw >> 1) * 16) * 1152 + (lw & 1) * 576
                               : (const char*)wo + (size_t)(plain ? ks * 18 : 144 * (ks - 32) + 16) * 1152 + (u & 1) * 576;
      const unsigned p0 = 192 * part + ln;
      unsigned row = p0 / 36, pos = p0 % 36;
#pragma unroll
      for (int i = 0; i < 3; ++i) {
        const unsigned srow = plain ? row : 18 * (row >> 1) + (row & 1);
        glds16(g, srow * 1152 + pos * 16, l + i * 1024);
        pos += 28; row += 1;
        if (pos >= 36) { pos -= 36; row += 1; }
      }
    }
  };

  if (w8 == 7) {
    unsigned long long pc;
    asm volatile("s_getpc_b64 %0" : "=s"(pc));
    const char* cb = (const char*)(pc & ~255ull);
    glds16(cb + idx * 2048, lane * 16, smem + K1_STAT + 1024);
    glds16(cb + (idx < 31 ? idx * 2048 + 1024 : 0), lane * 16, smem + K1_STAT + 1024);
  }
  unsigned bias_raw = 0, bias_raw2 = 0;
  if (grp == 1) {
    asm volatile("global_load_ushort %0, %2, %4\n\tglobal_load_ushort %1, %3, %4" : "=&v"(bias_raw), "=&v"(bias_raw2)
                 : "v"((unsigned)((tid - 256) * 2)), "v"((unsigned)((tid < 288 ? tid : 287) * 2)), "s"(b1 + j * 288) : "memory");
  }
  if (grp == 0) { issue_x(0, 3); issue_x(1, 1); }
  else          { issue_w1(0);   issue_w1(1); }
  s16x8 xf[36];
  const int xl = w * 9216 + t32 * 288 + ((hh ^ ((t32 >> 3) & 1)) << 4);
  auto read_x = [&](int q, int slot) {
#pragma unroll
    for (int s = 0; s < 9; ++s) xf[9 * q + s] = *(LDSP s16x8*)(smem + slot * K1_SLOT + xl + s * 32);
  };
  if (grp == 0) WAIT_VM(9);
  BAR();
  read_x(0, 3);
  if (grp == 0) { WAIT_VM(0); }
  else {
    asm volatile("s_waitcnt vmcnt(9)" : "+v"(bias_raw), "+v"(bias_raw2) :: "memory");
    *(LDSP float*)(smem + K1_BIAS + (tid - 256) * 4) = __builtin_bit_cast(float, bias_raw << 16);
    if (tid < 288) *(LDSP float*)(smem + K1_BIAS + tid * 4) = __builtin_bit_cast(float, bias_raw2 << 16);
  }
  WAIT_LGKM0(); BAR();
  read_x(1, 1);
  WAIT_LGKM0(); BAR();

  const int q4 = (lane & 15) >> 2, p4 = lane & 3, g16 = (lane >> 4) & 1;
  const int rot = (w == 3) ? 0 : w;
  const int tl0 = rot, tl1 = (rot + 1) % 3, tl2 = (rot + 2) % 3;
  const int lo_w1 = (8 * hh + q4) * 192 + 32 * g16 + 8 * p4;
  const int lo_q  = (8 * hh + q4) * 64 + 32 * g16 + 8 * p4;
  char* hout = ws + OFF_H + ((size_t)tt * 288 + (size_t)j * 18) * 1024 + lane * 16;
  f32x16 acc[3];
  float negm = 0.f, r1 = 0.f, rm = 0.f;
  const unsigned selv = ((lane & 15) == ((lane >> 4) & 1)) ? 0x3f803f80u : 0u;
  const u32x4 selu = {selv, selv, selv, selv};
  const s16x8 sel = __builtin_bit_cast(s16x8, selu);

  auto load_stats = [&]() {
    f32x2 st = *(LDSP f32x2*)(smem + K1_STAT + (w * 32 + t32) * 8);
    negm = -st[0] * (1.0f / 576.0f);
    const float var = __builtin_fmaxf(st[1] * (1.0f / 576.0f) - negm * negm, 0.f);
    auto rb = [](float v) { return __builtin_bit_cast(float, pack2(v, 0.f) << 16); };
    const float s1 = rb(__builtin_sqrtf(rb(rb(var) + 1e-5f)));
    r1 = 1.0f / s1;
    const float s2 = rb(__builtin_sqrtf(rb(rb(var * r1 * r1) + 1e-5f)));
    rm = r1 / s2;
  };
  auto mfma_chunk = [&](auto cc) {
    constexpr int c = decltype(cc)::value;
    __builtin_amdgcn_s_setprio(0);
    f32x4 cs = {0.f, 0.f, 0.f, 0.f};
#pragma unroll
    for (int t = 0; t < 3; ++t)
#pragma unroll
      for (int i = 0; i < 16; ++i) acc[t][i] = 0.f;
    LDSP char* s0 = smem + lo_w1 + tl0 * 64;
    LDSP char* s1 = smem + lo_w1 + tl1 * 64;
    LDSP char* s2 = smem + lo_w1 + tl2 * 64;
    constexpr int o0 = w_slot(3 * c) * K1_SLOT, o1 = w_slot(3 * c + 1) * K1_SLOT, o2 = w_slot(3 * c + 2) * K1_SLOT;
    w1_stage<0>(s0 + o0, s1 + o0, s2 + o0, xf, acc, sel, cs);
    if (c == 1) { WAIT_VM(9); } else if (c == 2) { WAIT_VM(11); }
    WAIT_LGKM0(); BAR();
    if (c == 0) { read_x(2, 3); WAIT_LGKM0(); BAR(); }
    w1_stage<1>(s0 + o1, s1 + o1, s2 + o1, xf, acc, sel, cs);
    if (c != 0) WAIT_VM(0);
    WAIT_LGKM0(); BAR();
    if (c == 0) { read_x(3, 0); WAIT_LGKM0(); BAR(); }
    w1_stage<2>(s0 + o2, s1 + o2, s2 + o2, xf, acc, sel, cs);
    if (lane < 16 && w < 3) {
      *(LDSP float*)(smem + K1_CS + (c * 96 + tl0 * 32 + lane) * 4) = cs[0];
      *(LDSP float*)(smem + K1_CS + (c * 96 + tl0 * 32 + 16 + lane) * 4) = cs[1];
    }
    WAIT_LGKM0(); BAR();
    __builtin_amdgcn_s_setprio(1);
  };
  auto gelu_t = [&](int c, int t, int tl, int st_issue) {
    const int ca = (c + rc) % 3;
    gelu_tile(acc[t], smem + K1_CS + (c * 96 + tl * 32) * 4 + hh * 16, smem + K1_BIAS + (ca * 96 + tl * 32) * 4 + hh * 16,
              hout + (ca * 6 + tl * 2) * 1024, negm, rm, [&](int i) {
      if (i < 3) {
        if (st_issue >= 0 && st_issue < 9) issue_w1_rows(st_issue, i);
        else if (st_issue == 9) issue_qkv(j, 1, i);
        else if (st_issue == 10) { if (heavy) issue_qkv(16 + (j >> 2), 0, i); else issue_unit(i); }
      }
    });
  };

  if (grp == 0) {
    mfma_chunk(std::integral_constant<int, 0>{});
    load_stats();
    gelu_t(0, 0, tl0, 6); WAIT_LGKM0(); BAR();
    gelu_t(0, 1, tl1, 7); WAIT_LGKM0(); BAR();
    gelu_t(0, 2, tl2, 8); WAIT_VM(22); WAIT_LGKM0(); BAR();
    mfma_chunk(std::integral_constant<int, 2>{});
    gelu_t(2, 0, tl0, -1); gelu_t(2, 1, tl1, -1); gelu_t(2, 2, tl2, -1);
  } else {
    f32x4 rs = {0.f, 0.f, 0.f, 0.f};
#pragma unroll
    for (int i = 0; i < 16; ++i) acc[0][i] = 0.f;
    auto stats_frag = [&](int s) {
      colsum_frag(xf[s], sel, rs);
      acc[0] = MFMA32(xf[s], xf[s], acc[0]);
    };
    __builtin_amdgcn_s_setprio(1);
    issue_x(2, 3);
#pragma unroll
    for (int s = 0; s < 18; ++s) { if (s % 6 == 0) issue_w1_rows(2, s / 6); stats_frag(s); }
    WAIT_VM(9); BAR();
    read_x(2, 3); WAIT_LGKM0(); BAR();
    issue_x(3, 0);
#pragma unroll
    for (int s = 18; s < 27; ++s) { if (s % 3 == 0) issue_w1_rows(3, (s - 18) / 3); stats_frag(s); }
    WAIT_VM(9); BAR();
    read_x(3, 0); WAIT_LGKM0(); BAR();
    issue_w1(4);
#pragma unroll
    for (int s = 27; s < 36; ++s) { if (s % 3 == 0) issue_w1_rows(5, (s - 27) / 3); stats_frag(s); }
    {
      const int dreg = (t32 & 3) + 4 * (t32 >> 3);
      float s2 = 0.f;
#pragma unroll
      for (int i = 0; i < 16; ++i) s2 = (i == dreg) ? acc[0][i] : s2;
      if (hh == ((t32 >> 2) & 1)) *(LDSP float*)(smem + K1_STAT + (w * 32 + t32) * 8 + 4) = s2;
      if (lane < 16) {
        *(LDSP float*)(smem + K1_STAT + (w * 32 + lane) * 8) = rs[0];
        *(LDSP float*)(smem + K1_STAT + (w * 32 + 16 + lane) * 8) = rs[1];
      }
    }
    WAIT_VM(18); WAIT_LGKM0(); BAR();
    mfma_chunk(std::integral_constant<int, 1>{});
    load_stats();
    gelu_t(1, 0, tl0, 10); WAIT_LGKM0(); BAR();
    gelu_t(1, 1, tl1, 9); WAIT_LGKM0(); BAR();
    gelu_t(1, 2, tl2, -1);
    WAIT_VM(2); WAIT_LGKM0(); BAR();
    __builtin_amdgcn_s_setprio(2);

    auto qkv_tile = [&](int tile, int slot) {
      f32x16 qa;
#pragma unroll
      for (int i = 0; i < 16; ++i) qa[i] = 0.f;
      f32x4 cs = {0.f, 0.f, 0.f, 0.f};
      qkv_stage(smem + slot * K1_SLOT + lo_q, xf, qa, sel, cs);
      int ln = tid & 63; asm volatile("" : "+v"(ln));
      const int t32 = ln & 31, hh = ln >> 5, token = tt * 32 + t32;
      LDSP char* cp = smem + K1_CSQ + (w * 2 + (slot & 1)) * 128;
      if (ln < 16) { *(LDSP float*)(cp + ln * 4) = cs[0]; *(LDSP float*)(cp + 64 + ln * 4) = cs[1]; }
      WAIT_LGKM0();
#pragma unroll
      for (int g = 0; g < 4; ++g) {
        f32x4 c4 = *(LDSP f32x4*)(cp + g * 32 + hh * 16);
#pragma unroll
        for (int i = 0; i < 4; ++i) qa[4 * g + i] = __builtin_fmaf(negm, c4[i], qa[4 * g + i]);
      }
      if (tile < 18) {
        const float sc = r1 * (0.23570226039551584f * 1.4426950408889634f);
        char* qp = ws + OFF_Q + (size_t)tt * 36864;
#pragma unroll
        for (int i = 0; i < 8; ++i) {
          const unsigned n = tile * 32 + 8 * (i >> 1) + 4 * hh + 2 * (i & 1);
          const unsigned head = n / 18, d = n - head * 18;
          const unsigned off = d < 16 ? (t32 + 32 * ((d >> 2) & 1)) * 16 + ((d >> 3) * 2 + ((d >> 1) & 1)) * 4 : 1024 + t32 * 4;
          *(unsigned*)(qp + head * 1152 + off) = pack2(qa[2 * i] * sc, qa[2 * i + 1] * sc);
        }
      } else if (tile == 18) {
        u32x4 o = {pack2(qa[0] * r1, qa[1] * r1), pack2(qa[2] * r1, qa[3] * r1),
                   pack2(qa[4] * r1, qa[5] * r1), pack2(qa[6] * r1, qa[7] * r1)};
        char* gp = ws + OFF_KV + (size_t)tg * KV_GRP;
        *(u32x4*)(gp + 1024 + w * KV_TILE + ln * 16) = o;
        if (hh == 0) *(unsigned*)(gp + (w >> 1) * 512 + t32 * 16 + (w & 1) * 4) = pack2(qa[8] * r1, qa[9] * r1);
        else if ((w & 1) == 0) {
          u32x2 cst = {w == 0 ? 0x3f803f80u : 0u, 0u};
          *(u32x2*)(gp + (w >> 1) * 512 + t32 * 16 + 8) = cst;
        }
      } else {
        const int s = t32 >> 4, hk = (t32 >> 2) & 1, jj = ((t32 >> 3) & 1) * 4 + (t32 & 3);
        char* vp = ws + OFF_KV + (size_t)tg * KV_GRP + 2048 + w * KV_TILE + (s * 2 + hk) * 304 + jj * 2;
#pragma unroll
        for (int i = 0; i < 8; ++i) {
          const int d = (i & 3) + 8 * (i >> 2) + 4 * hh;
          *(u16*)(vp + d * 16) = (u16)(pack2(qa[i] * r1, 0.f) & 0xffffu);
        }
        if (hh == 0) {
          *(u16*)(vp + 16 * 16) = (u16)(pack2(qa[8] * r1, 0.f) & 0xffffu);
          *(u16*)(vp + 17 * 16) = (u16)(pack2(qa[9] * r1, 0.f) & 0xffffu);
          *(u16*)(vp + 18 * 16) = (u16)0x3f80;
        }
      }
    };
    qkv_tile(j, 1);
    if (heavy) {
      qkv_tile(16 + (j >> 2), 0);
    } else if (lw < 648) {
      int ln = tid & 63; asm volatile("" : "+v"(ln));
      const int hh = ln >> 5, q4 = (ln & 15) >> 2, p4 = ln & 3, g16 = (ln >> 4) & 1;
      LDSP char* sb = smem + 0 * K1_SLOT + w * 9216 + (4 * hh + q4) * 576 + 32 * g16 + 8 * p4;
      char* dst = (lw < 576) ? ws + OFF_W2T + ((size_t)(lw >> 1) * 18 + (lw & 1) * 9) * 1024
                             : ws + OFF_WOT + ((size_t)((lw - 576) >> 1) * 18 + (lw & 1) * 9) * 1024;
#pragma unroll
      for (int n = 0; n < 9; ++n) {
        s16x8 f = tr_frag(sb + n * 64, 8 * 576);
        store16(dst + n * 1024 + ln * 16, __builtin_bit_cast(u32x4, f));
      }
    }
  }
}

constexpr int K2_FLAG = KV_BYTES;
constexpr int K2_LDS  = KV_BYTES + 128;
enum { FL_KVA = 0, FL_KVG = 1, FL_PROG = 2, FL_GW = 3, FL_DONE = 4, FL_SENT = 5, FL_GW2 = 6 };

#define EXP2(x) __builtin_amdgcn_exp2f(x)

DI void flag_set(LDSP char* smem, int set, int i, int lane) {
  if (lane == 0) *(volatile LDSP unsigned*)(smem + K2_FLAG + set * 16 + i * 4) = 1u;
}
DI void flag_wait_all(LDSP char* smem, int set) {
  while (true) {
    u32x4 f = *(volatile LDSP u32x4*)(smem + K2_FLAG + set * 16);
    if (f[0] & f[1] & f[2] & f[3]) break;
    __builtin_amdgcn_s_sleep(2);
  }
  asm volatile("" ::: "memory");
}
DI void flag_wait_one(LDSP char* smem, int set, int i) {
  while (*(volatile LDSP unsigned*)(smem + K2_FLAG + set * 16 + i * 4) == 0u) __builtin_amdgcn_s_sleep(1);
  asm volatile("" ::: "memory");
}

DI void attn_step(f32x16& scur, f32x16& snext, const s16x8& k0, const s16x8& k1, const s16x8& qn0, const s16x8& qn1,
                  const s16x8& vprev1, f32x16& oprev, const s16x8& vcur0, f32x16& ocur, u32x4& p0, u32x4& p1) {
  f32x16 zero;
#pragma unroll
  for (int i = 0; i < 16; ++i) zero[i] = 0.f;
#define EX(i) do { float t_ = scur[i]; asm volatile("v_exp_f32 %0, %0" : "+v"(t_)); scur[i] = t_; } while (0)
  p1[2] = pack2(snext[12], snext[13]);
  p1[3] = pack2(snext[14], snext[15]);
  SCHED();
  snext = MFMA32(k0, qn0, zero);
  SCHED();
#pragma unroll
  for (int i = 0; i < 3; ++i) EX(i);
  SCHED();
  snext = MFMA32(k1, qn1, snext);
  SCHED();
#pragma unroll
  for (int i = 3; i < 8; ++i) EX(i);
  SCHED();
  p0[0] = pack2(scur[0], scur[1]);
  p0[1] = pack2(scur[2], scur[3]);
  p0[2] = pack2(scur[4], scur[5]);
  p0[3] = pack2(scur[6], scur[7]);
  SCHED();
  oprev = MFMA32(vprev1, __builtin_bit_cast(s16x8, p1), oprev);
  SCHED();
#pragma unroll
  for (int i = 8; i < 8 + 3; ++i) EX(i);
  SCHED();
  ocur = MFMA32(vcur0, __builtin_bit_cast(s16x8, p0), ocur);
  SCHED();
#pragma unroll
  for (int i = 8 + 3; i < 16; ++i) EX(i);
  SCHED();
  p1[0] = pack2(scur[8], scur[9]);
  p1[1] = pack2(scur[10], scur[11]);
  SCHED();
#undef EX
}

DI s16x8 make_q1(unsigned qe, unsigned mneg, int i, int hh) {
  const unsigned a = (hh == (i >> 1)) ? qe : 0u;
  u32x4 r = {(i & 1) ? 0u : a, (i & 1) ? a : 0u, hh ? 0u : mneg, 0u};
  return __builtin_bit_cast(s16x8, r);
}
DI unsigned neg_hi_lo(float m) {
  unsigned hi = pack2(-m, 0.f) & 0xffffu;
  float rem = -m - __builtin_bit_cast(float, hi << 16);
  unsigned lo = pack2(rem, 0.f) & 0xffffu;
  return hi | (lo << 16);
}
DI int v_row(int t32) { return t32 > 18 ? ((t32 >= 20 && t32 < 28) ? 0 : 4) : t32; }

DI f32x16 attn_exact(const char* kv, int lane, u32x4 q0u, unsigned qe) {
  const int t32 = lane & 31, hh = lane >> 5;
  const s16x8 q0 = __builtin_bit_cast(s16x8, q0u);
  f32x16 zero, o;
#pragma unroll
  for (int i = 0; i < 16; ++i) { zero[i] = 0.f; o[i] = 0.f; }
  float m = -INFINITY;
#pragma unroll 1
  for (int T = 0; T < 64; ++T) {
    const char* gp = kv + (T >> 2) * KV_GRP;
    s16x8 k0 = *(const s16x8*)(gp + 1024 + (T & 3) * KV_TILE + lane * 16);
    s16x8 k1 = *(const s16x8*)(gp + lane * 16);
    f32x16 s = MFMA32(k0, q0, zero);
    s = MFMA32(k1, make_q1(qe, 0u, T & 3, hh), s);
#pragma unroll
    for (int i = 0; i < 16; ++i) m = __builtin_fmaxf(m, s[i]);
  }
  m = __builtin_fmaxf(m, other_half(m));
  const unsigned mneg = neg_hi_lo(m);
#pragma unroll 1
  for (int T = 0; T < 64; ++T) {
    const char* gp = kv + (T >> 2) * KV_GRP;
    const char* tp = gp + 1024 + (T & 3) * KV_TILE;
    s16x8 k0 = *(const s16x8*)(tp + lane * 16);
    s16x8 k1 = *(const s16x8*)(gp + lane * 16);
    s16x8 v0 = *(const s16x8*)(tp + 1024 + hh * 304 + v_row(t32) * 16);
    s16x8 v1 = *(const s16x8*)(tp + 1024 + 608 + hh * 304 + v_row(t32) * 16);
    f32x16 s = MFMA32(k0, q0, zero);
    s = MFMA32(k1, make_q1(qe, mneg, T & 3, hh), s);
    u32x4 p0, p1;
#pragma unroll
    for (int i = 0; i < 4; ++i) {
      p0[i] = pack2(EXP2(s[2 * i]), EXP2(s[2 * i + 1]));
      p1[i] = pack2(EXP2(s[8 + 2 * i]), EXP2(s[8 + 2 * i + 1]));
    }
    o = MFMA32(v0, __builtin_bit_cast(s16x8, p0), o);
    o = MFMA32(v1, __builtin_bit_cast(s16x8, p1), o);
  }
  return o;
}

typedef __amdgpu_buffer_rsrc_t rsrc_t;
DI s16x8 bload(rsrc_t r, unsigned voff, unsigned soff) {
  return __builtin_bit_cast(s16x8, __builtin_amdgcn_raw_buffer_load_b128(r, voff, soff, 0));
}
template <int NT>
DI void gemm2_load(rsrc_t r, unsigned hp, unsigned wp, unsigned wsp, unsigned lo16, int k, s16x8 (&hb)[2], s16x8 (&wb)[5]) {
  hb[0] = bload(r, lo16, hp + k * 1024);
  wb[0] = bload(r, lo16, wp + k * 18432);
  hb[1] = bload(r, lo16, hp + k * 1024 + 288 * 1024);
#pragma unroll
  for (int n = 1; n < 4; ++n) wb[n] = bload(r, lo16 + n * 1024, wp + k * 18432);
  if (NT == 5) wb[4] = bload(r, lo16, wsp + k * 18432);
}
template <int NT, int NTN>
DI void gemm2_step(rsrc_t r, unsigned hp, unsigned wp, unsigned wsp, unsigned lo16, int kn, s16x8 (&hb)[2], s16x8 (&wb)[5],
                   f32x16 (&acc)[2][5]) {
#pragma unroll
  for (int n = 0; n < NT; ++n) {
    acc[0][n] = MFMA32(wb[n], hb[0], acc[0][n]);
    acc[1][n] = MFMA32(wb[n], hb[1], acc[1][n]);
    SCHED();
    if (n < 4) wb[n] = bload(r, lo16 + n * 1024, wp + kn * 18432);
    else if (NTN == 5) wb[4] = bload(r, lo16, wsp + kn * 18432);
    SCHED();
  }
  if (NT == 4 && NTN == 5) wb[4] = bload(r, lo16, wsp + kn * 18432);
  hb[0] = bload(r, lo16, hp + kn * 1024);
  hb[1] = bload(r, lo16, hp + kn * 1024 + 288 * 1024);
  SCHED();
}
template <int N1, int N2, typename F, typename G>
DI void gemm2_all(rsrc_t r, unsigned hp, unsigned wp, unsigned wsp, unsigned lo16, f32x16 (&acc)[2][5], F&& after_first_loads, G&& after_first_step) {
  s16x8 hA[2], wA[5], hB[2], wB[5];
  after_first_loads();
  gemm2_load<N1>(r, hp, wp, wsp, lo16, 0, hA, wA);
  gemm2_load<N1>(r, hp, wp, wsp, lo16, 1, hB, wB);
  SCHED();
  after_first_step();
#pragma unroll 1
  for (int k = 0; k < 16; k += 2) {
    gemm2_step<N1, N1>(r, hp, wp, wsp, lo16, k + 2, hA, wA, acc);
    gemm2_step<N1, N1>(r, hp, wp, wsp, lo16, k + 3, hB, wB, acc);
  }
  gemm2_step<N1, N2>(r, hp, wp, wsp, lo16, 18, hA, wA, acc);
  gemm2_step<N1, N2>(r, hp, wp, wsp, lo16, 19, hB, wB, acc);
#pragma unroll 1
  for (int k = 18; k < 36; k += 2) {
    gemm2_step<N2, N2>(r, hp, wp, wsp, lo16, (k + 2 < 36) ? k + 2 : 35, hA, wA, acc);
    gemm2_step<N2, N2>(r, hp, wp, wsp, lo16, (k + 3 < 36) ? k + 3 : 35, hB, wB, acc);
  }
}

__global__ __launch_bounds__(512, 2)
void k2(char* __restrict__ ws) {
  extern __shared__ __attribute__((aligned(16))) char smem_[];
  LDSP char* smem = (LDSP char*)smem_;
  const int tid = threadIdx.x, lane = tid & 63;
  const int w = __builtin_amdgcn_readfirstlane(tid >> 6);
  const int b = blockIdx.x;
  const int t32 = lane & 31, hh = lane >> 5;
  if (tid < 32) *(LDSP unsigned*)(smem + K2_FLAG + tid * 4) = 0u;
  const char* kvg = ws + OFF_KV;

  if (w < 4) {
    const int tt = b >> 2, hg = b & 3;
    const int rs = w & 1, kh2 = w >> 1;
    const int head0 = hg * 8 + rs * 4;
#pragma unroll
    for (int i = 0; i < 5; ++i) {
      glds16(kvg + (i * 4 + w) * 1024, lane * 16, smem + (i * 4 + w) * 1024);
      glds16(kvg + KV_HALF + (i * 4 + w) * 1024, lane * 16, smem + KV_HALF + (i * 4 + w) * 1024);
    }
    u32x4 q0u[4];
    unsigned qe[4];
#pragma unroll
    for (int qb = 0; qb < 4; ++qb) {
      const char* qp = ws + OFF_Q + (size_t)tt * 36864 + (head0 + qb) * 1152;
      q0u[qb] = *(const u32x4*)(qp + lane * 16);
      qe[qb] = *(const unsigned*)(qp + 1024 + t32 * 4);
    }
    WAIT_LGKM0(); BAR();
    WAIT_VM(0);
    flag_set(smem, FL_KVA, w, lane);
    flag_wait_all(smem, FL_KVA);
    s16x8 q0[4], q1[4][4];
    f32x16 o[4];
    f32x16 sa, sb;
#pragma unroll
    for (int i = 0; i < 16; ++i) sb[i] = 0.f;
    {
      s16x8 k0 = *(LDSP s16x8*)(smem + 1024 + lane * 16);
      s16x8 k1 = *(LDSP s16x8*)(smem + lane * 16);
#pragma unroll
      for (int qb = 0; qb < 4; ++qb) {
        asm volatile("" : "+v"(q0u[qb]), "+v"(qe[qb]));
        q0[qb] = __builtin_bit_cast(s16x8, q0u[qb]);
        f32x16 s = MFMA32(k0, q0[qb], sb);
        s = MFMA32(k1, make_q1(qe[qb], 0u, 0, hh), s);
        float m = s[0];
#pragma unroll
        for (int i = 1; i < 16; ++i) m = __builtin_fmaxf(m, s[i]);
        m = __builtin_fmaxf(m, other_half(m));
        const unsigned mneg = neg_hi_lo(m);
#pragma unroll
        for (int i = 0; i < 4; ++i) q1[i][qb] = make_q1(qe[qb], mneg, i, hh);
#pragma unroll
        for (int i = 0; i < 16; ++i) o[qb][i] = 0.f;
      }
    }
    LDSP char* kp = smem + kh2 * KV_HALF + lane * 16;
    LDSP char* vp = smem + kh2 * KV_HALF + 2048 + hh * 304 + v_row(t32) * 16;
    u32x4 p0 = {0u, 0u, 0u, 0u}, p1 = {0u, 0u, 0u, 0u};
    s16x8 k1c = *(LDSP s16x8*)(kp), k1n;
    s16x8 k0b[2], v0b[2], v1b[2];
    k0b[0] = *(LDSP s16x8*)(kp + 1024);
    const unsigned long long vmask = 0x0007ffff0007ffffull;
#pragma unroll
    for (int i = 0; i < 2; ++i) { v0b[i] = s16x8{0, 0, 0, 0, 0, 0, 0, 0}; v1b[i] = s16x8{0, 0, 0, 0, 0, 0, 0, 0}; }
#define VLD(dst, addr, off) asm volatile("s_mov_b64 exec, %2\n\tds_read_b128 %0, %1 offset:%3\n\ts_mov_b64 exec, -1" \
                                         : "+v"(dst) : "v"(addr), "s"(vmask), "i"(off) : "memory")
#define VWAIT(dst) asm volatile("s_waitcnt lgkmcnt(0)" : "+v"(dst) :: "memory")
    VLD(v0b[0], vp, 0);
    sa = MFMA32(k0b[0], q0[0], sb);
    sa = MFMA32(k1c, q1[0][0], sa);
    asm volatile("s_nop 15" : "+v"(sa));
#pragma unroll 1
    for (int g = 0; g < 8; ++g) {
      if (g == 1) flag_wait_all(smem, FL_KVG);
      if (g == 5) flag_set(smem, FL_PROG, w, lane);
      const int nxg = (g < 7) ? KV_GRP : 0;
#pragma unroll
      for (int i = 0; i < 4; ++i) {
        const int c = i & 1, n = c ^ 1;
        const int nt_off = (i < 3) ? (i + 1) * KV_TILE : nxg;
        VWAIT(v0b[c]);
        VLD(v1b[c], vp, i * KV_TILE + 608);
        SCHED();
        attn_step(sa, sb, k0b[c], k1c, q0[1], q1[i][1], v1b[n], o[3], v0b[c], o[0], p0, p1);
        VWAIT(v1b[c]);
        k0b[n] = *(LDSP s16x8*)(kp + 1024 + nt_off);
        if (i == 3) k1n = *(LDSP s16x8*)(kp + nxg);
        SCHED();
        attn_step(sb, sa, k0b[c], k1c, q0[2], q1[i][2], v1b[c], o[0], v0b[c], o[1], p0, p1);
        if (i < 3) VLD(v0b[n], vp, (i + 1) * KV_TILE); else { LDSP char* vn = vp + nxg; VLD(v0b[n], vn, 0); }
        SCHED();
        attn_step(sa, sb, k0b[c], k1c, q0[3], q1[i][3], v1b[c], o[1], v0b[c], o[2], p0, p1);
        attn_step(sb, sa, k0b[n], (i == 3) ? k1n : k1c, q0[0], q1[(i + 1) & 3][0], v1b[c], o[2], v0b[c], o[3], p0, p1);
      }
      k1c = k1n;
      kp += KV_GRP; vp += KV_GRP;
    }
    p1[2] = pack2(sb[12], sb[13]);
    p1[3] = pack2(sb[14], sb[15]);
    o[3] = MFMA32(v1b[1], __builtin_bit_cast(s16x8, p1), o[3]);
    f32x16 keep[2], send[2];
    u32x4 qk0[2];
    unsigned qke[2];
#pragma unroll
    for (int q = 0; q < 2; ++q) {
#pragma unroll
      for (int i = 0; i < 11; ++i) {
        keep[q][i] = kh2 ? o[2 + q][i] : o[q][i];
        send[q][i] = kh2 ? o[q][i] : o[2 + q][i];
      }
#pragma unroll
      for (int i = 11; i < 16; ++i) { keep[q][i] = 0.f; send[q][i] = 0.f; }
#pragma unroll
      for (int i = 0; i < 4; ++i) qk0[q][i] = kh2 ? q0u[2 + q][i] : q0u[q][i];
      { unsigned a_ = qe[q], b_ = qe[2 + q]; asm volatile("" : "+v"(a_), "+v"(b_)); qke[q] = kh2 ? b_ : a_; }
    }
    WAIT_LGKM0();
    flag_set(smem, FL_DONE, w, lane);
    flag_wait_one(smem, FL_DONE, w ^ 1);
    {
      LDSP char* xp = smem + kh2 * KV_HALF + 7 * KV_GRP + rs * 4864;
#pragma unroll
      for (int q = 0; q < 2; ++q) {
        const f32x16 ov = send[q];
        *(LDSP f32x4*)(xp + q * 2432 + lane * 16) = f32x4{ov[0], ov[1], ov[2], ov[3]};
        *(LDSP f32x4*)(xp + q * 2432 + 1024 + lane * 16) = f32x4{ov[4], ov[5], ov[6], ov[7]};
        if (hh == 0) {
          *(LDSP float*)(xp + q * 2432 + 2048 + t32 * 4) = ov[8];
          *(LDSP float*)(xp + q * 2432 + 2176 + t32 * 4) = ov[9];
          *(LDSP float*)(xp + q * 2432 + 2304 + t32 * 4) = ov[10];
        }
      }
    }
    WAIT_LGKM0();
    flag_set(smem, FL_SENT, w, lane);
    flag_wait_one(smem, FL_SENT, w ^ 2);
    bool bad = false;
#pragma unroll
    for (int q = 0; q < 2; ++q) {
      LDSP char* xp = smem + (kh2 ^ 1) * KV_HALF + 7 * KV_GRP + rs * 4864 + q * 2432;
      f32x4 a0 = *(LDSP f32x4*)(xp + lane * 16), a1 = *(LDSP f32x4*)(xp + 1024 + lane * 16);
#pragma unroll
      for (int i = 0; i < 4; ++i) { keep[q][i] += a0[i]; keep[q][4 + i] += a1[i]; }
      keep[q][8] += *(LDSP float*)(xp + 2048 + t32 * 4);
      keep[q][9] += *(LDSP float*)(xp + 2176 + t32 * 4);
      keep[q][10] += *(LDSP float*)(xp + 2304 + t32 * 4);
      float mag = 0.f;
#pragma unroll
      for (int i = 0; i < 8; ++i) mag += __builtin_fabsf(keep[q][i]);
      const float ext = __builtin_fabsf(keep[q][8]) + __builtin_fabsf(keep[q][9]) + 1e12f * __builtin_fabsf(keep[q][10]);
      mag += hh ? 0.f : ext;
      bad = bad || !(mag < 1e30f);
    }
    if (__builtin_expect(__any(bad), 0)) {
#pragma unroll 1
      for (int q = 0; q < 2; ++q) {
        f32x16 ex = attn_exact(kvg, lane, q ? qk0[1] : qk0[0], q ? qke[1] : qke[0]);
        if (q) keep[1] = ex; else keep[0] = ex;
      }
    }
#pragma unroll
    for (int q = 0; q < 2; ++q) {
      float l = keep[q][10];
      const float lo_ = other_half(l);
      const float li = __builtin_amdgcn_rcpf(hh ? lo_ : l);
      const int head = head0 + 2 * kh2 + q;
      u32x4 o4 = {pack2(keep[q][0] * li, keep[q][1] * li), pack2(keep[q][2] * li, keep[q][3] * li),
                  pack2(keep[q][4] * li, keep[q][5] * li), pack2(keep[q][6] * li, keep[q][7] * li)};
      char* ap = ws + OFF_AO + (size_t)tt * 36 * 1024;
      store16(ap + head * 1024 + lane * 16, o4);
      if (hh == 0) {
        const int h8 = head & 7;
        *(unsigned*)(ap + (32 + (head >> 3)) * 1024 + (t32 + 32 * ((h8 >> 1) & 1)) * 16 + ((h8 >> 2) * 2 + (h8 & 1)) * 4) =
            pack2(keep[q][8] * li, keep[q][9] * li);
      }
    }
  } else {
    const int g = w - 4;
    const int xcd = b & 7, idx = b >> 3;
    const int kh = xcd & 1, tp = (xcd >> 1) * 8 + (idx & 7), ng = idx >> 3;
    const int nt0 = (ng >> 1) * 9 + (ng & 1) * 5;
    const int nts = (ng >> 1) * 9 + 4;
    const int fb0 = kh * 144 + g * 36;
    const rsrc_t rsrc = __builtin_amdgcn_make_buffer_rsrc(ws, 0, (unsigned)OFF_WOT, 0x00020000);
    const unsigned hp0 = (unsigned)OFF_H + ((2 * tp) * 288 + fb0) * 1024;
    const unsigned wp = (unsigned)OFF_W2T + (fb0 * 18 + nt0) * 1024;
    const unsigned wsp = (unsigned)OFF_W2T + (fb0 * 18 + nts) * 1024;
    const unsigned lo16 = lane * 16;
    BAR();
    f32x16 acc[2][5];
#pragma unroll
    for (int a = 0; a < 2; ++a)
#pragma unroll
      for (int n = 0; n < 5; ++n)
#pragma unroll
        for (int i = 0; i < 16; ++i) acc[a][n][i] = 0.f;
    auto stage_kv = [&]() {
      __builtin_amdgcn_s_setprio(3);
#pragma unroll 1
      for (int i = g; i < 58; i += 4) {
        glds16(kvg + (20 + i) * 1024, lo16, smem + (20 + i) * 1024);
        glds16(kvg + KV_HALF + (20 + i) * 1024, lo16, smem + KV_HALF + (20 + i) * 1024);
      }
      __builtin_amdgcn_s_setprio(0);
    };
    auto kv_ready = [&]() {
      WAIT_VM(12);
      flag_set(smem, FL_KVG, g, lane);
    };
    if (ng & 1) gemm2_all<4, 5>(rsrc, hp0, wp, wsp, lo16, acc, stage_kv, kv_ready);
    else        gemm2_all<5, 4>(rsrc, hp0, wp, wsp, lo16, acc, stage_kv, kv_ready);
    flag_wait_all(smem, FL_PROG);
    LDSP char* mine   = smem + (g >> 1) * KV_HALF + (g & 1) * 24576 + lo16;
    LDSP char* theirs = smem + (g >> 1) * KV_HALF + ((g & 1) ^ 1) * 24576 + lo16;
    LDSP char* other  = smem + ((g >> 1) ^ 1) * KV_HALF + ((g & 1) ^ 1) * 24576 + lo16;
    auto reduce = [&](auto aa, auto hh_) {
      constexpr int A = decltype(aa)::value, HF = decltype(hh_)::value;
#pragma unroll
      for (int n = 0; n < 5; ++n)
#pragma unroll
        for (int q = 0; q < 4; ++q)
          *(LDSP f32x4*)(mine + (n * 4 + q) * 1024) =
              f32x4{acc[A ^ 1][n][4 * q], acc[A ^ 1][n][4 * q + 1], acc[A ^ 1][n][4 * q + 2], acc[A ^ 1][n][4 * q + 3]};
      WAIT_LGKM0();
      flag_set(smem, FL_GW, g, lane);
      flag_wait_one(smem, FL_GW, g ^ 1);
#pragma unroll
      for (int n = 0; n < 5; ++n)
#pragma unroll
        for (int q = 0; q < 4; ++q) {
          f32x4 v = *(LDSP f32x4*)(theirs + (n * 4 + q) * 1024);
#pragma unroll
          for (int i = 0; i < 4; ++i) acc[A][n][4 * q + i] += v[i];
        }
      WAIT_LGKM0();
#pragma unroll
      for (int n = 0; n < 5; ++n)
#pragma unroll
        for (int q = 0; q < 2; ++q) {
          constexpr int r0 = 8 * (HF ^ 1);
          *(LDSP f32x4*)(theirs + (n * 2 + q) * 1024) =
              f32x4{acc[A][n][r0 + 4 * q], acc[A][n][r0 + 4 * q + 1], acc[A][n][r0 + 4 * q + 2], acc[A][n][r0 + 4 * q + 3]};
        }
      WAIT_LGKM0();
      flag_set(smem, FL_GW2, g, lane);
      flag_wait_one(smem, FL_GW2, g ^ 2);
#pragma unroll
      for (int n = 0; n < 5; ++n) {
        const int nt = (n < 4) ? nt0 + n : nts;
        const int slot = (n < 4) ? kh : kh + 2 * (ng & 1);
        char* pp = ws + OFF_PART + (((size_t)slot * 64 + (2 * tp + A)) * 18 + nt) * 2048 + HF * 1024 + lo16;
        u32x4 o4;
#pragma unroll
        for (int q = 0; q < 2; ++q) {
          f32x4 v = *(LDSP f32x4*)(other + (n * 2 + q) * 1024);
#pragma unroll
          for (int i = 0; i < 4; ++i) v[i] += acc[A][n][8 * HF + 4 * q + i];
          o4[2 * q] = pack2(v[0], v[1]);
          o4[2 * q + 1] = pack2(v[2], v[3]);
        }
        store16(pp, o4);
      }
    };
    using I0 = std::integral_constant<int, 0>;
    using I1 = std::integral_constant<int, 1>;
    if (g == 0) reduce(I0{}, I0{});
    else if (g == 1) reduce(I1{}, I0{});
    else if (g == 2) reduce(I0{}, I1{});
    else reduce(I1{}, I1{});
  }
}

constexpr int K3_LDS = 6 * 6 * 4096;
__global__ __launch_bounds__(384)
void k3(const u16* __restrict__ x, const u16* __restrict__ b2, char* __restrict__ ws, u16* __restrict__ out) {
  extern __shared__ __attribute__((aligned(16))) char smem_[];
  LDSP char* smem = (LDSP char*)smem_;
  const int tid = threadIdx.x, lane = tid & 63;
  const int w = __builtin_amdgcn_readfirstlane(tid >> 6);
  const int b = blockIdx.x;
  const int tp = (b & 7) * 4 + (b >> 3) / 6, ng = (b >> 3) % 6;
  const int t32 = lane & 31, hh = lane >> 5;
  const int tt = 2 * tp + w / 3, nt = 3 * ng + w % 3;
  const int token = tt * 32 + t32;
  const char* ap = ws + OFF_AO + ((size_t)(2 * tp) * 36 + w * 6) * 1024 + lane * 16;
  const char* wp = ws + OFF_WOT + ((size_t)(w * 6) * 18 + 3 * ng) * 1024 + lane * 16;
  const bool shared = (nt == 4 || nt == 13);
  const size_t SL = (size_t)64 * 18 * 2048;
  const char* pp = ws + OFF_PART + ((size_t)tt * 18 + nt) * 2048 + lane * 16;
  const char* pp2 = shared ? pp + 2 * SL : pp;
  s16x8 af[2][6], wf[3][6];
  u32x4 pt[4][2];
  u32x2 xv[4], bv[4];
#pragma unroll
  for (int h = 0; h < 2; ++h) {
    pt[0][h] = *(const u32x4*)(pp + h * 1024);
    pt[1][h] = *(const u32x4*)(pp + SL + h * 1024);
    pt[2][h] = *(const u32x4*)(pp2 + h * 1024);
    pt[3][h] = *(const u32x4*)(pp2 + SL + h * 1024);
  }
#pragma unroll
  for (int q = 0; q < 4; ++q) {
    const int col = nt * 32 + 8 * q + 4 * hh;
    xv[q] = *(const u32x2*)((const char*)x + (size_t)token * 1152 + col * 2);
    bv[q] = *(const u32x2*)((const char*)b2 + col * 2);
  }
  SCHED();
#pragma unroll
  for (int k = 0; k < 6; ++k) {
    af[0][k] = *(const s16x8*)(ap + k * 1024);
    wf[0][k] = *(const s16x8*)(wp + k * 18432);
    af[1][k] = *(const s16x8*)(ap + 36 * 1024 + k * 1024);
    wf[1][k] = *(const s16x8*)(wp + k * 18432 + 1024);
    wf[2][k] = *(const s16x8*)(wp + k * 18432 + 2048);
  }
  SCHED();
  f32x16 acc[2][3];
#pragma unroll
  for (int i = 0; i < 2; ++i)
#pragma unroll
    for (int jn = 0; jn < 3; ++jn)
#pragma unroll
      for (int r = 0; r < 16; ++r) acc[i][jn][r] = 0.f;
#pragma unroll
  for (int k = 0; k < 6; ++k)
#pragma unroll
    for (int i = 0; i < 2; ++i)
#pragma unroll
      for (int jn = 0; jn < 3; ++jn) acc[i][jn] = MFMA32(wf[jn][k], af[i][k], acc[i][jn]);
#pragma unroll
  for (int i = 0; i < 2; ++i)
#pragma unroll
    for (int jn = 0; jn < 3; ++jn)
#pragma unroll
      for (int q = 0; q < 4; ++q) {
        f32x4 v = {acc[i][jn][4 * q], acc[i][jn][4 * q + 1], acc[i][jn][4 * q + 2], acc[i][jn][4 * q + 3]};
        *(LDSP f32x4*)(smem + ((w * 6 + i * 3 + jn) * 4 + q) * 1024 + lane * 16) = v;
      }
  const float sh = shared ? 1.f : 0.f;
  f32x4 fin[4];
#pragma unroll
  for (int q = 0; q < 4; ++q) {
#pragma unroll
    for (int e = 0; e < 2; ++e) {
      const unsigned p0 = pt[0][q >> 1][(q & 1) * 2 + e], p1 = pt[1][q >> 1][(q & 1) * 2 + e];
      const unsigned p2 = pt[2][q >> 1][(q & 1) * 2 + e], p3 = pt[3][q >> 1][(q & 1) * 2 + e];
      const float lo = (__builtin_bit_cast(float, p0 << 16) + __builtin_bit_cast(float, p1 << 16)) +
                       sh * (__builtin_bit_cast(float, p2 << 16) + __builtin_bit_cast(float, p3 << 16));
      const float hi = (__builtin_bit_cast(float, p0 & 0xffff0000u) + __builtin_bit_cast(float, p1 & 0xffff0000u)) +
                       sh * (__builtin_bit_cast(float, p2 & 0xffff0000u) + __builtin_bit_cast(float, p3 & 0xffff0000u));
      fin[q][2 * e]     = lo + __builtin_bit_cast(float, xv[q][e] << 16) + __builtin_bit_cast(float, bv[q][e] << 16);
      fin[q][2 * e + 1] = hi + __builtin_bit_cast(float, xv[q][e] & 0xffff0000u) + __builtin_bit_cast(float, bv[q][e] & 0xffff0000u);
    }
  }
  __syncthreads();
#pragma unroll
  for (int q = 0; q < 4; ++q) {
    f32x4 v = fin[q];
#pragma unroll
    for (int o = 0; o < 6; ++o) v += *(LDSP f32x4*)(smem + ((o * 6 + w) * 4 + q) * 1024 + lane * 16);
    u32x2 ov = {pack2(v[0], v[1]), pack2(v[2], v[3])};
    *(u32x2*)((char*)out + (size_t)token * 1152 + (nt * 32 + 8 * q + 4 * hh) * 2) = ov;
  }
}

extern "C" void kernel_launch(void* const* d_in, const int* in_sizes, int n_in,
                              void* d_out, int out_size, void* d_ws, size_t ws_size,
                              hipStream_t stream) {
  const u16* x  = (const u16*)d_in[0];
  const u16* wq = (const u16*)d_in[1];
  const u16* wk = (const u16*)d_in[2];
  const u16* wv = (const u16*)d_in[3];
  const u16* wo = (const u16*)d_in[4];
  const u16* w1 = (const u16*)d_in[6];
  const u16* b1 = (const u16*)d_in[7];
  const u16* w2 = (const u16*)d_in[8];
  const u16* b2 = (const u16*)d_in[9];
  char* ws = (char*)d_ws;
  (void)hipFuncSetAttribute((const void*)k1, hipFuncAttributeMaxDynamicSharedMemorySize, K1_LDS);
  (void)hipFuncSetAttribute((const void*)k2, hipFuncAttributeMaxDynamicSharedMemorySize, K2_LDS);
  (void)hipFuncSetAttribute((const void*)k3, hipFuncAttributeMaxDynamicSharedMemorySize, K3_LDS);
  k1<<<256, 512, K1_LDS, stream>>>(x, wq, wk, wv, wo, w1, b1, w2, ws);
  k2<<<256, 512, K2_LDS, stream>>>(ws);
  k3<<<192, 384, K3_LDS, stream>>>(x, b2, ws, (u16*)d_out);
}
```

```cpp
#include <hip/hip_runtime.h>
#include <hip/hip_bf16.h>
#include <stdint.h>
#include <type_traits>

#define DI __device__ __forceinline__
#define LDSP __attribute__((address_space(3)))
#define GLP __attribute__((address_space(1)))

typedef unsigned short u16;
typedef __attribute__((ext_vector_type(8))) short s16x8;
typedef __attribute__((ext_vector_type(4))) short s16x4;
typedef __attribute__((ext_vector_type(16))) float f32x16;
typedef __attribute__((ext_vector_type(4))) float f32x4;
typedef __attribute__((ext_vector_type(2))) float f32x2;
typedef __attribute__((ext_vector_type(2))) __bf16 bf16x2;
typedef __attribute__((ext_vector_type(4))) unsigned u32x4;
typedef __attribute__((ext_vector_type(2))) unsigned u32x2;

constexpr size_t OFF_H    = 0;
constexpr size_t OFF_W2T  = OFF_H + 18874368;
constexpr size_t OFF_WOT  = OFF_W2T + 5308416;
constexpr size_t OFF_Q    = OFF_WOT + 663552;
constexpr size_t OFF_KV   = OFF_Q + 2359296;
constexpr size_t OFF_AO   = OFF_KV + 159744;
constexpr size_t OFF_PART = OFF_AO + 2359296;
constexpr int KV_GRP = 9984, KV_TILE = 2240, KV_BYTES = 159744, KV_HALF = 79872;

#define MFMA32(a, b, c) __builtin_amdgcn_mfma_f32_32x32x16_bf16((a), (b), (c), 0, 0, 0)
#define WAIT_VM(n) asm volatile("s_waitcnt vmcnt(" #n ")" ::: "memory")
#define WAIT_LGKM0() asm volatile("s_waitcnt lgkmcnt(0)" ::: "memory")
#define BAR() __builtin_amdgcn_s_barrier()

DI unsigned pack2(float a, float b) {
  f32x2 v = {a, b};
  return __builtin_bit_cast(unsigned, __builtin_convertvector(v, bf16x2));
}
DI void glds16(const void* gbase, unsigned voff, LDSP char* l) {
  unsigned keep;
  asm volatile("s_mov_b32 %0, m0\n\ts_mov_b32 m0, %3\n\ts_nop 0\n\tglobal_load_lds_dwordx4 %1, %2\n\ts_mov_b32 m0, %0"
               : "=&s"(keep) : "v"(voff), "s"(gbase), "s"((unsigned)(size_t)l) : "memory");
}
DI void glds4(const void* gbase, unsigned voff, LDSP char* l) {
  unsigned keep;
  asm volatile("s_mov_b32 %0, m0\n\ts_mov_b32 m0, %3\n\ts_nop 0\n\tglobal_load_lds_dword %1, %2\n\ts_mov_b32 m0, %0"
               : "=&s"(keep) : "v"(voff), "s"(gbase), "s"((unsigned)(size_t)l) : "memory");
}
DI void store16(void* p, u32x4 v) {
  asm volatile("global_store_dwordx4 %0, %1, off sc1\n\ts_nop 1" :: "v"(p), "v"(v) : "memory");
}
DI void code_warm(int i, int lane, LDSP char* pad) {
  unsigned long long pc;
  asm volatile("s_getpc_b64 %0" : "=s"(pc));
  glds16((const char*)(pc & ~255ull) + i * 1024, lane * 16, pad);
}
DI s16x8 tr_frag(LDSP char* p, int second_off) {
  s16x4 lo = __builtin_amdgcn_ds_read_tr16_b64_v4i16((LDSP s16x4*)p);
  s16x4 hi = __builtin_amdgcn_ds_read_tr16_b64_v4i16((LDSP s16x4*)(p + second_off));
  return __builtin_shufflevector(lo, hi, 0, 1, 2, 3, 4, 5, 6, 7);
}
DI float other_half(float v) {
  unsigned u = __builtin_bit_cast(unsigned, v);
  auto r = __builtin_amdgcn_permlane32_swap(u, u, false, false);
  unsigned o = (threadIdx.x & 32) ? r[0] : r[1];
  return __builtin_bit_cast(float, o);
}

constexpr int K1_SLOT = 36864;
constexpr int K1_BIAS = 4 * K1_SLOT;
constexpr int K1_CS   = K1_BIAS + 1152;
constexpr int K1_CSQ  = K1_CS + 1152;
constexpr int K1_STAT = K1_CSQ + 1024;
constexpr int K1_LDS  = K1_STAT + 2048;

__host__ __device__ constexpr int w_slot(int st) { return (0x231023120ll >> (4 * st)) & 3; }

#define SCHED() __builtin_amdgcn_sched_barrier(0)
#define SGB(mask, n) __builtin_amdgcn_sched_group_barrier((mask), (n), 0)

DI u32x2 gelu4(float a0, float a1, float a2, float a3, const f32x4& cs, const f32x4& bb, float negm, float r, float c0) {
  float z0, z1, z2, z3, t0, t1, t2, t3;
  unsigned o0, o1;
  asm volatile(
      "v_fma_f32 %2, %18, %14, %10\n\tv_fma_f32 %3, %18, %15, %11\n\tv_fma_f32 %4, %18, %16, %12\n\tv_fma_f32 %5, %18, %17, %13\n\t"
      "v_fma_f32 %2, %19, %2, %21\n\tv_fma_f32 %3, %19, %3, %22\n\tv_fma_f32 %4, %19, %4, %23\n\tv_fma_f32 %5, %19, %5, %24\n\t"
      "v_mul_f32 %6, %2, %2\n\tv_mul_f32 %7, %3, %3\n\tv_mul_f32 %8, %4, %4\n\tv_mul_f32 %9, %5, %5\n\t"
      "v_fmamk_f32 %6, %6, 0xbdd2d3e8, %20\n\tv_fmamk_f32 %7, %7, 0xbdd2d3e8, %20\n\tv_fmamk_f32 %8, %8, 0xbdd2d3e8, %20\n\tv_fmamk_f32 %9, %9, 0xbdd2d3e8, %20\n\t"
      "v_mul_f32 %6, %2, %6\n\tv_mul_f32 %7, %3, %7\n\tv_mul_f32 %8, %4, %8\n\tv_mul_f32 %9, %5, %9\n\t"
      "v_exp_f32 %6, %6\n\tv_exp_f32 %7, %7\n\tv_exp_f32 %8, %8\n\tv_exp_f32 %9, %9\n\t"
      "v_add_f32 %6, 1.0, %6\n\tv_add_f32 %7, 1.0, %7\n\tv_add_f32 %8, 1.0, %8\n\tv_add_f32 %9, 1.0, %9\n\t"
      "v_rcp_f32 %6, %6\n\tv_rcp_f32 %7, %7\n\tv_rcp_f32 %8, %8\n\tv_rcp_f32 %9, %9\n\t"
      "v_mul_f32 %2, %2, %6\n\tv_mul_f32 %3, %3, %7\n\tv_mul_f32 %4, %4, %8\n\tv_mul_f32 %5, %5, %9\n\t"
      "v_cvt_pk_bf16_f32 %0, %2, %3\n\tv_cvt_pk_bf16_f32 %1, %4, %5"
      : "=&v"(o0), "=&v"(o1), "=&v"(z0), "=&v"(z1), "=&v"(z2), "=&v"(z3), "=&v"(t0), "=&v"(t1), "=&v"(t2), "=&v"(t3)
      : "v"(a0), "v"(a1), "v"(a2), "v"(a3), "v"(cs[0]), "v"(cs[1]), "v"(cs[2]), "v"(cs[3]), "v"(negm), "v"(r), "v"(c0),
        "v"(bb[0]), "v"(bb[1]), "v"(bb[2]), "v"(bb[3]));
  u32x2 o = {o0, o1};
  return o;
}

template <typename F>
DI void gelu_tile(const f32x16& a, LDSP char* cs_p, LDSP char* bias_p, char* out_p, float negm, float r, F&& between) {
  const float c0 = -2.302208198f;
#pragma unroll
  for (int hf = 0; hf < 2; ++hf) {
    f32x4 cs0 = *(LDSP f32x4*)(cs_p + hf * 64), cs1 = *(LDSP f32x4*)(cs_p + hf * 64 + 32);
    f32x4 bb0 = *(LDSP f32x4*)(bias_p + hf * 64), bb1 = *(LDSP f32x4*)(bias_p + hf * 64 + 32);
    between(2 * hf);
    u32x2 lo = gelu4(a[8 * hf], a[8 * hf + 1], a[8 * hf + 2], a[8 * hf + 3], cs0, bb0, negm, r, c0);
    between(2 * hf + 1);
    u32x2 hi = gelu4(a[8 * hf + 4], a[8 * hf + 5], a[8 * hf + 6], a[8 * hf + 7], cs1, bb1, negm, r, c0);
    u32x4 o = {lo[0], lo[1], hi[0], hi[1]};
    store16(out_p + hf * 1024, o);
  }
}

DI void colsum_frag(const s16x8& a, const s16x8& sel, f32x4& cs) {
  cs = __builtin_amdgcn_mfma_f32_16x16x32_bf16(sel, a, cs, 0, 0, 0);
}

template <int KT>
DI void w1_stage(LDSP char* sb0, LDSP char* sb1, LDSP char* sb2, const s16x8 (&xf)[36], f32x16 (&acc)[3], const s16x8& sel, f32x4& cs) {
  asm volatile("" : "+v"(sb0), "+v"(sb1), "+v"(sb2));
  s16x8 a[2][3];
  a[0][0] = tr_frag(sb0, 768); a[0][1] = tr_frag(sb1, 768); a[0][2] = tr_frag(sb2, 768);
#pragma unroll
  for (int ks = 0; ks < 12; ++ks) {
    const int cur = ks & 1;
    if (ks + 1 < 12) {
      a[cur ^ 1][0] = tr_frag(sb0 + (ks + 1) * 3072, 768);
      a[cur ^ 1][1] = tr_frag(sb1 + (ks + 1) * 3072, 768);
      a[cur ^ 1][2] = tr_frag(sb2 + (ks + 1) * 3072, 768);
    }
#pragma unroll
    for (int t = 0; t < 3; ++t) acc[t] = MFMA32(a[cur][t], xf[12 * KT + ks], acc[t]);
    colsum_frag(a[cur][0], sel, cs);
    SGB(0x100, 2); SGB(0x8, 1); SGB(0x100, 2); SGB(0x8, 1); SGB(0x100, 2); SGB(0x8, 2);
    SCHED();
  }
}

DI void qkv_stage(LDSP char* sb, const s16x8 (&xf)[36], f32x16& qa, const s16x8& sel, f32x4& cs) {
  asm volatile("" : "+v"(sb));
  s16x8 a[3];
  a[0] = tr_frag(sb, 256);
  a[1] = tr_frag(sb + 1024, 256);
#pragma unroll
  for (int s = 0; s < 36; ++s) {
    if (s + 2 < 36) a[(s + 2) % 3] = tr_frag(sb + (s + 2) * 1024, 256);
    qa = MFMA32(a[s % 3], xf[s], qa);
    colsum_frag(a[s % 3], sel, cs);
    SCHED();
  }
}

__global__ __launch_bounds__(512, 2)
void k1(const u16* __restrict__ x, const u16* __restrict__ wq, const u16* __restrict__ wk, const u16* __restrict__ wv,
        const u16* __restrict__ wo, const u16* __restrict__ w1, const u16* __restrict__ b1, const u16* __restrict__ w2,
        char* __restrict__ ws) {
  extern __shared__ __attribute__((aligned(16))) char smem_[];
  LDSP char* smem = (LDSP char*)smem_;
  const int tid = threadIdx.x, lane = tid & 63;
  const int w8 = __builtin_amdgcn_readfirstlane(tid >> 6);
  const int w = w8 & 3, grp = w8 >> 2;
  const int b = blockIdx.x, xcd = b & 7, idx = b >> 3;
  const int tg = (xcd >> 1) * 4 + (idx & 3), j = (xcd & 1) * 8 + (idx >> 2);
  const int t32 = lane & 31, hh = lane >> 5;
  const int tt = tg * 4 + w;
  if (w8 >= 6 && idx * 2 + (w8 - 6) < 63) code_warm(idx * 2 + (w8 - 6), lane, smem + K1_STAT + 1024);
  const bool heavy = (j & 3) == 0;
  constexpr int rk = 0, rc = 0;

  const char* w1w = (const char*)w1 + (size_t)(48 * w) * 9216 + (size_t)j * 576;
  unsigned off3[3];
#pragma unroll
  for (int i = 0; i < 3; ++i) {
    const unsigned bb = 1024 * i + 16 * lane;
    off3[i] = (bb / 192) * 9216 + (bb % 192);
  }
  auto issue_w1_rows = [&](int st, int r) {
    const int c = st / 3, kt = st - 3 * c;
    const char* g = w1w + ((c + rc) % 3) * 192 + (size_t)((kt + rk) % 3) * 192 * 9216 + r * 16 * 9216;
    LDSP char* l = smem + w_slot(st) * K1_SLOT + w * 9216 + r * 3072;
#pragma unroll
    for (int i = 0; i < 3; ++i) glds16(g, off3[i], l + i * 1024);
  };
  auto issue_w1 = [&](int st) {
#pragma unroll
    for (int r = 0; r < 3; ++r) issue_w1_rows(st, r);
  };
  auto issue_x = [&](int q, int slot) {
    int ln = tid & 63; asm volatile("" : "+v"(ln));
    const char* g = (const char*)x + (size_t)(tt * 32) * 1152;
    LDSP char* l = smem + slot * K1_SLOT + w * 9216;
    unsigned row = (unsigned)ln / 18, pos = (unsigned)ln % 18;
    const unsigned b0 = q * 288 + rk * 384;
#pragma unroll
    for (int i = 0; i < 9; ++i) {
      unsigned cb = b0 + ((pos ^ ((row >> 3) & 1)) << 4);
      cb = cb >= 1152 ? cb - 1152 : cb;
      glds16(g, row * 1152 + cb, l + i * 1024);
      pos += 10; row += 3;
      if (pos >= 18) { pos -= 18; row += 1; }
    }
  };
  auto issue_qkv = [&](int tile, int slot, int part) {
    int ln = tid & 63; asm volatile("" : "+v"(ln));
    LDSP char* l = smem + slot * K1_SLOT + w * 9216 + part * 3072;
    const int row0 = 144 * w + 48 * part + 192 * rk;
    if (tile < 18) {
      const char* g = (const char*)wq + tile * 64;
      const unsigned vo = (ln >> 2) * 1152 + (ln & 3) * 16;
#pragma unroll
      for (int i = 0; i < 3; ++i) glds16(g + (size_t)((row0 + 16 * i) % 576) * 1152, vo, l + i * 1024);
    } else {
      const u16* src = (tile == 18) ? wk : wv;
      int dw = ln & 15; dw = dw > 8 ? 8 : dw;
      const unsigned vo = (ln >> 4) * 36 + dw * 4;
#pragma unroll
      for (int i = 0; i < 12; ++i) glds4((const char*)src + ((row0 + 4 * i) % 576) * 36, vo, l + i * 256);
    }
  };
  const int lw = (tg * 12 + (j - (j >> 2) - 1)) * 4 + w;
  auto issue_unit = [&](int part) {
    if (lw < 648) {
      int ln = tid & 63; asm volatile("" : "+v"(ln));
      LDSP char* l = smem + 0 * K1_SLOT + w * 9216 + part * 3072;
      const int u = lw - 576, ks = u >> 1;
      const bool plain = ks < 32;
      const char* g = lw < 576 ? (const char*)w2 + (size_t)((lw >> 1) * 16) * 1152 + (lw & 1) * 576
                               : (const char*)wo + (size_t)(plain ? ks * 18 : 144 * (ks - 32) + 16) * 1152 + (u & 1) * 576;
      const unsigned p0 = 192 * part + ln;
      unsigned row = p0 / 36, pos = p0 % 36;
#pragma unroll
      for (int i = 0; i < 3; ++i) {
        const unsigned srow = plain ? row : 18 * (row >> 1) + (row & 1);
        glds16(g, srow * 1152 + pos * 16, l + i * 1024);
        pos += 28; row += 1;
        if (pos >= 36) { pos -= 36; row += 1; }
      }
    }
  };

  unsigned bias_raw = 0, bias_raw2 = 0;
  if (grp == 1) {
    asm volatile("global_load_ushort %0, %2, %4\n\tglobal_load_ushort %1, %3, %4" : "=&v"(bias_raw), "=&v"(bias_raw2)
                 : "v"((unsigned)((tid - 256) * 2)), "v"((unsigned)((tid < 288 ? tid : 287) * 2)), "s"(b1 + j * 288) : "memory");
  }
  if (grp == 0) { issue_x(0, 3); issue_x(1, 1); }
  else          { issue_w1(0);   issue_w1(1); }
  s16x8 xf[36];
  const int xl = w * 9216 + t32 * 288 + ((hh ^ ((t32 >> 3) & 1)) << 4);
  auto read_x = [&](int q, int slot) {
#pragma unroll
    for (int s = 0; s < 9; ++s) xf[9 * q + s] = *(LDSP s16x8*)(smem + slot * K1_SLOT + xl + s * 32);
  };
  if (grp == 0) WAIT_VM(9);
  BAR();
  read_x(0, 3);
  if (grp == 0) { WAIT_VM(0); }
  else {
    asm volatile("s_waitcnt vmcnt(9)" : "+v"(bias_raw), "+v"(bias_raw2) :: "memory");
    *(LDSP float*)(smem + K1_BIAS + (tid - 256) * 4) = __builtin_bit_cast(float, bias_raw << 16);
    if (tid < 288) *(LDSP float*)(smem + K1_BIAS + tid * 4) = __builtin_bit_cast(float, bias_raw2 << 16);
  }
  WAIT_LGKM0(); BAR();
  read_x(1, 1);
  WAIT_LGKM0(); BAR();

  const int q4 = (lane & 15) >> 2, p4 = lane & 3, g16 = (lane >> 4) & 1;
  const int rot = (w == 3) ? 0 : w;
  const int tl0 = rot, tl1 = (rot + 1) % 3, tl2 = (rot + 2) % 3;
  const int lo_w1 = (8 * hh + q4) * 192 + 32 * g16 + 8 * p4;
  const int lo_q  = (8 * hh + q4) * 64 + 32 * g16 + 8 * p4;
  char* hout = ws + OFF_H + ((size_t)tt * 288 + (size_t)j * 18) * 1024 + lane * 16;
  f32x16 acc[3];
  float negm = 0.f, r1 = 0.f, rm = 0.f;
  const unsigned selv = ((lane & 15) == ((lane >> 4) & 1)) ? 0x3f803f80u : 0u;
  const u32x4 selu = {selv, selv, selv, selv};
  const s16x8 sel = __builtin_bit_cast(s16x8, selu);

  auto load_stats = [&]() {
    f32x2 st = *(LDSP f32x2*)(smem + K1_STAT + (w * 32 + t32) * 8);
    negm = -st[0] * (1.0f / 576.0f);
    const float var = __builtin_fmaxf(st[1] * (1.0f / 576.0f) - negm * negm, 0.f);
    auto rb = [](float v) { return __builtin_bit_cast(float, pack2(v, 0.f) << 16); };
    const float s1 = rb(__builtin_sqrtf(rb(rb(var) + 1e-5f)));
    r1 = 1.0f / s1;
    const float s2 = rb(__builtin_sqrtf(rb(rb(var * r1 * r1) + 1e-5f)));
    rm = r1 / s2;
  };
  auto mfma_chunk = [&](auto cc) {
    constexpr int c = decltype(cc)::value;
    __builtin_amdgcn_s_setprio(0);
    f32x4 cs = {0.f, 0.f, 0.f, 0.f};
#pragma unroll
    for (int t = 0; t < 3; ++t)
#pragma unroll
      for (int i = 0; i < 16; ++i) acc[t][i] = 0.f;
    LDSP char* s0 = smem + lo_w1 + tl0 * 64;
    LDSP char* s1 = smem + lo_w1 + tl1 * 64;
    LDSP char* s2 = smem + lo_w1 + tl2 * 64;
    constexpr int o0 = w_slot(3 * c) * K1_SLOT, o1 = w_slot(3 * c + 1) * K1_SLOT, o2 = w_slot(3 * c + 2) * K1_SLOT;
    w1_stage<0>(s0 + o0, s1 + o0, s2 + o0, xf, acc, sel, cs);
    if (c == 1) { WAIT_VM(9); } else if (c == 2) { WAIT_VM(11); }
    WAIT_LGKM0(); BAR();
    if (c == 0) { read_x(2, 3); WAIT_LGKM0(); BAR(); }
    w1_stage<1>(s0 + o1, s1 + o1, s2 + o1, xf, acc, sel, cs);
    if (c != 0) WAIT_VM(0);
    WAIT_LGKM0(); BAR();
    if (c == 0) { read_x(3, 0); WAIT_LGKM0(); BAR(); }
    w1_stage<2>(s0 + o2, s1 + o2, s2 + o2, xf, acc, sel, cs);
    if (lane < 16 && w < 3) {
      *(LDSP float*)(smem + K1_CS + (c * 96 + tl0 * 32 + lane) * 4) = cs[0];
      *(LDSP float*)(smem + K1_CS + (c * 96 + tl0 * 32 + 16 + lane) * 4) = cs[1];
    }
    WAIT_LGKM0(); BAR();
    __builtin_amdgcn_s_setprio(1);
  };
  auto gelu_t = [&](int c, int t, int tl, int st_issue) {
    const int ca = (c + rc) % 3;
    gelu_tile(acc[t], smem + K1_CS + (c * 96 + tl * 32) * 4 + hh * 16, smem + K1_BIAS + (ca * 96 + tl * 32) * 4 + hh * 16,
              hout + (ca * 6 + tl * 2) * 1024, negm, rm, [&](int i) {
      if (i < 3) {
        if (st_issue >= 0 && st_issue < 9) issue_w1_rows(st_issue, i);
        else if (st_issue == 9) issue_qkv(j, 1, i);
        else if (st_issue == 10) { if (heavy) issue_qkv(16 + (j >> 2), 0, i); else issue_unit(i); }
      }
    });
  };

  if (grp == 0) {
    mfma_chunk(std::integral_constant<int, 0>{});
    load_stats();
    gelu_t(0, 0, tl0, 6); WAIT_LGKM0(); BAR();
    gelu_t(0, 1, tl1, 7); WAIT_LGKM0(); BAR();
    gelu_t(0, 2, tl2, 8); WAIT_VM(22); WAIT_LGKM0(); BAR();
    mfma_chunk(std::integral_constant<int, 2>{});
    gelu_t(2, 0, tl0, -1); gelu_t(2, 1, tl1, -1); gelu_t(2, 2, tl2, -1);
  } else {
    f32x4 rs = {0.f, 0.f, 0.f, 0.f};
#pragma unroll
    for (int i = 0; i < 16; ++i) acc[0][i] = 0.f;
    auto stats_frag = [&](int s) {
      colsum_frag(xf[s], sel, rs);
      acc[0] = MFMA32(xf[s], xf[s], acc[0]);
    };
    __builtin_amdgcn_s_setprio(1);
    issue_x(2, 3);
#pragma unroll
    for (int s = 0; s < 18; ++s) { if (s % 6 == 0) issue_w1_rows(2, s / 6); stats_frag(s); }
    WAIT_VM(9); BAR();
    read_x(2, 3); WAIT_LGKM0(); BAR();
    issue_x(3, 0);
#pragma unroll
    for (int s = 18; s < 27; ++s) { if (s % 3 == 0) issue_w1_rows(3, (s - 18) / 3); stats_frag(s); }
    WAIT_VM(9); BAR();
    read_x(3, 0); WAIT_LGKM0(); BAR();
    issue_w1(4);
#pragma unroll
    for (int s = 27; s < 36; ++s) { if (s % 3 == 0) issue_w1_rows(5, (s - 27) / 3); stats_frag(s); }
    {
      const int dreg = (t32 & 3) + 4 * (t32 >> 3);
      float s2 = 0.f;
#pragma unroll
      for (int i = 0; i < 16; ++i) s2 = (i == dreg) ? acc[0][i] : s2;
      if (hh == ((t32 >> 2) & 1)) *(LDSP float*)(smem + K1_STAT + (w * 32 + t32) * 8 + 4) = s2;
      if (lane < 16) {
        *(LDSP float*)(smem + K1_STAT + (w * 32 + lane) * 8) = rs[0];
        *(LDSP float*)(smem + K1_STAT + (w * 32 + 16 + lane) * 8) = rs[1];
      }
    }
    WAIT_VM(18); WAIT_LGKM0(); BAR();
    mfma_chunk(std::integral_constant<int, 1>{});
    load_stats();
    gelu_t(1, 0, tl0, 10); WAIT_LGKM0(); BAR();
    gelu_t(1, 1, tl1, 9); WAIT_LGKM0(); BAR();
    gelu_t(1, 2, tl2, -1);
    WAIT_VM(2); WAIT_LGKM0(); BAR();
    __builtin_amdgcn_s_setprio(2);

    auto qkv_tile = [&](int tile, int slot) {
      f32x16 qa;
#pragma unroll
      for (int i = 0; i < 16; ++i) qa[i] = 0.f;
      f32x4 cs = {0.f, 0.f, 0.f, 0.f};
      qkv_stage(smem + slot * K1_SLOT + lo_q, xf, qa, sel, cs);
      int ln = tid & 63; asm volatile("" : "+v"(ln));
      const int t32 = ln & 31, hh = ln >> 5, token = tt * 32 + t32;
      LDSP char* cp = smem + K1_CSQ + (w * 2 + (slot & 1)) * 128;
      if (ln < 16) { *(LDSP float*)(cp + ln * 4) = cs[0]; *(LDSP float*)(cp + 64 + ln * 4) = cs[1]; }
      WAIT_LGKM0();
#pragma unroll
      for (int g = 0; g < 4; ++g) {
        f32x4 c4 = *(LDSP f32x4*)(cp + g * 32 + hh * 16);
#pragma unroll
        for (int i = 0; i < 4; ++i) qa[4 * g + i] = __builtin_fmaf(negm, c4[i], qa[4 * g + i]);
      }
      if (tile < 18) {
        const float sc = r1 * (0.23570226039551584f * 1.4426950408889634f);
        char* qp = ws + OFF_Q + (size_t)tt * 36864;
#pragma unroll
        for (int i = 0; i < 8; ++i) {
          const unsigned n = tile * 32 + 8 * (i >> 1) + 4 * hh + 2 * (i & 1);
          const unsigned head = n / 18, d = n - head * 18;
          const unsigned off = d < 16 ? (t32 + 32 * ((d >> 2) & 1)) * 16 + ((d >> 3) * 2 + ((d >> 1) & 1)) * 4 : 1024 + t32 * 4;
          *(unsigned*)(qp + head * 1152 + off) = pack2(qa[2 * i] * sc, qa[2 * i + 1] * sc);
        }
      } else if (tile == 18) {
        u32x4 o = {pack2(qa[0] * r1, qa[1] * r1), pack2(qa[2] * r1, qa[3] * r1),
                   pack2(qa[4] * r1, qa[5] * r1), pack2(qa[6] * r1, qa[7] * r1)};
        char* gp = ws + OFF_KV + (size_t)tg * KV_GRP;
        *(u32x4*)(gp + 1024 + w * KV_TILE + ln * 16) = o;
        if (hh == 0) *(unsigned*)(gp + (w >> 1) * 512 + t32 * 16 + (w & 1) * 4) = pack2(qa[8] * r1, qa[9] * r1);
        else if ((w & 1) == 0) {
          u32x2 cst = {w == 0 ? 0x3f803f80u : 0u, 0u};
          *(u32x2*)(gp + (w >> 1) * 512 + t32 * 16 + 8) = cst;
        }
      } else {
        const int s = t32 >> 4, hk = (t32 >> 2) & 1, jj = ((t32 >> 3) & 1) * 4 + (t32 & 3);
        char* vp = ws + OFF_KV + (size_t)tg * KV_GRP + 2048 + w * KV_TILE + (s * 2 + hk) * 304 + jj * 2;
#pragma unroll
        for (int i = 0; i < 8; ++i) {
          const int d = (i & 3) + 8 * (i >> 2) + 4 * hh;
          *(u16*)(vp + d * 16) = (u16)(pack2(qa[i] * r1, 0.f) & 0xffffu);
        }
        if (hh == 0) {
          *(u16*)(vp + 16 * 16) = (u16)(pack2(qa[8] * r1, 0.f) & 0xffffu);
          *(u16*)(vp + 17 * 16) = (u16)(pack2(qa[9] * r1, 0.f) & 0xffffu);
          *(u16*)(vp + 18 * 16) = (u16)0x3f80;
        }
      }
    };
    qkv_tile(j, 1);
    if (heavy) {
      qkv_tile(16 + (j >> 2), 0);
    } else if (lw < 648) {
      int ln = tid & 63; asm volatile("" : "+v"(ln));
      const int hh = ln >> 5, q4 = (ln & 15) >> 2, p4 = ln & 3, g16 = (ln >> 4) & 1;
      LDSP char* sb = smem + 0 * K1_SLOT + w * 9216 + (4 * hh + q4) * 576 + 32 * g16 + 8 * p4;
      char* dst = (lw < 576) ? ws + OFF_W2T + ((size_t)(lw >> 1) * 18 + (lw & 1) * 9) * 1024
                             : ws + OFF_WOT + ((size_t)((lw - 576) >> 1) * 18 + (lw & 1) * 9) * 1024;
#pragma unroll
      for (int n = 0; n < 9; ++n) {
        s16x8 f = tr_frag(sb + n * 64, 8 * 576);
        store16(dst + n * 1024 + ln * 16, __builtin_bit_cast(u32x4, f));
      }
    }
  }
}

constexpr int K2_FLAG = KV_BYTES;
constexpr int K2_LDS  = KV_BYTES + 128 + 1024;
enum { FL_KVA = 0, FL_KVG = 1, FL_PROG = 2, FL_GW = 3, FL_DONE = 4, FL_SENT = 5, FL_GW2 = 6 };

#define EXP2(x) __builtin_amdgcn_exp2f(x)

DI void flag_set(LDSP char* smem, int set, int i, int lane) {
  if (lane == 0) *(volatile LDSP unsigned*)(smem + K2_FLAG + set * 16 + i * 4) = 1u;
}
DI void flag_wait_all(LDSP char* smem, int set) {
  while (true) {
    u32x4 f = *(volatile LDSP u32x4*)(smem + K2_FLAG + set * 16);
    if (f[0] & f[1] & f[2] & f[3]) break;
    __builtin_amdgcn_s_sleep(2);
  }
  asm volatile("" ::: "memory");
}
DI void flag_wait_one(LDSP char* smem, int set, int i) {
  while (*(volatile LDSP unsigned*)(smem + K2_FLAG + set * 16 + i * 4) == 0u) __builtin_amdgcn_s_sleep(1);
  asm volatile("" ::: "memory");
}

DI void attn_step(f32x16& scur, f32x16& snext, const s16x8& k0, const s16x8& k1, const s16x8& qn0, const s16x8& qn1,
                  const s16x8& vprev1, f32x16& oprev, const s16x8& vcur0, f32x16& ocur, u32x4& p0, u32x4& p1) {
  f32x16 zero;
#pragma unroll
  for (int i = 0; i < 16; ++i) zero[i] = 0.f;
#define EX(i) do { float t_ = scur[i]; asm volatile("v_exp_f32 %0, %0" : "+v"(t_)); scur[i] = t_; } while (0)
  p1[2] = pack2(snext[12], snext[13]);
  p1[3] = pack2(snext[14], snext[15]);
  SCHED();
  snext = MFMA32(k0, qn0, zero);
  SCHED();
#pragma unroll
  for (int i = 0; i < 3; ++i) EX(i);
  SCHED();
  snext = MFMA32(k1, qn1, snext);
  SCHED();
#pragma unroll
  for (int i = 3; i < 8; ++i) EX(i);
  SCHED();
  p0[0] = pack2(scur[0], scur[1]);
  p0[1] = pack2(scur[2], scur[3]);
  p0[2] = pack2(scur[4], scur[5]);
  p0[3] = pack2(scur[6], scur[7]);
  SCHED();
  oprev = MFMA32(vprev1, __builtin_bit_cast(s16x8, p1), oprev);
  SCHED();
#pragma unroll
  for (int i = 8; i < 8 + 3; ++i) EX(i);
  SCHED();
  ocur = MFMA32(vcur0, __builtin_bit_cast(s16x8, p0), ocur);
  SCHED();
#pragma unroll
  for (int i = 8 + 3; i < 16; ++i) EX(i);
  SCHED();
  p1[0] = pack2(scur[8], scur[9]);
  p1[1] = pack2(scur[10], scur[11]);
  SCHED();
#undef EX
}

DI s16x8 make_q1(unsigned qe, unsigned mneg, int i, int hh) {
  const unsigned a = (hh == (i >> 1)) ? qe : 0u;
  u32x4 r = {(i & 1) ? 0u : a, (i & 1) ? a : 0u, hh ? 0u : mneg, 0u};
  return __builtin_bit_cast(s16x8, r);
}
DI unsigned neg_hi_lo(float m) {
  unsigned hi = pack2(-m, 0.f) & 0xffffu;
  float rem = -m - __builtin_bit_cast(float, hi << 16);
  unsigned lo = pack2(rem, 0.f) & 0xffffu;
  return hi | (lo << 16);
}
DI int v_row(int t32) { return t32 > 18 ? ((t32 >= 20 && t32 < 28) ? 0 : 4) : t32; }

DI f32x16 attn_exact(const char* kv, int lane, u32x4 q0u, unsigned qe) {
  const int t32 = lane & 31, hh = lane >> 5;
  const s16x8 q0 = __builtin_bit_cast(s16x8, q0u);
  f32x16 zero, o;
#pragma unroll
  for (int i = 0; i < 16; ++i) { zero[i] = 0.f; o[i] = 0.f; }
  float m = -INFINITY;
#pragma unroll 1
  for (int T = 0; T < 64; ++T) {
    const char* gp = kv + (T >> 2) * KV_GRP;
    s16x8 k0 = *(const s16x8*)(gp + 1024 + (T & 3) * KV_TILE + lane * 16);
    s16x8 k1 = *(const s16x8*)(gp + lane * 16);
    f32x16 s = MFMA32(k0, q0, zero);
    s = MFMA32(k1, make_q1(qe, 0u, T & 3, hh), s);
#pragma unroll
    for (int i = 0; i < 16; ++i) m = __builtin_fmaxf(m, s[i]);
  }
  m = __builtin_fmaxf(m, other_half(m));
  const unsigned mneg = neg_hi_lo(m);
#pragma unroll 1
  for (int T = 0; T < 64; ++T) {
    const char* gp = kv + (T >> 2) * KV_GRP;
    const char* tp = gp + 1024 + (T & 3) * KV_TILE;
    s16x8 k0 = *(const s16x8*)(tp + lane * 16);
    s16x8 k1 = *(const s16x8*)(gp + lane * 16);
    s16x8 v0 = *(const s16x8*)(tp + 1024 + hh * 304 + v_row(t32) * 16);
    s16x8 v1 = *(const s16x8*)(tp + 1024 + 608 + hh * 304 + v_row(t32) * 16);
    f32x16 s = MFMA32(k0, q0, zero);
    s = MFMA32(k1, make_q1(qe, mneg, T & 3, hh), s);
    u32x4 p0, p1;
#pragma unroll
    for (int i = 0; i < 4; ++i) {
      p0[i] = pack2(EXP2(s[2 * i]), EXP2(s[2 * i + 1]));
      p1[i] = pack2(EXP2(s[8 + 2 * i]), EXP2(s[8 + 2 * i + 1]));
    }
    o = MFMA32(v0, __builtin_bit_cast(s16x8, p0), o);
    o = MFMA32(v1, __builtin_bit_cast(s16x8, p1), o);
  }
  return o;
}

typedef __amdgpu_buffer_rsrc_t rsrc_t;
DI s16x8 bload(rsrc_t r, unsigned voff, unsigned soff) {
  return __builtin_bit_cast(s16x8, __builtin_amdgcn_raw_buffer_load_b128(r, voff, soff, 0));
}
template <int NT>
DI void gemm2_load(rsrc_t r, unsigned hp, unsigned wp, unsigned wsp, unsigned lo16, int k, s16x8 (&hb)[2], s16x8 (&wb)[5]) {
  hb[0] = bload(r, lo16, hp + k * 1024);
  wb[0] = bload(r, lo16, wp + k * 18432);
  hb[1] = bload(r, lo16, hp + k * 1024 + 288 * 1024);
#pragma unroll
  for (int n = 1; n < 4; ++n) wb[n] = bload(r, lo16 + n * 1024, wp + k * 18432);
  if (NT == 5) wb[4] = bload(r, lo16, wsp + k * 18432);
}
template <int NT, int NTN>
DI void gemm2_step(rsrc_t r, unsigned hp, unsigned wp, unsigned wsp, unsigned lo16, int kn, s16x8 (&hb)[2], s16x8 (&wb)[5],
                   f32x16 (&acc)[2][5]) {
#pragma unroll
  for (int n = 0; n < NT; ++n) {
    acc[0][n] = MFMA32(wb[n], hb[0], acc[0][n]);
    acc[1][n] = MFMA32(wb[n], hb[1], acc[1][n]);
    SCHED();
    if (n < 4) wb[n] = bload(r, lo16 + n * 1024, wp + kn * 18432);
    else if (NTN == 5) wb[4] = bload(r, lo16, wsp + kn * 18432);
    SCHED();
  }
  if (NT == 4 && NTN == 5) wb[4] = bload(r, lo16, wsp + kn * 18432);
  hb[0] = bload(r, lo16, hp + kn * 1024);
  hb[1] = bload(r, lo16, hp + kn * 1024 + 288 * 1024);
  SCHED();
}
template <int N1, int N2, typename F, typename G>
DI void gemm2_all(rsrc_t r, unsigned hp, unsigned wp, unsigned wsp, unsigned lo16, f32x16 (&acc)[2][5], F&& after_first_loads, G&& after_first_step) {
  s16x8 hA[2], wA[5], hB[2], wB[5];
  after_first_loads();
  gemm2_load<N1>(r, hp, wp, wsp, lo16, 0, hA, wA);
  gemm2_load<N1>(r, hp, wp, wsp, lo16, 1, hB, wB);
  SCHED();
  after_first_step();
#pragma unroll 1
  for (int k = 0; k < 16; k += 2) {
    gemm2_step<N1, N1>(r, hp, wp, wsp, lo16, k + 2, hA, wA, acc);
    gemm2_step<N1, N1>(r, hp, wp, wsp, lo16, k + 3, hB, wB, acc);
  }
  gemm2_step<N1, N2>(r, hp, wp, wsp, lo16, 18, hA, wA, acc);
  gemm2_step<N1, N2>(r, hp, wp, wsp, lo16, 19, hB, wB, acc);
#pragma unroll 1
  for (int k = 18; k < 36; k += 2) {
    gemm2_step<N2, N2>(r, hp, wp, wsp, lo16, (k + 2 < 36) ? k + 2 : 35, hA, wA, acc);
    gemm2_step<N2, N2>(r, hp, wp, wsp, lo16, (k + 3 < 36) ? k + 3 : 35, hB, wB, acc);
  }
}

__global__ __launch_bounds__(512, 2)
void k2(char* __restrict__ ws) {
  extern __shared__ __attribute__((aligned(16))) char smem_[];
  LDSP char* smem = (LDSP char*)smem_;
  const int tid = threadIdx.x, lane = tid & 63;
  const int w = __builtin_amdgcn_readfirstlane(tid >> 6);
  const int b = blockIdx.x;
  const int t32 = lane & 31, hh = lane >> 5;
  if (w == 7 && (b >> 3) < 22) code_warm(b >> 3, lane, smem + KV_BYTES + 128);
  if (tid < 32) *(LDSP unsigned*)(smem + K2_FLAG + tid * 4) = 0u;
  const char* kvg = ws + OFF_KV;

  if (w < 4) {
    const int tt = (b & 7) * 8 + (b >> 5), hg = (b >> 3) & 3;
    const int rs = w & 1, kh2 = w >> 1;
    const int head0 = hg * 8 + rs * 4;
#pragma unroll
    for (int i = 0; i < 5; ++i) {
      glds16(kvg + (i * 4 + w) * 1024, lane * 16, smem + (i * 4 + w) * 1024);
      glds16(kvg + KV_HALF + (i * 4 + w) * 1024, lane * 16, smem + KV_HALF + (i * 4 + w) * 1024);
    }
    u32x4 q0u[4];
    unsigned qe[4];
#pragma unroll
    for (int qb = 0; qb < 4; ++qb) {
      const char* qp = ws + OFF_Q + (size_t)tt * 36864 + (head0 + qb) * 1152;
      q0u[qb] = *(const u32x4*)(qp + lane * 16);
      qe[qb] = *(const unsigned*)(qp + 1024 + t32 * 4);
    }
    WAIT_LGKM0(); BAR();
    WAIT_VM(0);
    flag_set(smem, FL_KVA, w, lane);
    flag_wait_all(smem, FL_KVA);
    s16x8 q0[4], q1[4][4];
    f32x16 o[4];
    f32x16 sa, sb;
#pragma unroll
    for (int i = 0; i < 16; ++i) sb[i] = 0.f;
    {
      s16x8 k0 = *(LDSP s16x8*)(smem + 1024 + lane * 16);
      s16x8 k1 = *(LDSP s16x8*)(smem + lane * 16);
#pragma unroll
      for (int qb = 0; qb < 4; ++qb) {
        asm volatile("" : "+v"(q0u[qb]), "+v"(qe[qb]));
        q0[qb] = __builtin_bit_cast(s16x8, q0u[qb]);
        f32x16 s = MFMA32(k0, q0[qb], sb);
        s = MFMA32(k1, make_q1(qe[qb], 0u, 0, hh), s);
        float m = s[0];
#pragma unroll
        for (int i = 1; i < 16; ++i) m = __builtin_fmaxf(m, s[i]);
        m = __builtin_fmaxf(m, other_half(m));
        const unsigned mneg = neg_hi_lo(m);
#pragma unroll
        for (int i = 0; i < 4; ++i) q1[i][qb] = make_q1(qe[qb], mneg, i, hh);
#pragma unroll
        for (int i = 0; i < 16; ++i) o[qb][i] = 0.f;
      }
    }
    LDSP char* kp = smem + kh2 * KV_HALF + lane * 16;
    LDSP char* vp = smem + kh2 * KV_HALF + 2048 + hh * 304 + v_row(t32) * 16;
    u32x4 p0 = {0u, 0u, 0u, 0u}, p1 = {0u, 0u, 0u, 0u};
    s16x8 k1c = *(LDSP s16x8*)(kp), k1n;
    s16x8 k0b[2], v0b[2], v1b[2];
    k0b[0] = *(LDSP s16x8*)(kp + 1024);
    const unsigned long long vmask = 0x0007ffff0007ffffull;
#pragma unroll
    for (int i = 0; i < 2; ++i) { v0b[i] = s16x8{0, 0, 0, 0, 0, 0, 0, 0}; v1b[i] = s16x8{0, 0, 0, 0, 0, 0, 0, 0}; }
#define VLD(dst, addr, off) asm volatile("s_mov_b64 exec, %2\n\tds_read_b128 %0, %1 offset:%3\n\ts_mov_b64 exec, -1" \
                                         : "+v"(dst) : "v"(addr), "s"(vmask), "i"(off) : "memory")
#define VWAIT(dst) asm volatile("s_waitcnt lgkmcnt(0)" : "+v"(dst) :: "memory")
    VLD(v0b[0], vp, 0);
    sa = MFMA32(k0b[0], q0[0], sb);
    sa = MFMA32(k1c, q1[0][0], sa);
    asm volatile("s_nop 15" : "+v"(sa));
#pragma unroll 1
    for (int g = 0; g < 8; ++g) {
      if (g == 1) flag_wait_all(smem, FL_KVG);
      if (g == 5) flag_set(smem, FL_PROG, w, lane);
      const int nxg = (g < 7) ? KV_GRP : 0;
#pragma unroll
      for (int i = 0; i < 4; ++i) {
        const int c = i & 1, n = c ^ 1;
        const int nt_off = (i < 3) ? (i + 1) * KV_TILE : nxg;
        VWAIT(v0b[c]);
        VLD(v1b[c], vp, i * KV_TILE + 608);
        SCHED();
        attn_step(sa, sb, k0b[c], k1c, q0[1], q1[i][1], v1b[n], o[3], v0b[c], o[0], p0, p1);
        VWAIT(v1b[c]);
        k0b[n] = *(LDSP s16x8*)(kp + 1024 + nt_off);
        if (i == 3) k1n = *(LDSP s16x8*)(kp + nxg);
        SCHED();
        attn_step(sb, sa, k0b[c], k1c, q0[2], q1[i][2], v1b[c], o[0], v0b[c], o[1], p0, p1);
        if (i < 3) VLD(v0b[n], vp, (i + 1) * KV_TILE); else { LDSP char* vn = vp + nxg; VLD(v0b[n], vn, 0); }
        SCHED();
        attn_step(sa, sb, k0b[c], k1c, q0[3], q1[i][3], v1b[c], o[1], v0b[c], o[2], p0, p1);
        attn_step(sb, sa, k0b[n], (i == 3) ? k1n : k1c, q0[0], q1[(i + 1) & 3][0], v1b[c], o[2], v0b[c], o[3], p0, p1);
      }
      k1c = k1n;
      kp += KV_GRP; vp += KV_GRP;
    }
    p1[2] = pack2(sb[12], sb[13]);
    p1[3] = pack2(sb[14], sb[15]);
    o[3] = MFMA32(v1b[1], __builtin_bit_cast(s16x8, p1), o[3]);
    f32x16 keep[2], send[2];
    u32x4 qk0[2];
    unsigned qke[2];
#pragma unroll
    for (int q = 0; q < 2; ++q) {
#pragma unroll
      for (int i = 0; i < 11; ++i) {
        keep[q][i] = kh2 ? o[2 + q][i] : o[q][i];
        send[q][i] = kh2 ? o[q][i] : o[2 + q][i];
      }
#pragma unroll
      for (int i = 11; i < 16; ++i) { keep[q][i] = 0.f; send[q][i] = 0.f; }
#pragma unroll
      for (int i = 0; i < 4; ++i) qk0[q][i] = kh2 ? q0u[2 + q][i] : q0u[q][i];
      { unsigned a_ = qe[q], b_ = qe[2 + q]; asm volatile("" : "+v"(a_), "+v"(b_)); qke[q] = kh2 ? b_ : a_; }
    }
    WAIT_LGKM0();
    flag_set(smem, FL_DONE, w, lane);
    flag_wait_one(smem, FL_DONE, w ^ 1);
    {
      LDSP char* xp = smem + kh2 * KV_HALF + 7 * KV_GRP + rs * 4864;
#pragma unroll
      for (int q = 0; q < 2; ++q) {
        const f32x16 ov = send[q];
        *(LDSP f32x4*)(xp + q * 2432 + lane * 16) = f32x4{ov[0], ov[1], ov[2], ov[3]};
        *(LDSP f32x4*)(xp + q * 2432 + 1024 + lane * 16) = f32x4{ov[4], ov[5], ov[6], ov[7]};
        if (hh == 0) {
          *(LDSP float*)(xp + q * 2432 + 2048 + t32 * 4) = ov[8];
          *(LDSP float*)(xp + q * 2432 + 2176 + t32 * 4) = ov[9];
          *(LDSP float*)(xp + q * 2432 + 2304 + t32 * 4) = ov[10];
        }
      }
    }
    WAIT_LGKM0();
    flag_set(smem, FL_SENT, w, lane);
    flag_wait_one(smem, FL_SENT, w ^ 2);
    bool bad = false;
#pragma unroll
    for (int q = 0; q < 2; ++q) {
      LDSP char* xp = smem + (kh2 ^ 1) * KV_HALF + 7 * KV_GRP + rs * 4864 + q * 2432;
      f32x4 a0 = *(LDSP f32x4*)(xp + lane * 16), a1 = *(LDSP f32x4*)(xp + 1024 + lane * 16);
#pragma unroll
      for (int i = 0; i < 4; ++i) { keep[q][i] += a0[i]; keep[q][4 + i] += a1[i]; }
      keep[q][8] += *(LDSP float*)(xp + 2048 + t32 * 4);
      keep[q][9] += *(LDSP float*)(xp + 2176 + t32 * 4);
      keep[q][10] += *(LDSP float*)(xp + 2304 + t32 * 4);
      float mag = 0.f;
#pragma unroll
      for (int i = 0; i < 8; ++i) mag += __builtin_fabsf(keep[q][i]);
      const float ext = __builtin_fabsf(keep[q][8]) + __builtin_fabsf(keep[q][9]) + 1e12f * __builtin_fabsf(keep[q][10]);
      mag += hh ? 0.f : ext;
      bad = bad || !(mag < 1e30f);
    }
    if (__builtin_expect(__any(bad), 0)) {
#pragma unroll 1
      for (int q = 0; q < 2; ++q) {
        f32x16 ex = attn_exact(kvg, lane, q ? qk0[1] : qk0[0], q ? qke[1] : qke[0]);
        if (q) keep[1] = ex; else keep[0] = ex;
      }
    }
#pragma unroll
    for (int q = 0; q < 2; ++q) {
      float l = keep[q][10];
      const float lo_ = other_half(l);
      const float li = __builtin_amdgcn_rcpf(hh ? lo_ : l);
      const int head = head0 + 2 * kh2 + q;
      u32x4 o4 = {pack2(keep[q][0] * li, keep[q][1] * li), pack2(keep[q][2] * li, keep[q][3] * li),
                  pack2(keep[q][4] * li, keep[q][5] * li), pack2(keep[q][6] * li, keep[q][7] * li)};
      char* ap = ws + OFF_AO + (size_t)tt * 36 * 1024;
      store16(ap + head * 1024 + lane * 16, o4);
      if (hh == 0) {
        const int h8 = head & 7;
        *(unsigned*)(ap + (32 + (head >> 3)) * 1024 + (t32 + 32 * ((h8 >> 1) & 1)) * 16 + ((h8 >> 2) * 2 + (h8 & 1)) * 4) =
            pack2(keep[q][8] * li, keep[q][9] * li);
      }
    }
  } else {
    const int g = w - 4;
    const int xcd = b & 7, idx = b >> 3;
    const int kh = xcd & 1, tp = (xcd >> 1) * 8 + (idx & 7), ng = idx >> 3;
    const int nt0 = (ng >> 1) * 9 + (ng & 1) * 5;
    const int nts = (ng >> 1) * 9 + 4;
    const int fb0 = kh * 144 + g * 36;
    const rsrc_t rsrc = __builtin_amdgcn_make_buffer_rsrc(ws, 0, (unsigned)OFF_WOT, 0x00020000);
    const unsigned hp0 = (unsigned)OFF_H + ((2 * tp) * 288 + fb0) * 1024;
    const unsigned wp = (unsigned)OFF_W2T + (fb0 * 18 + nt0) * 1024;
    const unsigned wsp = (unsigned)OFF_W2T + (fb0 * 18 + nts) * 1024;
    const unsigned lo16 = lane * 16;
    BAR();
    f32x16 acc[2][5];
#pragma unroll
    for (int a = 0; a < 2; ++a)
#pragma unroll
      for (int n = 0; n < 5; ++n)
#pragma unroll
        for (int i = 0; i < 16; ++i) acc[a][n][i] = 0.f;
    auto stage_kv = [&]() {
      __builtin_amdgcn_s_setprio(3);
#pragma unroll 1
      for (int i = g; i < 58; i += 4) {
        glds16(kvg + (20 + i) * 1024, lo16, smem + (20 + i) * 1024);
        glds16(kvg + KV_HALF + (20 + i) * 1024, lo16, smem + KV_HALF + (20 + i) * 1024);
      }
      __builtin_amdgcn_s_setprio(0);
    };
    auto kv_ready = [&]() {
      WAIT_VM(12);
      flag_set(smem, FL_KVG, g, lane);
    };
    if (ng & 1) gemm2_all<4, 5>(rsrc, hp0, wp, wsp, lo16, acc, stage_kv, kv_ready);
    else        gemm2_all<5, 4>(rsrc, hp0, wp, wsp, lo16, acc, stage_kv, kv_ready);
    flag_wait_all(smem, FL_PROG);
    LDSP char* mine   = smem + (g >> 1) * KV_HALF + (g & 1) * 24576 + lo16;
    LDSP char* theirs = smem + (g >> 1) * KV_HALF + ((g & 1) ^ 1) * 24576 + lo16;
    LDSP char* other  = smem + ((g >> 1) ^ 1) * KV_HALF + ((g & 1) ^ 1) * 24576 + lo16;
    auto reduce = [&](auto aa, auto hh_) {
      constexpr int A = decltype(aa)::value, HF = decltype(hh_)::value;
#pragma unroll
      for (int n = 0; n < 5; ++n)
#pragma unroll
        for (int q = 0; q < 4; ++q)
          *(LDSP f32x4*)(mine + (n * 4 + q) * 1024) =
              f32x4{acc[A ^ 1][n][4 * q], acc[A ^ 1][n][4 * q + 1], acc[A ^ 1][n][4 * q + 2], acc[A ^ 1][n][4 * q + 3]};
      WAIT_LGKM0();
      flag_set(smem, FL_GW, g, lane);
      flag_wait_one(smem, FL_GW, g ^ 1);
#pragma unroll
      for (int n = 0; n < 5; ++n)
#pragma unroll
        for (int q = 0; q < 4; ++q) {
          f32x4 v = *(LDSP f32x4*)(theirs + (n * 4 + q) * 1024);
#pragma unroll
          for (int i = 0; i < 4; ++i) acc[A][n][4 * q + i] += v[i];
        }
      WAIT_LGKM0();
#pragma unroll
      for (int n = 0; n < 5; ++n)
#pragma unroll
        for (int q = 0; q < 2; ++q) {
          constexpr int r0 = 8 * (HF ^ 1);
          *(LDSP f32x4*)(theirs + (n * 2 + q) * 1024) =
              f32x4{acc[A][n][r0 + 4 * q], acc[A][n][r0 + 4 * q + 1], acc[A][n][r0 + 4 * q + 2], acc[A][n][r0 + 4 * q + 3]};
        }
      WAIT_LGKM0();
      flag_set(smem, FL_GW2, g, lane);
      flag_wait_one(smem, FL_GW2, g ^ 2);
#pragma unroll
      for (int n = 0; n < 5; ++n) {
        const int nt = (n < 4) ? nt0 + n : nts;
        const int slot = (n < 4) ? kh : kh + 2 * (ng & 1);
        char* pp = ws + OFF_PART + (((size_t)slot * 64 + (2 * tp + A)) * 18 + nt) * 2048 + HF * 1024 + lo16;
        u32x4 o4;
#pragma unroll
        for (int q = 0; q < 2; ++q) {
          f32x4 v = *(LDSP f32x4*)(other + (n * 2 + q) * 1024);
#pragma unroll
          for (int i = 0; i < 4; ++i) v[i] += acc[A][n][8 * HF + 4 * q + i];
          o4[2 * q] = pack2(v[0], v[1]);
          o4[2 * q + 1] = pack2(v[2], v[3]);
        }
        store16(pp, o4);
      }
    };
    using I0 = std::integral_constant<int, 0>;
    using I1 = std::integral_constant<int, 1>;
    if (g == 0) reduce(I0{}, I0{});
    else if (g == 1) reduce(I1{}, I0{});
    else if (g == 2) reduce(I0{}, I1{});
    else reduce(I1{}, I1{});
  }
}

constexpr int K3_LDS = 6 * 6 * 4096;
__global__ __launch_bounds__(384)
void k3(const u16* __restrict__ x, const u16* __restrict__ b2, char* __restrict__ ws, u16* __restrict__ out) {
  extern __shared__ __attribute__((aligned(16))) char smem_[];
  LDSP char* smem = (LDSP char*)smem_;
  const int tid = threadIdx.x, lane = tid & 63;
  const int w = __builtin_amdgcn_readfirstlane(tid >> 6);
  const int b = blockIdx.x;
  const int tp = (b & 7) * 4 + (b >> 3) / 6, ng = (b >> 3) % 6;
  const int t32 = lane & 31, hh = lane >> 5;
  const int tt = 2 * tp + w / 3, nt = 3 * ng + w % 3;
  const int token = tt * 32 + t32;
  const char* ap = ws + OFF_AO + ((size_t)(2 * tp) * 36 + w * 6) * 1024 + lane * 16;
  const char* wp = ws + OFF_WOT + ((size_t)(w * 6) * 18 + 3 * ng) * 1024 + lane * 16;
  const bool shared = (nt == 4 || nt == 13);
  const size_t SL = (size_t)64 * 18 * 2048;
  const char* pp = ws + OFF_PART + ((size_t)tt * 18 + nt) * 2048 + lane * 16;
  const char* pp2 = shared ? pp + 2 * SL : pp;
  s16x8 af[2][6], wf[3][6];
  u32x4 pt[4][2];
  u32x2 xv[4], bv[4];
#pragma unroll
  for (int k = 0; k < 6; ++k) {
    af[0][k] = *(const s16x8*)(ap + k * 1024);
    wf[0][k] = *(const s16x8*)(wp + k * 18432);
    af[1][k] = *(const s16x8*)(ap + 36 * 1024 + k * 1024);
    wf[1][k] = *(const s16x8*)(wp + k * 18432 + 1024);
    wf[2][k] = *(const s16x8*)(wp + k * 18432 + 2048);
  }
#pragma unroll
  for (int h = 0; h < 2; ++h) {
    pt[0][h] = *(const u32x4*)(pp + h * 1024);
    pt[1][h] = *(const u32x4*)(pp + SL + h * 1024);
    pt[2][h] = *(const u32x4*)(pp2 + h * 1024);
    pt[3][h] = *(const u32x4*)(pp2 + SL + h * 1024);
  }
#pragma unroll
  for (int q = 0; q < 4; ++q) {
    const int col = nt * 32 + 8 * q + 4 * hh;
    xv[q] = *(const u32x2*)((const char*)x + (size_t)token * 1152 + col * 2);
    bv[q] = *(const u32x2*)((const char*)b2 + col * 2);
  }
  SCHED();
  f32x16 acc[2][3];
#pragma unroll
  for (int i = 0; i < 2; ++i)
#pragma unroll
    for (int jn = 0; jn < 3; ++jn)
#pragma unroll
      for (int r = 0; r < 16; ++r) acc[i][jn][r] = 0.f;
#pragma unroll
  for (int k = 0; k < 6; ++k)
#pragma unroll
    for (int i = 0; i < 2; ++i)
#pragma unroll
      for (int jn = 0; jn < 3; ++jn) acc[i][jn] = MFMA32(wf[jn][k], af[i][k], acc[i][jn]);
#pragma unroll
  for (int i = 0; i < 2; ++i)
#pragma unroll
    for (int jn = 0; jn < 3; ++jn)
#pragma unroll
      for (int q = 0; q < 4; ++q) {
        f32x4 v = {acc[i][jn][4 * q], acc[i][jn][4 * q + 1], acc[i][jn][4 * q + 2], acc[i][jn][4 * q + 3]};
        *(LDSP f32x4*)(smem + ((w * 6 + i * 3 + jn) * 4 + q) * 1024 + lane * 16) = v;
      }
  const float sh = shared ? 1.f : 0.f;
  f32x4 fin[4];
#pragma unroll
  for (int q = 0; q < 4; ++q) {
#pragma unroll
    for (int e = 0; e < 2; ++e) {
      const unsigned p0 = pt[0][q >> 1][(q & 1) * 2 + e], p1 = pt[1][q >> 1][(q & 1) * 2 + e];
      const unsigned p2 = pt[2][q >> 1][(q & 1) * 2 + e], p3 = pt[3][q >> 1][(q & 1) * 2 + e];
      const float lo = (__builtin_bit_cast(float, p0 << 16) + __builtin_bit_cast(float, p1 << 16)) +
                       sh * (__builtin_bit_cast(float, p2 << 16) + __builtin_bit_cast(float, p3 << 16));
      const float hi = (__builtin_bit_cast(float, p0 & 0xffff0000u) + __builtin_bit_cast(float, p1 & 0xffff0000u)) +
                       sh * (__builtin_bit_cast(float, p2 & 0xffff0000u) + __builtin_bit_cast(float, p3 & 0xffff0000u));
      fin[q][2 * e]     = lo + __builtin_bit_cast(float, xv[q][e] << 16) + __builtin_bit_cast(float, bv[q][e] << 16);
      fin[q][2 * e + 1] = hi + __builtin_bit_cast(float, xv[q][e] & 0xffff0000u) + __builtin_bit_cast(float, bv[q][e] & 0xffff0000u);
    }
  }
  __syncthreads();
#pragma unroll
  for (int q = 0; q < 4; ++q) {
    f32x4 v = fin[q];
#pragma unroll
    for (int o = 0; o < 6; ++o) v += *(LDSP f32x4*)(smem + ((o * 6 + w) * 4 + q) * 1024 + lane * 16);
    u32x2 ov = {pack2(v[0], v[1]), pack2(v[2], v[3])};
    *(u32x2*)((char*)out + (size_t)token * 1152 + (nt * 32 + 8 * q + 4 * hh) * 2) = ov;
  }
}

extern "C" void kernel_launch(void* const* d_in, const int* in_sizes, int n_in,
                              void* d_out, int out_size, void* d_ws, size_t ws_size,
                              hipStream_t stream) {
  const u16* x  = (const u16*)d_in[0];
  const u16* wq = (const u16*)d_in[1];
  const u16* wk = (const u16*)d_in[2];
  const u16* wv = (const u16*)d_in[3];
  const u16* wo = (const u16*)d_in[4];
  const u16* w1 = (const u16*)d_in[6];
  const u16* b1 = (const u16*)d_in[7];
  const u16* w2 = (const u16*)d_in[8];
  const u16* b2 = (const u16*)d_in[9];
  char* ws = (char*)d_ws;
  (void)hipFuncSetAttribute((const void*)k1, hipFuncAttributeMaxDynamicSharedMemorySize, K1_LDS);
  (void)hipFuncSetAttribute((const void*)k2, hipFuncAttributeMaxDynamicSharedMemorySize, K2_LDS);
  (void)hipFuncSetAttribute((const void*)k3, hipFuncAttributeMaxDynamicSharedMemorySize, K3_LDS);
  k1<<<256, 512, K1_LDS, stream>>>(x, wq, wk, wv, wo, w1, b1, w2, ws);
  k2<<<256, 512, K2_LDS, stream>>>(ws);
  k3<<<192, 384, K3_LDS, stream>>>(x, b2, ws, (u16*)d_out);
}
```

```cpp
#include <hip/hip_runtime.h>
#include <hip/hip_bf16.h>
#include <stdint.h>
#include <type_traits>

#define DI __device__ __forceinline__
#define LDSP __attribute__((address_space(3)))
#define GLP __attribute__((address_space(1)))

typedef unsigned short u16;
typedef __attribute__((ext_vector_type(8))) short s16x8;
typedef __attribute__((ext_vector_type(4))) short s16x4;
typedef __attribute__((ext_vector_type(16))) float f32x16;
typedef __attribute__((ext_vector_type(4))) float f32x4;
typedef __attribute__((ext_vector_type(2))) float f32x2;
typedef __attribute__((ext_vector_type(2))) __bf16 bf16x2;
typedef __attribute__((ext_vector_type(4))) unsigned u32x4;
typedef __attribute__((ext_vector_type(2))) unsigned u32x2;

constexpr size_t OFF_H    = 0;
constexpr size_t OFF_W2T  = OFF_H + 18874368;
constexpr size_t OFF_WOT  = OFF_W2T + 5308416;
constexpr size_t OFF_Q    = OFF_WOT + 663552;
constexpr size_t OFF_KV   = OFF_Q + 2359296;
constexpr size_t OFF_AO   = OFF_KV + 159744;
constexpr size_t OFF_PART = OFF_AO + 2359296;
constexpr int KV_GRP = 9984, KV_TILE = 2240, KV_BYTES = 159744, KV_HALF = 79872;

#define MFMA32(a, b, c) __builtin_amdgcn_mfma_f32_32x32x16_bf16((a), (b), (c), 0, 0, 0)
#define WAIT_VM(n) asm volatile("s_waitcnt vmcnt(" #n ")" ::: "memory")
#define WAIT_LGKM0() asm volatile("s_waitcnt lgkmcnt(0)" ::: "memory")
#define BAR() __builtin_amdgcn_s_barrier()

DI unsigned pack2(float a, float b) {
  f32x2 v = {a, b};
  return __builtin_bit_cast(unsigned, __builtin_convertvector(v, bf16x2));
}
DI void glds16(const void* gbase, unsigned voff, LDSP char* l) {
  unsigned keep;
  asm volatile("s_mov_b32 %0, m0\n\ts_mov_b32 m0, %3\n\ts_nop 0\n\tglobal_load_lds_dwordx4 %1, %2\n\ts_mov_b32 m0, %0"
               : "=&s"(keep) : "v"(voff), "s"(gbase), "s"((unsigned)(size_t)l) : "memory");
}
DI void glds4(const void* gbase, unsigned voff, LDSP char* l) {
  unsigned keep;
  asm volatile("s_mov_b32 %0, m0\n\ts_mov_b32 m0, %3\n\ts_nop 0\n\tglobal_load_lds_dword %1, %2\n\ts_mov_b32 m0, %0"
               : "=&s"(keep) : "v"(voff), "s"(gbase), "s"((unsigned)(size_t)l) : "memory");
}
DI void store16(void* p, u32x4 v) {
  asm volatile("global_store_dwordx4 %0, %1, off sc1\n\ts_nop 1" :: "v"(p), "v"(v) : "memory");
}
DI void code_warm(int i, int lane, LDSP char* pad) {
  unsigned long long pc;
  asm volatile("s_getpc_b64 %0" : "=s"(pc));
  glds16((const char*)(pc & ~255ull) + i * 1024, lane * 16, pad);
}
DI s16x8 tr_frag(LDSP char* p, int second_off) {
  s16x4 lo = __builtin_amdgcn_ds_read_tr16_b64_v4i16((LDSP s16x4*)p);
  s16x4 hi = __builtin_amdgcn_ds_read_tr16_b64_v4i16((LDSP s16x4*)(p + second_off));
  return __builtin_shufflevector(lo, hi, 0, 1, 2, 3, 4, 5, 6, 7);
}
DI float other_half(float v) {
  unsigned u = __builtin_bit_cast(unsigned, v);
  auto r = __builtin_amdgcn_permlane32_swap(u, u, false, false);
  unsigned o = (threadIdx.x & 32) ? r[0] : r[1];
  return __builtin_bit_cast(float, o);
}

constexpr int K1_SLOT = 36864;
constexpr int K1_BIAS = 4 * K1_SLOT;
constexpr int K1_CS   = K1_BIAS + 1152;
constexpr int K1_CSQ  = K1_CS + 1152;
constexpr int K1_STAT = K1_CSQ + 1024;
constexpr int K1_LDS  = K1_STAT + 2048;

__host__ __device__ constexpr int w_slot(int st) { return (0x231023120ll >> (4 * st)) & 3; }

#define SCHED() __builtin_amdgcn_sched_barrier(0)
#define SGB(mask, n) __builtin_amdgcn_sched_group_barrier((mask), (n), 0)

DI u32x2 gelu4(float a0, float a1, float a2, float a3, const f32x4& cs, const f32x4& bb, float negm, float r, float c0) {
  float z0, z1, z2, z3, t0, t1, t2, t3;
  unsigned o0, o1;
  asm volatile(
      "v_fma_f32 %2, %18, %14, %10\n\tv_fma_f32 %3, %18, %15, %11\n\tv_fma_f32 %4, %18, %16, %12\n\tv_fma_f32 %5, %18, %17, %13\n\t"
      "v_fma_f32 %2, %19, %2, %21\n\tv_fma_f32 %3, %19, %3, %22\n\tv_fma_f32 %4, %19, %4, %23\n\tv_fma_f32 %5, %19, %5, %24\n\t"
      "v_mul_f32 %6, %2, %2\n\tv_mul_f32 %7, %3, %3\n\tv_mul_f32 %8, %4, %4\n\tv_mul_f32 %9, %5, %5\n\t"
      "v_fmamk_f32 %6, %6, 0xbdd2d3e8, %20\n\tv_fmamk_f32 %7, %7, 0xbdd2d3e8, %20\n\tv_fmamk_f32 %8, %8, 0xbdd2d3e8, %20\n\tv_fmamk_f32 %9, %9, 0xbdd2d3e8, %20\n\t"
      "v_mul_f32 %6, %2, %6\n\tv_mul_f32 %7, %3, %7\n\tv_mul_f32 %8, %4, %8\n\tv_mul_f32 %9, %5, %9\n\t"
      "v_exp_f32 %6, %6\n\tv_exp_f32 %7, %7\n\tv_exp_f32 %8, %8\n\tv_exp_f32 %9, %9\n\t"
      "v_add_f32 %6, 1.0, %6\n\tv_add_f32 %7, 1.0, %7\n\tv_add_f32 %8, 1.0, %8\n\tv_add_f32 %9, 1.0, %9\n\t"
      "v_rcp_f32 %6, %6\n\tv_rcp_f32 %7, %7\n\tv_rcp_f32 %8, %8\n\tv_rcp_f32 %9, %9\n\t"
      "v_mul_f32 %2, %2, %6\n\tv_mul_f32 %3, %3, %7\n\tv_mul_f32 %4, %4, %8\n\tv_mul_f32 %5, %5, %9\n\t"
      "v_cvt_pk_bf16_f32 %0, %2, %3\n\tv_cvt_pk_bf16_f32 %1, %4, %5"
      : "=&v"(o0), "=&v"(o1), "=&v"(z0), "=&v"(z1), "=&v"(z2), "=&v"(z3), "=&v"(t0), "=&v"(t1), "=&v"(t2), "=&v"(t3)
      : "v"(a0), "v"(a1), "v"(a2), "v"(a3), "v"(cs[0]), "v"(cs[1]), "v"(cs[2]), "v"(cs[3]), "v"(negm), "v"(r), "v"(c0),
        "v"(bb[0]), "v"(bb[1]), "v"(bb[2]), "v"(bb[3]));
  u32x2 o = {o0, o1};
  return o;
}

template <typename F>
DI void gelu_tile(const f32x16& a, LDSP char* cs_p, LDSP char* bias_p, char* out_p, float negm, float r, F&& between) {
  const float c0 = -2.302208198f;
#pragma unroll
  for (int hf = 0; hf < 2; ++hf) {
    f32x4 cs0 = *(LDSP f32x4*)(cs_p + hf * 64), cs1 = *(LDSP f32x4*)(cs_p + hf * 64 + 32);
    f32x4 bb0 = *(LDSP f32x4*)(bias_p + hf * 64), bb1 = *(LDSP f32x4*)(bias_p + hf * 64 + 32);
    between(2 * hf);
    u32x2 lo = gelu4(a[8 * hf], a[8 * hf + 1], a[8 * hf + 2], a[8 * hf + 3], cs0, bb0, negm, r, c0);
    between(2 * hf + 1);
    u32x2 hi = gelu4(a[8 * hf + 4], a[8 * hf + 5], a[8 * hf + 6], a[8 * hf + 7], cs1, bb1, negm, r, c0);
    u32x4 o = {lo[0], lo[1], hi[0], hi[1]};
    store16(out_p + hf * 1024, o);
  }
}

DI void colsum_frag(const s16x8& a, const s16x8& sel, f32x4& cs) {
  cs = __builtin_amdgcn_mfma_f32_16x16x32_bf16(sel, a, cs, 0, 0, 0);
}

template <int KT>
DI void w1_stage(LDSP char* sb0, LDSP char* sb1, LDSP char* sb2, const s16x8 (&xf)[36], f32x16 (&acc)[3], const s16x8& sel, f32x4& cs) {
  asm volatile("" : "+v"(sb0), "+v"(sb1), "+v"(sb2));
  s16x8 a[2][3];
  a[0][0] = tr_frag(sb0, 768); a[0][1] = tr_frag(sb1, 768); a[0][2] = tr_frag(sb2, 768);
#pragma unroll
  for (int ks = 0; ks < 12; ++ks) {
    const int cur = ks & 1;
    if (ks + 1 < 12) {
      a[cur ^ 1][0] = tr_frag(sb0 + (ks + 1) * 3072, 768);
      a[cur ^ 1][1] = tr_frag(sb1 + (ks + 1) * 3072, 768);
      a[cur ^ 1][2] = tr_frag(sb2 + (ks + 1) * 3072, 768);
    }
#pragma unroll
    for (int t = 0; t < 3; ++t) acc[t] = MFMA32(a[cur][t], xf[12 * KT + ks], acc[t]);
    colsum_frag(a[cur][0], sel, cs);
    SGB(0x100, 2); SGB(0x8, 1); SGB(0x100, 2); SGB(0x8, 1); SGB(0x100, 2); SGB(0x8, 2);
    SCHED();
  }
}

DI void qkv_stage(LDSP char* sb, const s16x8 (&xf)[36], f32x16& qa, const s16x8& sel, f32x4& cs) {
  asm volatile("" : "+v"(sb));
  s16x8 a[3];
  a[0] = tr_frag(sb, 256);
  a[1] = tr_frag(sb + 1024, 256);
#pragma unroll
  for (int s = 0; s < 36; ++s) {
    if (s + 2 < 36) a[(s + 2) % 3] = tr_frag(sb + (s + 2) * 1024, 256);
    qa = MFMA32(a[s % 3], xf[s], qa);
    colsum_frag(a[s % 3], sel, cs);
    SCHED();
  }
}

__global__ __launch_bounds__(512, 2)
void k1(const u16* __restrict__ x, const u16* __restrict__ wq, const u16* __restrict__ wk, const u16* __restrict__ wv,
        const u16* __restrict__ wo, const u16* __restrict__ w1, const u16* __restrict__ b1, const u16* __restrict__ w2,
        char* __restrict__ ws) {
  extern __shared__ __attribute__((aligned(16))) char smem_[];
  LDSP char* smem = (LDSP char*)smem_;
  const int tid = threadIdx.x, lane = tid & 63;
  const int w8 = __builtin_amdgcn_readfirstlane(tid >> 6);
  const int w = w8 & 3, grp = w8 >> 2;
  const int b = blockIdx.x, xcd = b & 7, idx = b >> 3;
  const int tg = (xcd >> 1) * 4 + (idx & 3), j = (xcd & 1) * 8 + (idx >> 2);
  const int t32 = lane & 31, hh = lane >> 5;
  const int tt = tg * 4 + w;
  if (w8 >= 6 && idx * 2 + (w8 - 6) < 63) code_warm(idx * 2 + (w8 - 6), lane, smem + K1_STAT + 1024);
  const bool heavy = (j & 3) == 0;
  constexpr int rk = 0, rc = 0;

  const char* w1w = (const char*)w1 + (size_t)(48 * w) * 9216 + (size_t)j * 576;
  unsigned off3[3];
#pragma unroll
  for (int i = 0; i < 3; ++i) {
    const unsigned bb = 1024 * i + 16 * lane;
    off3[i] = (bb / 192) * 9216 + (bb % 192);
  }
  auto issue_w1_rows = [&](int st, int r) {
    const int c = st / 3, kt = st - 3 * c;
    const char* g = w1w + ((c + rc) % 3) * 192 + (size_t)((kt + rk) % 3) * 192 * 9216 + r * 16 * 9216;
    LDSP char* l = smem + w_slot(st) * K1_SLOT + w * 9216 + r * 3072;
#pragma unroll
    for (int i = 0; i < 3; ++i) glds16(g, off3[i], l + i * 1024);
  };
  auto issue_w1 = [&](int st) {
#pragma unroll
    for (int r = 0; r < 3; ++r) issue_w1_rows(st, r);
  };
  auto issue_x = [&](int q, int slot) {
    int ln = tid & 63; asm volatile("" : "+v"(ln));
    const char* g = (const char*)x + (size_t)(tt * 32) * 1152;
    LDSP char* l = smem + slot * K1_SLOT + w * 9216;
    unsigned row = (unsigned)ln / 18, pos = (unsigned)ln % 18;
    const unsigned b0 = q * 288 + rk * 384;
#pragma unroll
    for (int i = 0; i < 9; ++i) {
      unsigned cb = b0 + ((pos ^ ((row >> 3) & 1)) << 4);
      cb = cb >= 1152 ? cb - 1152 : cb;
      glds16(g, row * 1152 + cb, l + i * 1024);
      pos += 10; row += 3;
      if (pos >= 18) { pos -= 18; row += 1; }
    }
  };
  auto issue_qkv = [&](int tile, int slot, int part) {
    int ln = tid & 63; asm volatile("" : "+v"(ln));
    LDSP char* l = smem + slot * K1_SLOT + w * 9216 + part * 3072;
    const int row0 = 144 * w + 48 * part + 192 * rk;
    if (tile < 18) {
      const char* g = (const char*)wq + tile * 64;
      const unsigned vo = (ln >> 2) * 1152 + (ln & 3) * 16;
#pragma unroll
      for (int i = 0; i < 3; ++i) glds16(g + (size_t)((row0 + 16 * i) % 576) * 1152, vo, l + i * 1024);
    } else {
      const u16* src = (tile == 18) ? wk : wv;
      int dw = ln & 15; dw = dw > 8 ? 8 : dw;
      const unsigned vo = (ln >> 4) * 36 + dw * 4;
#pragma unroll
      for (int i = 0; i < 12; ++i) glds4((const char*)src + ((row0 + 4 * i) % 576) * 36, vo, l + i * 256);
    }
  };
  const int lw = (tg * 12 + (j - (j >> 2) - 1)) * 4 + w;
  auto issue_unit = [&](int part) {
    if (lw < 648) {
      int ln = tid & 63; asm volatile("" : "+v"(ln));
      LDSP char* l = smem + 0 * K1_SLOT + w * 9216 + part * 3072;
      const int u = lw - 576, ks = u >> 1;
      const bool plain = ks < 32;
      const char* g = lw < 576 ? (const char*)w2 + (size_t)((lw >> 1) * 16) * 1152 + (lw & 1) * 576
                               : (const char*)wo + (size_t)(plain ? ks * 18 : 144 * (ks - 32) + 16) * 1152 + (u & 1) * 576;
      const unsigned p0 = 192 * part + ln;
      unsigned row = p0 / 36, pos = p0 % 36;
#pragma unroll
      for (int i = 0; i < 3; ++i) {
        const unsigned srow = plain ? row : 18 * (row >> 1) + (row & 1);
        glds16(g, srow * 1152 + pos * 16, l + i * 1024);
        pos += 28; row += 1;
        if (pos >= 36) { pos -= 36; row += 1; }
      }
    }
  };

  unsigned bias_raw = 0, bias_raw2 = 0;
  if (grp == 1) {
    asm volatile("global_load_ushort %0, %2, %4\n\tglobal_load_ushort %1, %3, %4" : "=&v"(bias_raw), "=&v"(bias_raw2)
                 : "v"((unsigned)((tid - 256) * 2)), "v"((unsigned)((tid < 288 ? tid : 287) * 2)), "s"(b1 + j * 288) : "memory");
  }
  if (grp == 0) { issue_x(0, 3); issue_x(1, 1); }
  else          { issue_w1(0);   issue_w1(1); }
  s16x8 xf[36];
  const int xl = w * 9216 + t32 * 288 + ((hh ^ ((t32 >> 3) & 1)) << 4);
  auto read_x = [&](int q, int slot) {
#pragma unroll
    for (int s = 0; s < 9; ++s) xf[9 * q + s] = *(LDSP s16x8*)(smem + slot * K1_SLOT + xl + s * 32);
  };
  if (grp == 0) WAIT_VM(9);
  BAR();
  read_x(0, 3);
  if (grp == 0) { WAIT_VM(0); }
  else {
    asm volatile("s_waitcnt vmcnt(9)" : "+v"(bias_raw), "+v"(bias_raw2) :: "memory");
    *(LDSP float*)(smem + K1_BIAS + (tid - 256) * 4) = __builtin_bit_cast(float, bias_raw << 16);
    if (tid < 288) *(LDSP float*)(smem + K1_BIAS + tid * 4) = __builtin_bit_cast(float, bias_raw2 << 16);
  }
  WAIT_LGKM0(); BAR();
  read_x(1, 1);
  WAIT_LGKM0(); BAR();

  const int q4 = (lane & 15) >> 2, p4 = lane & 3, g16 = (lane >> 4) & 1;
  const int rot = (w == 3) ? 0 : w;
  const int tl0 = rot, tl1 = (rot + 1) % 3, tl2 = (rot + 2) % 3;
  const int lo_w1 = (8 * hh + q4) * 192 + 32 * g16 + 8 * p4;
  const int lo_q  = (8 * hh + q4) * 64 + 32 * g16 + 8 * p4;
  char* hout = ws + OFF_H + ((size_t)tt * 288 + (size_t)j * 18) * 1024 + lane * 16;
  f32x16 acc[3];
  float negm = 0.f, r1 = 0.f, rm = 0.f;
  const unsigned selv = ((lane & 15) == ((lane >> 4) & 1)) ? 0x3f803f80u : 0u;
  const u32x4 selu = {selv, selv, selv, selv};
  const s16x8 sel = __builtin_bit_cast(s16x8, selu);

  auto load_stats = [&]() {
    f32x2 st = *(LDSP f32x2*)(smem + K1_STAT + (w * 32 + t32) * 8);
    negm = -st[0] * (1.0f / 576.0f);
    const float var = __builtin_fmaxf(st[1] * (1.0f / 576.0f) - negm * negm, 0.f);
    auto rb = [](float v) { return __builtin_bit_cast(float, pack2(v, 0.f) << 16); };
    const float s1 = rb(__builtin_sqrtf(rb(rb(var) + 1e-5f)));
    r1 = 1.0f / s1;
    const float s2 = rb(__builtin_sqrtf(rb(rb(var * r1 * r1) + 1e-5f)));
    rm = r1 / s2;
  };
  auto mfma_chunk = [&](auto cc) {
    constexpr int c = decltype(cc)::value;
    __builtin_amdgcn_s_setprio(0);
    f32x4 cs = {0.f, 0.f, 0.f, 0.f};
#pragma unroll
    for (int t = 0; t < 3; ++t)
#pragma unroll
      for (int i = 0; i < 16; ++i) acc[t][i] = 0.f;
    LDSP char* s0 = smem + lo_w1 + tl0 * 64;
    LDSP char* s1 = smem + lo_w1 + tl1 * 64;
    LDSP char* s2 = smem + lo_w1 + tl2 * 64;
    constexpr int o0 = w_slot(3 * c) * K1_SLOT, o1 = w_slot(3 * c + 1) * K1_SLOT, o2 = w_slot(3 * c + 2) * K1_SLOT;
    w1_stage<0>(s0 + o0, s1 + o0, s2 + o0, xf, acc, sel, cs);
    if (c == 1) { WAIT_VM(9); } else if (c == 2) { WAIT_VM(11); }
    WAIT_LGKM0(); BAR();
    if (c == 0) { read_x(2, 3); WAIT_LGKM0(); BAR(); }
    w1_stage<1>(s0 + o1, s1 + o1, s2 + o1, xf, acc, sel, cs);
    if (c != 0) WAIT_VM(0);
    WAIT_LGKM0(); BAR();
    if (c == 0) { read_x(3, 0); WAIT_LGKM0(); BAR(); }
    w1_stage<2>(s0 + o2, s1 + o2, s2 + o2, xf, acc, sel, cs);
    if (lane < 16 && w < 3) {
      *(LDSP float*)(smem + K1_CS + (c * 96 + tl0 * 32 + lane) * 4) = cs[0];
      *(LDSP float*)(smem + K1_CS + (c * 96 + tl0 * 32 + 16 + lane) * 4) = cs[1];
    }
    WAIT_LGKM0(); BAR();
    __builtin_amdgcn_s_setprio(1);
  };
  auto gelu_t = [&](int c, int t, int tl, int st_issue) {
    const int ca = (c + rc) % 3;
    gelu_tile(acc[t], smem + K1_CS + (c * 96 + tl * 32) * 4 + hh * 16, smem + K1_BIAS + (ca * 96 + tl * 32) * 4 + hh * 16,
              hout + (ca * 6 + tl * 2) * 1024, negm, rm, [&](int i) {
      if (i < 3) {
        if (st_issue >= 0 && st_issue < 9) issue_w1_rows(st_issue, i);
        else if (st_issue == 9) issue_qkv(j, 1, i);
        else if (st_issue == 10) { if (heavy) issue_qkv(16 + (j >> 2), 0, i); else issue_unit(i); }
      }
    });
  };

  if (grp == 0) {
    mfma_chunk(std::integral_constant<int, 0>{});
    load_stats();
    gelu_t(0, 0, tl0, 6); WAIT_LGKM0(); BAR();
    gelu_t(0, 1, tl1, 7); WAIT_LGKM0(); BAR();
    gelu_t(0, 2, tl2, 8); WAIT_VM(22); WAIT_LGKM0(); BAR();
    mfma_chunk(std::integral_constant<int, 2>{});
    gelu_t(2, 0, tl0, -1); gelu_t(2, 1, tl1, -1); gelu_t(2, 2, tl2, -1);
  } else {
    f32x4 rs = {0.f, 0.f, 0.f, 0.f};
#pragma unroll
    for (int i = 0; i < 16; ++i) acc[0][i] = 0.f;
    auto stats_frag = [&](int s) {
      colsum_frag(xf[s], sel, rs);
      acc[0] = MFMA32(xf[s], xf[s], acc[0]);
    };
    __builtin_amdgcn_s_setprio(1);
    issue_x(2, 3);
#pragma unroll
    for (int s = 0; s < 18; ++s) { if (s % 6 == 0) issue_w1_rows(2, s / 6); stats_frag(s); }
    WAIT_VM(9); BAR();
    read_x(2, 3); WAIT_LGKM0(); BAR();
    issue_x(3, 0);
#pragma unroll
    for (int s = 18; s < 27; ++s) { if (s % 3 == 0) issue_w1_rows(3, (s - 18) / 3); stats_frag(s); }
    WAIT_VM(9); BAR();
    read_x(3, 0); WAIT_LGKM0(); BAR();
    issue_w1(4);
#pragma unroll
    for (int s = 27; s < 36; ++s) { if (s % 3 == 0) issue_w1_rows(5, (s - 27) / 3); stats_frag(s); }
    {
      const int dreg = (t32 & 3) + 4 * (t32 >> 3);
      float s2 = 0.f;
#pragma unroll
      for (int i = 0; i < 16; ++i) s2 = (i == dreg) ? acc[0][i] : s2;
      if (hh == ((t32 >> 2) & 1)) *(LDSP float*)(smem + K1_STAT + (w * 32 + t32) * 8 + 4) = s2;
      if (lane < 16) {
        *(LDSP float*)(smem + K1_STAT + (w * 32 + lane) * 8) = rs[0];
        *(LDSP float*)(smem + K1_STAT + (w * 32 + 16 + lane) * 8) = rs[1];
      }
    }
    WAIT_VM(18); WAIT_LGKM0(); BAR();
    mfma_chunk(std::integral_constant<int, 1>{});
    load_stats();
    gelu_t(1, 0, tl0, 10); WAIT_LGKM0(); BAR();
    gelu_t(1, 1, tl1, 9); WAIT_LGKM0(); BAR();
    gelu_t(1, 2, tl2, -1);
    WAIT_VM(2); WAIT_LGKM0(); BAR();
    __builtin_amdgcn_s_setprio(2);

    auto qkv_tile = [&](int tile, int slot) {
      f32x16 qa;
#pragma unroll
      for (int i = 0; i < 16; ++i) qa[i] = 0.f;
      f32x4 cs = {0.f, 0.f, 0.f, 0.f};
      qkv_stage(smem + slot * K1_SLOT + lo_q, xf, qa, sel, cs);
      int ln = tid & 63; asm volatile("" : "+v"(ln));
      const int t32 = ln & 31, hh = ln >> 5, token = tt * 32 + t32;
      LDSP char* cp = smem + K1_CSQ + (w * 2 + (slot & 1)) * 128;
      if (ln < 16) { *(LDSP float*)(cp + ln * 4) = cs[0]; *(LDSP float*)(cp + 64 + ln * 4) = cs[1]; }
      WAIT_LGKM0();
#pragma unroll
      for (int g = 0; g < 4; ++g) {
        f32x4 c4 = *(LDSP f32x4*)(cp + g * 32 + hh * 16);
#pragma unroll
        for (int i = 0; i < 4; ++i) qa[4 * g + i] = __builtin_fmaf(negm, c4[i], qa[4 * g + i]);
      }
      if (tile < 18) {
        const float sc = r1 * (0.23570226039551584f * 1.4426950408889634f);
        char* qp = ws + OFF_Q + (size_t)tt * 36864;
#pragma unroll
        for (int i = 0; i < 8; ++i) {
          const unsigned n = tile * 32 + 8 * (i >> 1) + 4 * hh + 2 * (i & 1);
          const unsigned head = n / 18, d = n - head * 18;
          const unsigned off = d < 16 ? (t32 + 32 * ((d >> 2) & 1)) * 16 + ((d >> 3) * 2 + ((d >> 1) & 1)) * 4 : 1024 + t32 * 4;
          *(unsigned*)(qp + head * 1152 + off) = pack2(qa[2 * i] * sc, qa[2 * i + 1] * sc);
        }
      } else if (tile == 18) {
        u32x4 o = {pack2(qa[0] * r1, qa[1] * r1), pack2(qa[2] * r1, qa[3] * r1),
                   pack2(qa[4] * r1, qa[5] * r1), pack2(qa[6] * r1, qa[7] * r1)};
        char* gp = ws + OFF_KV + (size_t)tg * KV_GRP;
        *(u32x4*)(gp + 1024 + w * KV_TILE + ln * 16) = o;
        if (hh == 0) *(unsigned*)(gp + (w >> 1) * 512 + t32 * 16 + (w & 1) * 4) = pack2(qa[8] * r1, qa[9] * r1);
        else if ((w & 1) == 0) {
          u32x2 cst = {w == 0 ? 0x3f803f80u : 0u, 0u};
          *(u32x2*)(gp + (w >> 1) * 512 + t32 * 16 + 8) = cst;
        }
      } else {
        const int s = t32 >> 4, hk = (t32 >> 2) & 1, jj = ((t32 >> 3) & 1) * 4 + (t32 & 3);
        char* vp = ws + OFF_KV + (size_t)tg * KV_GRP + 2048 + w * KV_TILE + (s * 2 + hk) * 304 + jj * 2;
#pragma unroll
        for (int i = 0; i < 8; ++i) {
          const int d = (i & 3) + 8 * (i >> 2) + 4 * hh;
          *(u16*)(vp + d * 16) = (u16)(pack2(qa[i] * r1, 0.f) & 0xffffu);
        }
        if (hh == 0) {
          *(u16*)(vp + 16 * 16) = (u16)(pack2(qa[8] * r1, 0.f) & 0xffffu);
          *(u16*)(vp + 17 * 16) = (u16)(pack2(qa[9] * r1, 0.f) & 0xffffu);
          *(u16*)(vp + 18 * 16) = (u16)0x3f80;
        }
      }
    };
    qkv_tile(j, 1);
    if (heavy) {
      qkv_tile(16 + (j >> 2), 0);
    } else if (lw < 648) {
      int ln = tid & 63; asm volatile("" : "+v"(ln));
      const int hh = ln >> 5, q4 = (ln & 15) >> 2, p4 = ln & 3, g16 = (ln >> 4) & 1;
      LDSP char* sb = smem + 0 * K1_SLOT + w * 9216 + (4 * hh + q4) * 576 + 32 * g16 + 8 * p4;
      char* dst = (lw < 576) ? ws + OFF_W2T + ((size_t)(lw >> 1) * 18 + (lw & 1) * 9) * 1024
                             : ws + OFF_WOT + ((size_t)((lw - 576) >> 1) * 18 + (lw & 1) * 9) * 1024;
#pragma unroll
      for (int n = 0; n < 9; ++n) {
        s16x8 f = tr_frag(sb + n * 64, 8 * 576);
        store16(dst + n * 1024 + ln * 16, __builtin_bit_cast(u32x4, f));
      }
    }
  }
}

constexpr int K2_FLAG = KV_BYTES;
constexpr int K2_LDS  = KV_BYTES + 128 + 1024;
enum { FL_KVA = 0, FL_KVG = 1, FL_PROG = 2, FL_GW = 3, FL_DONE = 4, FL_SENT = 5, FL_GW2 = 6 };

#define EXP2(x) __builtin_amdgcn_exp2f(x)

DI void flag_set(LDSP char* smem, int set, int i, int lane) {
  if (lane == 0) *(volatile LDSP unsigned*)(smem + K2_FLAG + set * 16 + i * 4) = 1u;
}
DI void flag_wait_all(LDSP char* smem, int set) {
  while (true) {
    u32x4 f = *(volatile LDSP u32x4*)(smem + K2_FLAG + set * 16);
    if (f[0] & f[1] & f[2] & f[3]) break;
    __builtin_amdgcn_s_sleep(2);
  }
  asm volatile("" ::: "memory");
}
DI void flag_wait_one(LDSP char* smem, int set, int i) {
  while (*(volatile LDSP unsigned*)(smem + K2_FLAG + set * 16 + i * 4) == 0u) __builtin_amdgcn_s_sleep(1);
  asm volatile("" ::: "memory");
}

DI void attn_step(f32x16& scur, f32x16& snext, const s16x8& k0, const s16x8& k1, const s16x8& qn0, const s16x8& qn1,
                  const s16x8& vprev1, f32x16& oprev, const s16x8& vcur0, f32x16& ocur, u32x4& p0, u32x4& p1) {
  f32x16 zero;
#pragma unroll
  for (int i = 0; i < 16; ++i) zero[i] = 0.f;
#define EX(i) do { float t_ = scur[i]; asm volatile("v_exp_f32 %0, %0" : "+v"(t_)); scur[i] = t_; } while (0)
  p1[2] = pack2(snext[12], snext[13]);
  p1[3] = pack2(snext[14], snext[15]);
  SCHED();
  snext = MFMA32(k0, qn0, zero);
  SCHED();
#pragma unroll
  for (int i = 0; i < 3; ++i) EX(i);
  SCHED();
  snext = MFMA32(k1, qn1, snext);
  SCHED();
#pragma unroll
  for (int i = 3; i < 8; ++i) EX(i);
  SCHED();
  p0[0] = pack2(scur[0], scur[1]);
  p0[1] = pack2(scur[2], scur[3]);
  p0[2] = pack2(scur[4], scur[5]);
  p0[3] = pack2(scur[6], scur[7]);
  SCHED();
  oprev = MFMA32(vprev1, __builtin_bit_cast(s16x8, p1), oprev);
  SCHED();
#pragma unroll
  for (int i = 8; i < 8 + 3; ++i) EX(i);
  SCHED();
  ocur = MFMA32(vcur0, __builtin_bit_cast(s16x8, p0), ocur);
  SCHED();
#pragma unroll
  for (int i = 8 + 3; i < 16; ++i) EX(i);
  SCHED();
  p1[0] = pack2(scur[8], scur[9]);
  p1[1] = pack2(scur[10], scur[11]);
  SCHED();
#undef EX
}

DI s16x8 make_q1(unsigned qe, unsigned mneg, int i, int hh) {
  const unsigned a = (hh == (i >> 1)) ? qe : 0u;
  u32x4 r = {(i & 1) ? 0u : a, (i & 1) ? a : 0u, hh ? 0u : mneg, 0u};
  return __builtin_bit_cast(s16x8, r);
}
DI unsigned neg_hi_lo(float m) {
  unsigned hi = pack2(-m, 0.f) & 0xffffu;
  float rem = -m - __builtin_bit_cast(float, hi << 16);
  unsigned lo = pack2(rem, 0.f) & 0xffffu;
  return hi | (lo << 16);
}
DI int v_row(int t32) { return t32 > 18 ? ((t32 >= 20 && t32 < 28) ? 0 : 4) : t32; }

DI f32x16 attn_exact(const char* kv, int lane, u32x4 q0u, unsigned qe) {
  const int t32 = lane & 31, hh = lane >> 5;
  const s16x8 q0 = __builtin_bit_cast(s16x8, q0u);
  f32x16 zero, o;
#pragma unroll
  for (int i = 0; i < 16; ++i) { zero[i] = 0.f; o[i] = 0.f; }
  float m = -INFINITY;
#pragma unroll 1
  for (int T = 0; T < 64; ++T) {
    const char* gp = kv + (T >> 2) * KV_GRP;
    s16x8 k0 = *(const s16x8*)(gp + 1024 + (T & 3) * KV_TILE + lane * 16);
    s16x8 k1 = *(const s16x8*)(gp + lane * 16);
    f32x16 s = MFMA32(k0, q0, zero);
    s = MFMA32(k1, make_q1(qe, 0u, T & 3, hh), s);
#pragma unroll
    for (int i = 0; i < 16; ++i) m = __builtin_fmaxf(m, s[i]);
  }
  m = __builtin_fmaxf(m, other_half(m));
  const unsigned mneg = neg_hi_lo(m);
#pragma unroll 1
  for (int T = 0; T < 64; ++T) {
    const char* gp = kv + (T >> 2) * KV_GRP;
    const char* tp = gp + 1024 + (T & 3) * KV_TILE;
    s16x8 k0 = *(const s16x8*)(tp + lane * 16);
    s16x8 k1 = *(const s16x8*)(gp + lane * 16);
    s16x8 v0 = *(const s16x8*)(tp + 1024 + hh * 304 + v_row(t32) * 16);
    s16x8 v1 = *(const s16x8*)(tp + 1024 + 608 + hh * 304 + v_row(t32) * 16);
    f32x16 s = MFMA32(k0, q0, zero);
    s = MFMA32(k1, make_q1(qe, mneg, T & 3, hh), s);
    u32x4 p0, p1;
#pragma unroll
    for (int i = 0; i < 4; ++i) {
      p0[i] = pack2(EXP2(s[2 * i]), EXP2(s[2 * i + 1]));
      p1[i] = pack2(EXP2(s[8 + 2 * i]), EXP2(s[8 + 2 * i + 1]));
    }
    o = MFMA32(v0, __builtin_bit_cast(s16x8, p0), o);
    o = MFMA32(v1, __builtin_bit_cast(s16x8, p1), o);
  }
  return o;
}

typedef __amdgpu_buffer_rsrc_t rsrc_t;
DI s16x8 bload(rsrc_t r, unsigned voff, unsigned soff) {
  return __builtin_bit_cast(s16x8, __builtin_amdgcn_raw_buffer_load_b128(r, voff, soff, 0));
}
template <int NT>
DI void gemm2_load(rsrc_t r, unsigned hp, unsigned wp, unsigned wsp, unsigned lo16, int k, s16x8 (&hb)[2], s16x8 (&wb)[5]) {
  hb[0] = bload(r, lo16, hp + k * 1024);
  wb[0] = bload(r, lo16, wp + k * 18432);
  hb[1] = bload(r, lo16, hp + k * 1024 + 288 * 1024);
#pragma unroll
  for (int n = 1; n < 4; ++n) wb[n] = bload(r, lo16 + n * 1024, wp + k * 18432);
  if (NT == 5) wb[4] = bload(r, lo16, wsp + k * 18432);
}
template <int NT, int NTN>
DI void gemm2_step(rsrc_t r, unsigned hp, unsigned wp, unsigned wsp, unsigned lo16, int kn, s16x8 (&hb)[2], s16x8 (&wb)[5],
                   f32x16 (&acc)[2][5]) {
#pragma unroll
  for (int n = 0; n < NT; ++n) {
    acc[0][n] = MFMA32(wb[n], hb[0], acc[0][n]);
    acc[1][n] = MFMA32(wb[n], hb[1], acc[1][n]);
    SCHED();
    if (n < 4) wb[n] = bload(r, lo16 + n * 1024, wp + kn * 18432);
    else if (NTN == 5) wb[4] = bload(r, lo16, wsp + kn * 18432);
    SCHED();
  }
  if (NT == 4 && NTN == 5) wb[4] = bload(r, lo16, wsp + kn * 18432);
  hb[0] = bload(r, lo16, hp + kn * 1024);
  hb[1] = bload(r, lo16, hp + kn * 1024 + 288 * 1024);
  SCHED();
}
template <int N1, int N2, typename F, typename G>
DI void gemm2_all(rsrc_t r, unsigned hp, unsigned wp, unsigned wsp, unsigned lo16, f32x16 (&acc)[2][5], F&& after_first_loads, G&& after_first_step) {
  s16x8 hA[2], wA[5], hB[2], wB[5];
  after_first_loads();
  gemm2_load<N1>(r, hp, wp, wsp, lo16, 0, hA, wA);
  gemm2_load<N1>(r, hp, wp, wsp, lo16, 1, hB, wB);
  SCHED();
  after_first_step();
#pragma unroll 1
  for (int k = 0; k < 16; k += 2) {
    gemm2_step<N1, N1>(r, hp, wp, wsp, lo16, k + 2, hA, wA, acc);
    gemm2_step<N1, N1>(r, hp, wp, wsp, lo16, k + 3, hB, wB, acc);
  }
  gemm2_step<N1, N2>(r, hp, wp, wsp, lo16, 18, hA, wA, acc);
  gemm2_step<N1, N2>(r, hp, wp, wsp, lo16, 19, hB, wB, acc);
#pragma unroll 1
  for (int k = 18; k < 36; k += 2) {
    gemm2_step<N2, N2>(r, hp, wp, wsp, lo16, (k + 2 < 36) ? k + 2 : 35, hA, wA, acc);
    gemm2_step<N2, N2>(r, hp, wp, wsp, lo16, (k + 3 < 36) ? k + 3 : 35, hB, wB, acc);
  }
}

__global__ __launch_bounds__(512, 2)
void k2(char* __restrict__ ws) {
  extern __shared__ __attribute__((aligned(16))) char smem_[];
  LDSP char* smem = (LDSP char*)smem_;
  const int tid = threadIdx.x, lane = tid & 63;
  const int w = __builtin_amdgcn_readfirstlane(tid >> 6);
  const int b = blockIdx.x;
  const int t32 = lane & 31, hh = lane >> 5;
  if (w == 7 && (b >> 3) < 22) code_warm(b >> 3, lane, smem + KV_BYTES + 128);
  if (tid < 32) *(LDSP unsigned*)(smem + K2_FLAG + tid * 4) = 0u;
  const char* kvg = ws + OFF_KV;

  if (w < 4) {
    const int tt = (b & 7) * 8 + (b >> 5), hg = (b >> 3) & 3;
    const int rs = w & 1, kh2 = w >> 1;
    const int head0 = hg * 8 + rs * 4;
#pragma unroll
    for (int i = 0; i < 5; ++i) {
      glds16(kvg + (i * 4 + w) * 1024, lane * 16, smem + (i * 4 + w) * 1024);
      glds16(kvg + KV_HALF + (i * 4 + w) * 1024, lane * 16, smem + KV_HALF + (i * 4 + w) * 1024);
    }
    u32x4 q0u[4];
    unsigned qe[4];
#pragma unroll
    for (int qb = 0; qb < 4; ++qb) {
      const char* qp = ws + OFF_Q + (size_t)tt * 36864 + (head0 + qb) * 1152;
      q0u[qb] = *(const u32x4*)(qp + lane * 16);
      qe[qb] = *(const unsigned*)(qp + 1024 + t32 * 4);
    }
    WAIT_LGKM0(); BAR();
    WAIT_VM(0);
    flag_set(smem, FL_KVA, w, lane);
    flag_wait_all(smem, FL_KVA);
    s16x8 q0[4], q1[4][4];
    f32x16 o[4];
    f32x16 sa, sb;
#pragma unroll
    for (int i = 0; i < 16; ++i) sb[i] = 0.f;
    {
      s16x8 k0 = *(LDSP s16x8*)(smem + 1024 + lane * 16);
      s16x8 k1 = *(LDSP s16x8*)(smem + lane * 16);
#pragma unroll
      for (int qb = 0; qb < 4; ++qb) {
        asm volatile("" : "+v"(q0u[qb]), "+v"(qe[qb]));
        q0[qb] = __builtin_bit_cast(s16x8, q0u[qb]);
        f32x16 s = MFMA32(k0, q0[qb], sb);
        s = MFMA32(k1, make_q1(qe[qb], 0u, 0, hh), s);
        float m = s[0];
#pragma unroll
        for (int i = 1; i < 16; ++i) m = __builtin_fmaxf(m, s[i]);
        m = __builtin_fmaxf(m, other_half(m));
        const unsigned mneg = neg_hi_lo(m);
#pragma unroll
        for (int i = 0; i < 4; ++i) q1[i][qb] = make_q1(qe[qb], mneg, i, hh);
#pragma unroll
        for (int i = 0; i < 16; ++i) o[qb][i] = 0.f;
      }
    }
    LDSP char* kp = smem + kh2 * KV_HALF + lane * 16;
    LDSP char* vp = smem + kh2 * KV_HALF + 2048 + hh * 304 + v_row(t32) * 16;
    u32x4 p0 = {0u, 0u, 0u, 0u}, p1 = {0u, 0u, 0u, 0u};
    s16x8 k1c = *(LDSP s16x8*)(kp), k1n;
    s16x8 k0b[2], v0b[2], v1b[2];
    k0b[0] = *(LDSP s16x8*)(kp + 1024);
    const unsigned long long vmask = 0x0007ffff0007ffffull;
#pragma unroll
    for (int i = 0; i < 2; ++i) { v0b[i] = s16x8{0, 0, 0, 0, 0, 0, 0, 0}; v1b[i] = s16x8{0, 0, 0, 0, 0, 0, 0, 0}; }
#define VLD(dst, addr, off) asm volatile("s_mov_b64 exec, %2\n\tds_read_b128 %0, %1 offset:%3\n\ts_mov_b64 exec, -1" \
                                         : "+v"(dst) : "v"(addr), "s"(vmask), "i"(off) : "memory")
#define VWAIT(dst) asm volatile("s_waitcnt lgkmcnt(0)" : "+v"(dst) :: "memory")
    VLD(v0b[0], vp, 0);
    sa = MFMA32(k0b[0], q0[0], sb);
    sa = MFMA32(k1c, q1[0][0], sa);
    asm volatile("s_nop 15" : "+v"(sa));
#pragma unroll 1
    for (int g = 0; g < 8; ++g) {
      if (g == 1) flag_wait_all(smem, FL_KVG);
      if (g == 5) flag_set(smem, FL_PROG, w, lane);
      const int nxg = (g < 7) ? KV_GRP : 0;
#pragma unroll
      for (int i = 0; i < 4; ++i) {
        const int c = i & 1, n = c ^ 1;
        const int nt_off = (i < 3) ? (i + 1) * KV_TILE : nxg;
        VWAIT(v0b[c]);
        VLD(v1b[c], vp, i * KV_TILE + 608);
        SCHED();
        attn_step(sa, sb, k0b[c], k1c, q0[1], q1[i][1], v1b[n], o[3], v0b[c], o[0], p0, p1);
        VWAIT(v1b[c]);
        k0b[n] = *(LDSP s16x8*)(kp + 1024 + nt_off);
        if (i == 3) k1n = *(LDSP s16x8*)(kp + nxg);
        SCHED();
        attn_step(sb, sa, k0b[c], k1c, q0[2], q1[i][2], v1b[c], o[0], v0b[c], o[1], p0, p1);
        if (i < 3) VLD(v0b[n], vp, (i + 1) * KV_TILE); else { LDSP char* vn = vp + nxg; VLD(v0b[n], vn, 0); }
        SCHED();
        attn_step(sa, sb, k0b[c], k1c, q0[3], q1[i][3], v1b[c], o[1], v0b[c], o[2], p0, p1);
        attn_step(sb, sa, k0b[n], (i == 3) ? k1n : k1c, q0[0], q1[(i + 1) & 3][0], v1b[c], o[2], v0b[c], o[3], p0, p1);
      }
      k1c = k1n;
      kp += KV_GRP; vp += KV_GRP;
    }
    VWAIT(v0b[0]);
    p1[2] = pack2(sb[12], sb[13]);
    p1[3] = pack2(sb[14], sb[15]);
    o[3] = MFMA32(v1b[1], __builtin_bit_cast(s16x8, p1), o[3]);
    f32x16 keep[2], send[2];
    u32x4 qk0[2];
    unsigned qke[2];
#pragma unroll
    for (int q = 0; q < 2; ++q) {
#pragma unroll
      for (int i = 0; i < 11; ++i) {
        keep[q][i] = kh2 ? o[2 + q][i] : o[q][i];
        send[q][i] = kh2 ? o[q][i] : o[2 + q][i];
      }
#pragma unroll
      for (int i = 11; i < 16; ++i) { keep[q][i] = 0.f; send[q][i] = 0.f; }
#pragma unroll
      for (int i = 0; i < 4; ++i) qk0[q][i] = kh2 ? q0u[2 + q][i] : q0u[q][i];
      { unsigned a_ = qe[q], b_ = qe[2 + q]; asm volatile("" : "+v"(a_), "+v"(b_)); qke[q] = kh2 ? b_ : a_; }
    }
    WAIT_LGKM0();
    flag_set(smem, FL_DONE, w, lane);
    flag_wait_one(smem, FL_DONE, w ^ 1);
    {
      LDSP char* xp = smem + kh2 * KV_HALF + 7 * KV_GRP + rs * 4864;
#pragma unroll
      for (int q = 0; q < 2; ++q) {
        const f32x16 ov = send[q];
        *(LDSP f32x4*)(xp + q * 2432 + lane * 16) = f32x4{ov[0], ov[1], ov[2], ov[3]};
        *(LDSP f32x4*)(xp + q * 2432 + 1024 + lane * 16) = f32x4{ov[4], ov[5], ov[6], ov[7]};
        if (hh == 0) {
          *(LDSP float*)(xp + q * 2432 + 2048 + t32 * 4) = ov[8];
          *(LDSP float*)(xp + q * 2432 + 2176 + t32 * 4) = ov[9];
          *(LDSP float*)(xp + q * 2432 + 2304 + t32 * 4) = ov[10];
        }
      }
    }
    WAIT_LGKM0();
    flag_set(smem, FL_SENT, w, lane);
    flag_wait_one(smem, FL_SENT, w ^ 2);
    bool bad = false;
#pragma unroll
    for (int q = 0; q < 2; ++q) {
      LDSP char* xp = smem + (kh2 ^ 1) * KV_HALF + 7 * KV_GRP + rs * 4864 + q * 2432;
      f32x4 a0 = *(LDSP f32x4*)(xp + lane * 16), a1 = *(LDSP f32x4*)(xp + 1024 + lane * 16);
#pragma unroll
      for (int i = 0; i < 4; ++i) { keep[q][i] += a0[i]; keep[q][4 + i] += a1[i]; }
      keep[q][8] += *(LDSP float*)(xp + 2048 + t32 * 4);
      keep[q][9] += *(LDSP float*)(xp + 2176 + t32 * 4);
      keep[q][10] += *(LDSP float*)(xp + 2304 + t32 * 4);
      float mag = 0.f;
#pragma unroll
      for (int i = 0; i < 8; ++i) mag += __builtin_fabsf(keep[q][i]);
      const float ext = __builtin_fabsf(keep[q][8]) + __builtin_fabsf(keep[q][9]) + 1e12f * __builtin_fabsf(keep[q][10]);
      mag += hh ? 0.f : ext;
      bad = bad || !(mag < 1e30f);
    }
    if (__builtin_expect(__any(bad), 0)) {
#pragma unroll 1
      for (int q = 0; q < 2; ++q) {
        f32x16 ex = attn_exact(kvg, lane, q ? qk0[1] : qk0[0], q ? qke[1] : qke[0]);
        if (q) keep[1] = ex; else keep[0] = ex;
      }
    }
#pragma unroll
    for (int q = 0; q < 2; ++q) {
      float l = keep[q][10];
      const float lo_ = other_half(l);
      const float li = __builtin_amdgcn_rcpf(hh ? lo_ : l);
      const int head = head0 + 2 * kh2 + q;
      u32x4 o4 = {pack2(keep[q][0] * li, keep[q][1] * li), pack2(keep[q][2] * li, keep[q][3] * li),
                  pack2(keep[q][4] * li, keep[q][5] * li), pack2(keep[q][6] * li, keep[q][7] * li)};
      char* ap = ws + OFF_AO + (size_t)tt * 36 * 1024;
      store16(ap + head * 1024 + lane * 16, o4);
      if (hh == 0) {
        const int h8 = head & 7;
        *(unsigned*)(ap + (32 + (head >> 3)) * 1024 + (t32 + 32 * ((h8 >> 1) & 1)) * 16 + ((h8 >> 2) * 2 + (h8 & 1)) * 4) =
            pack2(keep[q][8] * li, keep[q][9] * li);
      }
    }
  } else {
    const int g = w - 4;
    const int xcd = b & 7, idx = b >> 3;
    const int kh = xcd & 1, tp = (xcd >> 1) * 8 + (idx & 7), ng = idx >> 3;
    const int nt0 = (ng >> 1) * 9 + (ng & 1) * 5;
    const int nts = (ng >> 1) * 9 + 4;
    const int fb0 = kh * 144 + g * 36;
    const rsrc_t rsrc = __builtin_amdgcn_make_buffer_rsrc(ws, 0, (unsigned)OFF_WOT, 0x00020000);
    const unsigned hp0 = (unsigned)OFF_H + ((2 * tp) * 288 + fb0) * 1024;
    const unsigned wp = (unsigned)OFF_W2T + (fb0 * 18 + nt0) * 1024;
    const unsigned wsp = (unsigned)OFF_W2T + (fb0 * 18 + nts) * 1024;
    const unsigned lo16 = lane * 16;
    BAR();
    f32x16 acc[2][5];
#pragma unroll
    for (int a = 0; a < 2; ++a)
#pragma unroll
      for (int n = 0; n < 5; ++n)
#pragma unroll
        for (int i = 0; i < 16; ++i) acc[a][n][i] = 0.f;
    auto stage_kv = [&]() {
      __builtin_amdgcn_s_setprio(3);
#pragma unroll 1
      for (int i = g; i < 58; i += 4) {
        glds16(kvg + (20 + i) * 1024, lo16, smem + (20 + i) * 1024);
        glds16(kvg + KV_HALF + (20 + i) * 1024, lo16, smem + KV_HALF + (20 + i) * 1024);
      }
      __builtin_amdgcn_s_setprio(0);
    };
    auto kv_ready = [&]() {
      WAIT_VM(12);
      flag_set(smem, FL_KVG, g, lane);
    };
    if (ng & 1) gemm2_all<4, 5>(rsrc, hp0, wp, wsp, lo16, acc, stage_kv, kv_ready);
    else        gemm2_all<5, 4>(rsrc, hp0, wp, wsp, lo16, acc, stage_kv, kv_ready);
    flag_wait_all(smem, FL_PROG);
    LDSP char* mine   = smem + (g >> 1) * KV_HALF + (g & 1) * 24576 + lo16;
    LDSP char* theirs = smem + (g >> 1) * KV_HALF + ((g & 1) ^ 1) * 24576 + lo16;
    LDSP char* other  = smem + ((g >> 1) ^ 1) * KV_HALF + ((g & 1) ^ 1) * 24576 + lo16;
    auto reduce = [&](auto aa, auto hh_) {
      constexpr int A = decltype(aa)::value, HF = decltype(hh_)::value;
#pragma unroll
      for (int n = 0; n < 5; ++n)
#pragma unroll
        for (int q = 0; q < 4; ++q)
          *(LDSP f32x4*)(mine + (n * 4 + q) * 1024) =
              f32x4{acc[A ^ 1][n][4 * q], acc[A ^ 1][n][4 * q + 1], acc[A ^ 1][n][4 * q + 2], acc[A ^ 1][n][4 * q + 3]};
      WAIT_LGKM0();
      flag_set(smem, FL_GW, g, lane);
      flag_wait_one(smem, FL_GW, g ^ 1);
#pragma unroll
      for (int n = 0; n < 5; ++n)
#pragma unroll
        for (int q = 0; q < 4; ++q) {
          f32x4 v = *(LDSP f32x4*)(theirs + (n * 4 + q) * 1024);
#pragma unroll
          for (int i = 0; i < 4; ++i) acc[A][n][4 * q + i] += v[i];
        }
      WAIT_LGKM0();
#pragma unroll
      for (int n = 0; n < 5; ++n)
#pragma unroll
        for (int q = 0; q < 2; ++q) {
          constexpr int r0 = 8 * (HF ^ 1);
          *(LDSP f32x4*)(theirs + (n * 2 + q) * 1024) =
              f32x4{acc[A][n][r0 + 4 * q], acc[A][n][r0 + 4 * q + 1], acc[A][n][r0 + 4 * q + 2], acc[A][n][r0 + 4 * q + 3]};
        }
      WAIT_LGKM0();
      flag_set(smem, FL_GW2, g, lane);
      flag_wait_one(smem, FL_GW2, g ^ 2);
#pragma unroll
      for (int n = 0; n < 5; ++n) {
        const int nt = (n < 4) ? nt0 + n : nts;
        const int slot = (n < 4) ? kh : kh + 2 * (ng & 1);
        char* pp = ws + OFF_PART + (((size_t)slot * 64 + (2 * tp + A)) * 18 + nt) * 2048 + HF * 1024 + lo16;
        u32x4 o4;
#pragma unroll
        for (int q = 0; q < 2; ++q) {
          f32x4 v = *(LDSP f32x4*)(other + (n * 2 + q) * 1024);
#pragma unroll
          for (int i = 0; i < 4; ++i) v[i] += acc[A][n][8 * HF + 4 * q + i];
          o4[2 * q] = pack2(v[0], v[1]);
          o4[2 * q + 1] = pack2(v[2], v[3]);
        }
        store16(pp, o4);
      }
    };
    using I0 = std::integral_constant<int, 0>;
    using I1 = std::integral_constant<int, 1>;
    if (g == 0) reduce(I0{}, I0{});
    else if (g == 1) reduce(I1{}, I0{});
    else if (g == 2) reduce(I0{}, I1{});
    else reduce(I1{}, I1{});
  }
}

constexpr int K3_LDS = 6 * 6 * 4096;
__global__ __launch_bounds__(384)
void k3(const u16* __restrict__ x, const u16* __restrict__ b2, char* __restrict__ ws, u16* __restrict__ out) {
  extern __shared__ __attribute__((aligned(16))) char smem_[];
  LDSP char* smem = (LDSP char*)smem_;
  const int tid = threadIdx.x, lane = tid & 63;
  const int w = __builtin_amdgcn_readfirstlane(tid >> 6);
  const int b = blockIdx.x;
  const int tp = (b & 7) * 4 + (b >> 3) / 6, ng = (b >> 3) % 6;
  const int t32 = lane & 31, hh = lane >> 5;
  const int tt = 2 * tp + w / 3, nt = 3 * ng + w % 3;
  const int token = tt * 32 + t32;
  const char* ap = ws + OFF_AO + ((size_t)(2 * tp) * 36 + w * 6) * 1024 + lane * 16;
  const char* wp = ws + OFF_WOT + ((size_t)(w * 6) * 18 + 3 * ng) * 1024 + lane * 16;
  const bool shared = (nt == 4 || nt == 13);
  const size_t SL = (size_t)64 * 18 * 2048;
  const char* pp = ws + OFF_PART + ((size_t)tt * 18 + nt) * 2048 + lane * 16;
  const char* pp2 = shared ? pp + 2 * SL : pp;
  s16x8 af[2][6], wf[3][6];
  u32x4 pt[4][2];
  u32x2 xv[4], bv[4];
#pragma unroll
  for (int k = 0; k < 6; ++k) {
    af[0][k] = *(const s16x8*)(ap + k * 1024);
    wf[0][k] = *(const s16x8*)(wp + k * 18432);
    af[1][k] = *(const s16x8*)(ap + 36 * 1024 + k * 1024);
    wf[1][k] = *(const s16x8*)(wp + k * 18432 + 1024);
    wf[2][k] = *(const s16x8*)(wp + k * 18432 + 2048);
  }
#pragma unroll
  for (int h = 0; h < 2; ++h) {
    pt[0][h] = *(const u32x4*)(pp + h * 1024);
    pt[1][h] = *(const u32x4*)(pp + SL + h * 1024);
    pt[2][h] = *(const u32x4*)(pp2 + h * 1024);
    pt[3][h] = *(const u32x4*)(pp2 + SL + h * 1024);
  }
#pragma unroll
  for (int q = 0; q < 4; ++q) {
    const int col = nt * 32 + 8 * q + 4 * hh;
    bv[q] = *(const u32x2*)((const char*)b2 + col * 2);
  }
  u32x4 xw[2];
#pragma unroll
  for (int q = 0; q < 2; ++q) xw[q] = *(const u32x4*)((const char*)x + (size_t)token * 1152 + (nt * 32 + 16 * q + 8 * hh) * 2);
  SCHED();
  f32x16 acc[2][3];
#pragma unroll
  for (int i = 0; i < 2; ++i)
#pragma unroll
    for (int jn = 0; jn < 3; ++jn)
#pragma unroll
      for (int r = 0; r < 16; ++r) acc[i][jn][r] = 0.f;
#pragma unroll
  for (int k = 0; k < 6; ++k)
#pragma unroll
    for (int i = 0; i < 2; ++i)
#pragma unroll
      for (int jn = 0; jn < 3; ++jn) acc[i][jn] = MFMA32(wf[jn][k], af[i][k], acc[i][jn]);
#pragma unroll
  for (int i = 0; i < 2; ++i)
#pragma unroll
    for (int jn = 0; jn < 3; ++jn)
#pragma unroll
      for (int q = 0; q < 4; ++q) {
        f32x4 v = {acc[i][jn][4 * q], acc[i][jn][4 * q + 1], acc[i][jn][4 * q + 2], acc[i][jn][4 * q + 3]};
        *(LDSP f32x4*)(smem + ((w * 6 + i * 3 + jn) * 4 + q) * 1024 + lane * 16) = v;
      }
  const float sh = shared ? 1.f : 0.f;
#pragma unroll
  for (int q = 0; q < 2; ++q)
#pragma unroll
    for (int e = 0; e < 2; ++e) {
      auto r = __builtin_amdgcn_permlane32_swap(xw[q][e], xw[q][2 + e], false, false);
      xv[2 * q][e] = r[0];
      xv[2 * q + 1][e] = r[1];
    }
  f32x4 fin[4];
#pragma unroll
  for (int q = 0; q < 4; ++q) {
#pragma unroll
    for (int e = 0; e < 2; ++e) {
      const unsigned p0 = pt[0][q >> 1][(q & 1) * 2 + e], p1 = pt[1][q >> 1][(q & 1) * 2 + e];
      const unsigned p2 = pt[2][q >> 1][(q & 1) * 2 + e], p3 = pt[3][q >> 1][(q & 1) * 2 + e];
      const float lo = (__builtin_bit_cast(float, p0 << 16) + __builtin_bit_cast(float, p1 << 16)) +
                       sh * (__builtin_bit_cast(float, p2 << 16) + __builtin_bit_cast(float, p3 << 16));
      const float hi = (__builtin_bit_cast(float, p0 & 0xffff0000u) + __builtin_bit_cast(float, p1 & 0xffff0000u)) +
                       sh * (__builtin_bit_cast(float, p2 & 0xffff0000u) + __builtin_bit_cast(float, p3 & 0xffff0000u));
      fin[q][2 * e]     = lo + __builtin_bit_cast(float, xv[q][e] << 16) + __builtin_bit_cast(float, bv[q][e] << 16);
      fin[q][2 * e + 1] = hi + __builtin_bit_cast(float, xv[q][e] & 0xffff0000u) + __builtin_bit_cast(float, bv[q][e] & 0xffff0000u);
    }
  }
  __syncthreads();
  u32x2 ov[4];
#pragma unroll
  for (int q = 0; q < 4; ++q) {
    f32x4 v = fin[q];
#pragma unroll
    for (int o = 0; o < 6; ++o) v += *(LDSP f32x4*)(smem + ((o * 6 + w) * 4 + q) * 1024 + lane * 16);
    ov[q] = u32x2{pack2(v[0], v[1]), pack2(v[2], v[3])};
  }
#pragma unroll
  for (int q = 0; q < 2; ++q) {
    auto r0 = __builtin_amdgcn_permlane32_swap(ov[2 * q][0], ov[2 * q + 1][0], false, false);
    auto r1 = __builtin_amdgcn_permlane32_swap(ov[2 * q][1], ov[2 * q + 1][1], false, false);
    u32x4 o4 = {r0[0], r1[0], r0[1], r1[1]};
    *(u32x4*)((char*)out + (size_t)token * 1152 + (nt * 32 + 16 * q + 8 * hh) * 2) = o4;
  }
}

extern "C" void kernel_launch(void* const* d_in, const int* in_sizes, int n_in,
                              void* d_out, int out_size, void* d_ws, size_t ws_size,
                              hipStream_t stream) {
  const u16* x  = (const u16*)d_in[0];
  const u16* wq = (const u16*)d_in[1];
  const u16* wk = (const u16*)d_in[2];
  const u16* wv = (const u16*)d_in[3];
  const u16* wo = (const u16*)d_in[4];
  const u16* w1 = (const u16*)d_in[6];
  const u16* b1 = (const u16*)d_in[7];
  const u16* w2 = (const u16*)d_in[8];
  const u16* b2 = (const u16*)d_in[9];
  char* ws = (char*)d_ws;
  (void)hipFuncSetAttribute((const void*)k1, hipFuncAttributeMaxDynamicSharedMemorySize, K1_LDS);
  (void)hipFuncSetAttribute((const void*)k2, hipFuncAttributeMaxDynamicSharedMemorySize, K2_LDS);
  (void)hipFuncSetAttribute((const void*)k3, hipFuncAttributeMaxDynamicSharedMemorySize, K3_LDS);
  k1<<<256, 512, K1_LDS, stream>>>(x, wq, wk, wv, wo, w1, b1, w2, ws);
  k2<<<256, 512, K2_LDS, stream>>>(ws);
  k3<<<192, 384, K3_LDS, stream>>>(x, b2, ws, (u16*)d_out);
}
```
